```python
import torch

def _softmax(x, axis=-1):
    return torch.softmax(x, dim=axis)
def _gelu(x):
    return torch.nn.functional.gelu(x, approximate="tanh")
def _silu(x):
    return torch.nn.functional.silu(x)
def _sigmoid(x):
    return torch.sigmoid(x)
def _layernorm(x, axis=-1, eps=1e-5):
    m = x.mean(dim=axis, keepdim=True)
    v = x.var(dim=axis, keepdim=True, unbiased=False)
    return (x - m) / torch.sqrt(v + eps)
def _scatter_add(tgt, idx, src, axis=0):
    return tgt.clone().index_add_(axis, idx, src)
def _segment_sum(src, seg, n):
    out = torch.zeros((n,) + src.shape[1:], dtype=src.dtype)
    return out.index_add_(0, seg, src)
def _pad(x, pw):
    flat = tuple(v for pair in reversed(pw) for v in pair)
    return torch.nn.functional.pad(x, flat)

kernel_name = 'concat_heads__gqa_block_cached__mha_block_cached__near_ridge'

def setup_inputs():

    _w = (lambda a: (lambda t: t.to(torch.bfloat16) if t.is_floating_point() else t)(torch.as_tensor(a)))
    def _A_setup():
        B, Sq, D = 8, 128, 640
        Hq, Hkv, Sk, Dh = 32, 32, 512, 128
        assert Hq % Hkv == 0, (Hq, Hkv)
        return {
            "x": _w((lambda *s: torch.randn(*s, dtype=torch.bfloat16) * 0.8)(B, Sq, D)),
            "k_cache": _w((lambda *s: torch.randn(*s, dtype=torch.bfloat16) * 0.8)(B, Hkv, Sk, Dh)),
            "v_cache": _w((lambda *s: torch.randn(*s, dtype=torch.bfloat16) * 0.05)(B, Hkv, Sk, Dh)),
            "wq": _w((lambda *s: torch.randn(*s, dtype=torch.bfloat16) * 0.8)(D, Hq * Dh) * (D ** -0.5)),
            "wo": _w((lambda *s: torch.randn(*s, dtype=torch.bfloat16) * 0.05)(Hq * Dh, D) * ((Hq * Dh) ** -0.5)),
        }
    _A_in = _A_setup()

    def _B_setup():
        B, Sq, D = 8, 128, 640
        H, Sk, Dh = 32, 512, 128
        return {
            "x": _w((lambda *s: torch.randn(*s, dtype=torch.bfloat16) * 0.8)(B, Sq, D)),
            "k_cache": _w((lambda *s: torch.randn(*s, dtype=torch.bfloat16) * 0.8)(B, H, Sk, Dh)),
            "v_cache": _w((lambda *s: torch.randn(*s, dtype=torch.bfloat16) * 0.05)(B, H, Sk, Dh)),
            "wq": _w((lambda *s: torch.randn(*s, dtype=torch.bfloat16) * 0.8)(D, H * Dh) * (D ** -0.5)),
            "wo": _w((lambda *s: torch.randn(*s, dtype=torch.bfloat16) * 0.05)(H * Dh, D) * ((H * Dh) ** -0.5)),
        }
    _B_in = _B_setup()

    return {
        "A_x": _A_in["x"],
        "A_k_cache": _A_in["k_cache"],
        "A_v_cache": _A_in["v_cache"],
        "A_wq": _A_in["wq"],
        "A_wo": _A_in["wo"],
        "B_x": _B_in["x"],
        "B_k_cache": _B_in["k_cache"],
        "B_v_cache": _B_in["v_cache"],
        "B_wq": _B_in["wq"],
        "B_wo": _B_in["wo"],
    }

def reference(A_x, A_k_cache, A_v_cache, A_wq, A_wo, B_x, B_k_cache, B_v_cache, B_wq, B_wo):
    def _A_ref(x, k_cache, v_cache, wq, wo):
        B, Sq, D = x.shape
        Hq, Dh = 32, 128
        Hkv = k_cache.shape[1]
        rep = Hq // Hkv
        q = (lambda x, *d: x.permute(*d))((x @ wq).reshape(B, Sq, Hq, Dh), 0, 2, 1, 3)
        k = (lambda x, n, axis: torch.repeat_interleave(x, n, dim=axis))(k_cache, rep, axis=1)
        v = (lambda x, n, axis: torch.repeat_interleave(x, n, dim=axis))(v_cache, rep, axis=1)
        scores = torch.einsum("bhqd,bhkd->bhqk", q, k) * (Dh ** -0.5)
        p = _softmax(scores, axis=-1)
        o = torch.einsum("bhqk,bhkd->bhqd", p, v)
        return (lambda x, *d: x.permute(*d))(o, 0, 2, 1, 3).reshape(B, Sq, Hq * Dh) @ wo

    def _B_ref(x, k_cache, v_cache, wq, wo):
        B, Sq, D = x.shape
        H, Dh = 32, 128
        q = (lambda x, *d: x.permute(*d))((x @ wq).reshape(B, Sq, H, Dh), 0, 2, 1, 3)
        scores = torch.einsum("bhqd,bhkd->bhqk", q, k_cache) * (Dh ** -0.5)
        p = _softmax(scores, axis=-1)
        o = torch.einsum("bhqk,bhkd->bhqd", p, v_cache)
        return (lambda x, *d: x.permute(*d))(o, 0, 2, 1, 3).reshape(B, Sq, H * Dh) @ wo

    a = _A_ref(A_x, A_k_cache, A_v_cache, A_wq, A_wo)
    b = _B_ref(B_x, B_k_cache, B_v_cache, B_wq, B_wo)
    return torch.concatenate([a, b], axis=-1)
```

```cpp
#include <hip/hip_runtime.h>
#include <hip/hip_bf16.h>
#include <stdint.h>

typedef short bf16x8 __attribute__((ext_vector_type(8)));
typedef short s16x4 __attribute__((ext_vector_type(4)));
typedef float f32x16 __attribute__((ext_vector_type(16)));
typedef float f32x4 __attribute__((ext_vector_type(4)));
typedef float f32x2 __attribute__((ext_vector_type(2)));
typedef __bf16 bf16x2_t __attribute__((ext_vector_type(2)));
typedef unsigned u32x4 __attribute__((ext_vector_type(4)));
typedef unsigned u32x2 __attribute__((ext_vector_type(2)));
#define LAS __attribute__((address_space(3)))
#define DI __device__ __forceinline__
typedef LAS char* lptr;
typedef __amdgpu_buffer_rsrc_t rsrc_t;

constexpr int NB = 8, SQ = 128, DM = 640, NH = 32, SK = 512, DH = 128;
constexpr float CEXP = 0.08838834764831845f * 1.4426950408889634f;
constexpr int SLOT = 16384;
constexpr int LDS_E = 8 * SLOT;
constexpr int LDS_X = 6 * SLOT;
constexpr int LDS_STAT = LDS_E + 24576;
constexpr int LDS1_BYTES = 10 * SLOT;
constexpr size_t OF_BYTES = (size_t)2 * NB * 8 * 128 * 1024;
constexpr size_t WP_BYTES = (size_t)2 * 40 * 128 * 1024;

#define MFMA32(a, b, c) __builtin_amdgcn_mfma_f32_32x32x16_bf16((a), (b), (c), 0, 0, 0)
#define MFMA16(a, b, c) __builtin_amdgcn_mfma_f32_16x16x32_bf16((a), (b), (c), 0, 0, 0)
#define WAITV(n) asm volatile("s_waitcnt vmcnt(" #n ")" ::: "memory")
#define BARRIER() asm volatile("s_waitcnt lgkmcnt(0)\n\ts_barrier" ::: "memory")

DI unsigned cvtpk(float lo, float hi) {
    f32x2 v = {lo, hi};
    bf16x2_t b = __builtin_convertvector(v, bf16x2_t);
    return __builtin_bit_cast(unsigned, b);
}
DI bf16x8 pack8(const f32x16& x, int s) {
    u32x4 w = {cvtpk(x[8 * s + 0], x[8 * s + 1]), cvtpk(x[8 * s + 2], x[8 * s + 3]),
               cvtpk(x[8 * s + 4], x[8 * s + 5]), cvtpk(x[8 * s + 6], x[8 * s + 7])};
    return __builtin_bit_cast(bf16x8, w);
}
DI void dma(unsigned lds, unsigned voff, rsrc_t srd, unsigned soff) {
    asm volatile("s_mov_b32 m0, %0\n\ts_nop 0\n\tbuffer_load_dwordx4 %1, %2, %3 offen lds"
                 :: "s"(lds), "v"(voff), "s"(srd), "s"(soff) : "memory");
}
DI bf16x8 lds128(lptr p) { return *(const LAS bf16x8*)p; }
DI s16x4 ldstr(lptr p) {
    typedef short v4i16_t __attribute__((ext_vector_type(4)));
    return __builtin_amdgcn_ds_read_tr16_b64_v4i16((LAS v4i16_t*)p);
}
DI bf16x8 cat(s16x4 a, s16x4 b) { return __builtin_shufflevector(a, b, 0, 1, 2, 3, 4, 5, 6, 7); }
DI float swapmax(float v) {
    auto r = __builtin_amdgcn_permlane32_swap(__float_as_uint(v), __float_as_uint(v), false, false);
    return fmaxf(__uint_as_float(r[0]), __uint_as_float(r[1]));
}
DI float swapsum(float v) {
    auto r = __builtin_amdgcn_permlane32_swap(__float_as_uint(v), __float_as_uint(v), false, false);
    return __uint_as_float(r[0]) + __uint_as_float(r[1]);
}
DI rsrc_t mkrsrc(const void* p, unsigned bytes) {
    return __builtin_amdgcn_make_buffer_rsrc((void*)p, (short)0, (int)bytes, 0x00020000);
}

__global__ __launch_bounds__(512, 2) void k1_attn(
    const char* xA, const char* kA, const char* vA, const char* wqA, const char* woA,
    const char* xB, const char* kB, const char* vB, const char* wqB, const char* woB,
    char* OF, char* WP)
{
    extern __shared__ __attribute__((aligned(16))) char smem_[];
    const lptr smem = (lptr)smem_;
    const unsigned lds0 = (unsigned)(uintptr_t)smem;
    const int tid = threadIdx.x, lane = tid & 63;
    const int wid = __builtin_amdgcn_readfirstlane(tid >> 6);
    const int bid = blockIdx.x;
    const int xg = bid & 7, ii = bid >> 3;
    const int br = xg >> 2, hp = 4 * (xg & 3) + (ii & 3), b = ii >> 2;
    const char* x = br ? xB : xA;
    const char* kc = br ? kB : kA;
    const char* vc = br ? vB : vA;
    const char* wq = br ? wqB : wqA;
    const int qb = wid & 3, kh = wid >> 2, lw = wid & 3;
    const int l31 = lane & 31, h = lane >> 5;

    const rsrc_t sx = mkrsrc(x + (size_t)b * SQ * DM * 2, SQ * DM * 2);
    const rsrc_t sw = mkrsrc(wq + (size_t)hp * 512, DM * 8192 - hp * 512);
    const rsrc_t sk = mkrsrc(kc + (size_t)(b * NH + 2 * hp) * SK * DH * 2, 2 * SK * DH * 2);
    const rsrc_t sv = mkrsrc(vc + (size_t)(b * NH + 2 * hp) * SK * DH * 2, 2 * SK * DH * 2);

    const unsigned dsub = lane >> 5, dr = (lane >> 2) & 7, dsl = lane & 3;
    const unsigned xr0 = 16u * (dsl ^ ((dr >> 2) & 3)), xr1 = 16u * (dsl ^ ((2 + (dr >> 2)) & 3));
    const unsigned rs = kh ? 1280u : 256u;
    const unsigned va0 = dr * rs + dsub * 64 + xr0, va1 = dr * rs + dsub * 64 + xr1;
    const unsigned va2 = dr * (kh ? 8192u : 256u) + dsub * 64 + 16 * dsl;

    auto issueK = [&](int n, unsigned ldsoff) {
#pragma unroll
        for (int gg = 0; gg < 4; ++gg)
#pragma unroll
            for (int pp = 0; pp < 2; ++pp) {
                const unsigned g = 4 * lw + gg;
                dma(lds0 + ldsoff + 2048 * g + 1024 * pp, (gg & 1) ? va1 : va0, sk, (unsigned)n * 32768u + 2048 * g + 128 * pp);
            }
    };
    auto issueV = [&](int n, unsigned ldsoff) {
#pragma unroll
        for (int gg = 0; gg < 4; ++gg)
#pragma unroll
            for (int pp = 0; pp < 2; ++pp) {
                const unsigned g = 4 * lw + gg;
                dma(lds0 + ldsoff + 2048 * g + 1024 * pp, va2, sv, (unsigned)n * 32768u + 2048 * g + 128 * pp);
            }
    };
    auto issueX = [&](int i) {
#pragma unroll
        for (int gg = 0; gg < 4; ++gg) {
            const unsigned g = 4 * lw + gg;
            dma(lds0 + LDS_E + (i & 1) * SLOT + 1024 * g, (gg & 1) ? va1 : va0, sx, g * 8 * 1280 + 128 * i);
        }
    };
    auto issueW = [&](int j) {
#pragma unroll
        for (int cq = 0; cq < 4; ++cq)
            dma(lds0 + (4 + (j & 3)) * SLOT + 4096 * lw + 1024 * cq, va2, sw, (unsigned)(32 * j + 8 * lw) * 8192u + 128 * cq);
    };

    if (kh == 0) { issueK(0, 0); issueV(0, 2 * SLOT); }
    else { issueX(0); issueW(0); issueW(1); issueX(1); issueW(2); issueW(3); }

    const int G1 = (lane >> 4) & 1, tq = (lane & 15) >> 2, tp = lane & 3;
    const int sig = ((tp & 1) << 1) | (tp >> 1);
    const unsigned wbase = 4096 * h + 64 * tq + 32 * G1 + 8 * sig + 1024 * kh;
    const unsigned rowq = 32 * qb + l31;
    const unsigned xsw = (rowq >> 2) & 3;
    const unsigned xb0 = 1024 * (rowq >> 3) + 64 * (rowq & 7) + 16 * ((0 + h) ^ xsw);
    const unsigned xb1 = 1024 * (rowq >> 3) + 64 * (rowq & 7) + 16 * ((2 + h) ^ xsw);

    f32x16 qa[2][2];
#pragma unroll
    for (int a = 0; a < 2; ++a)
#pragma unroll
        for (int e = 0; e < 2; ++e)
#pragma unroll
            for (int r = 0; r < 16; ++r) qa[a][e][r] = 0.f;

    auto qhalf = [&](int j) {
        const lptr wp = smem + (4 + (j & 3)) * SLOT + wbase;
        const lptr xp = smem + LDS_E + ((j >> 1) & 1) * SLOT + 512 * (j & 1);
#pragma unroll
        for (int s = 0; s < 2; ++s) {
            const bf16x8 xf = lds128(xp + (s ? xb1 : xb0));
#pragma unroll
            for (int hh = 0; hh < 2; ++hh)
#pragma unroll
                for (int e = 0; e < 2; ++e) {
                    const s16x4 lo = ldstr(wp + 8192 * s + 2048 * hh + 512 * e);
                    const s16x4 hi = ldstr(wp + 8192 * s + 2048 * hh + 512 * e + 256);
                    qa[hh][e] = MFMA32(cat(lo, hi), xf, qa[hh][e]);
                }
        }
    };
    if (kh) WAITV(16);
    BARRIER();
    qhalf(0);
    if (kh) WAITV(12);
    BARRIER();
    if (kh) issueW(4);
    qhalf(1);
#pragma unroll 1
    for (int i = 1; i < 9; ++i) {
        const int j = 2 * i;
        if (kh) WAITV(8);
        BARRIER();
        if (kh) { issueX(i + 1); issueW(j + 3); }
        qhalf(j);
        if (kh) WAITV(12);
        BARRIER();
        if (kh && i < 8) issueW(j + 4);
        qhalf(j + 1);
    }
    if (kh) WAITV(4);
    BARRIER();
    qhalf(18);
    if (kh) WAITV(0);
    BARRIER();
    qhalf(19);

    bf16x8 qf[8], q2own[4];
    bf16x8 qown[4];
#pragma unroll
    for (int e = 0; e < 2; ++e)
#pragma unroll
        for (int s = 0; s < 2; ++s) { qown[2 * e + s] = pack8(qa[0][e], s); q2own[2 * e + s] = pack8(qa[1][e], s); }
    BARRIER();
    if (kh == 0) { issueK(1, 4 * SLOT); issueV(1, 6 * SLOT); }
    const lptr qx_w = smem + LDS_E + (qb * 8 + 4 * kh) * 1024 + lane * 16;
    const lptr qx_r = smem + LDS_E + (qb * 8 + 4 * (kh ^ 1)) * 1024 + lane * 16;
#pragma unroll
    for (int f = 0; f < 4; ++f) *(LAS bf16x8*)(qx_w + 1024 * f) = qown[f];
    WAITV(24);
    BARRIER();
    {
        bf16x8 qo[4];
#pragma unroll
        for (int f = 0; f < 4; ++f) qo[f] = lds128(qx_r + 1024 * f);
#pragma unroll
        for (int f = 0; f < 4; ++f) { qf[f] = kh ? qo[f] : qown[f]; qf[4 + f] = kh ? qown[f] : qo[f]; }
    }

    const unsigned ksw = (l31 >> 2) & 3;
    const unsigned kb0 = kh * SLOT + 2048 * (l31 >> 3) + 64 * (l31 & 7) + 16 * ((0 + h) ^ ksw);
    const unsigned kb1 = kh * SLOT + 2048 * (l31 >> 3) + 64 * (l31 & 7) + 16 * ((2 + h) ^ ksw);
    const unsigned vb = 2 * SLOT + kh * SLOT + 64 * (4 * h + tq) + 32 * G1 + 8 * tp;

    f32x16 o[4];
#pragma unroll
    for (int d = 0; d < 4; ++d)
#pragma unroll
        for (int r = 0; r < 16; ++r) o[d][r] = 0.f;
    float m = -__builtin_inff(), l = 0.f;

    const char* wo = (bid >> 7) ? woB : woA;
    const rsrc_t swo = mkrsrc(wo + (size_t)(bid & 127) * 32 * 1280, 32 * 1280);
    auto issueWo = [&](int rnd) {
#pragma unroll
        for (int p = 0; p < 5; ++p) {
            const unsigned c = 64 * p + lane, row = c / 10, cc = c - row * 10;
            dma(lds0 + LDS_E + lw * 6144 + 1024 * p, row * 1280 + cc * 16, swo, (unsigned)(320 * lw + 160 * rnd));
        }
    };
    auto packWo = [&](int rnd) {
        const int g = lane >> 4;
        const lptr p0 = smem + LDS_E + lw * 6144 + (16 * (g >> 1) + 4 * (g & 1) + tq) * 160 + 8 * tp;
        char* dst = WP + ((size_t)((bid >> 7) * 40 + 10 * lw + 5 * rnd) * 128 + (bid & 127)) * 1024 + lane * 16;
#pragma unroll
        for (int nb = 0; nb < 5; ++nb) {
            const s16x4 lo = ldstr(p0 + nb * 32), hi = ldstr(p0 + nb * 32 + 8 * 160);
            *(bf16x8*)(dst + (size_t)nb * 128 * 1024) = cat(lo, hi);
        }
    };

#pragma unroll 1
    for (int i = 0; i < 8; ++i) {
        const unsigned par = (i & 1) * 4 * SLOT;
        f32x16 p0, p1;
#pragma unroll
        for (int r = 0; r < 16; ++r) { p0[r] = 0.f; p1[r] = 0.f; }
        {
            const lptr kp = smem + par;
#pragma unroll
            for (int s = 0; s < 8; ++s) {
                const lptr a = kp + ((s & 1) ? kb1 : kb0) + 512 * (s >> 1);
                const bf16x8 k0 = lds128(a), k1 = lds128(a + 8192);
                p0 = MFMA32(k0, qf[s], p0);
                p1 = MFMA32(k1, qf[s], p1);
            }
        }
        float mx = p0[0];
#pragma unroll
        for (int r = 1; r < 16; ++r) mx = fmaxf(mx, p0[r]);
#pragma unroll
        for (int r = 0; r < 16; ++r) mx = fmaxf(mx, p1[r]);
        mx = swapmax(mx);
        const float mn = fmaxf(m, mx);
        const float alpha = __builtin_amdgcn_exp2f((m - mn) * CEXP);
        m = mn;
        const float nm = -mn * CEXP;
        float rs0 = 0.f, rs1 = 0.f;
#pragma unroll
        for (int r = 0; r < 16; ++r) {
            p0[r] = __builtin_amdgcn_exp2f(fmaf(p0[r], CEXP, nm)); rs0 += p0[r];
            p1[r] = __builtin_amdgcn_exp2f(fmaf(p1[r], CEXP, nm)); rs1 += p1[r];
        }
        l = l * alpha + (rs0 + rs1);
        bf16x8 pf[4];
        pf[0] = pack8(p0, 0); pf[1] = pack8(p0, 1); pf[2] = pack8(p1, 0); pf[3] = pack8(p1, 1);
        if (__any(alpha < 1.f)) {
#pragma unroll
            for (int d = 0; d < 4; ++d)
#pragma unroll
                for (int r = 0; r < 16; ++r) o[d][r] *= alpha;
        }
        const bool last = (i & 3) == 3;
        float lt = 0.f;
        if (last) {
            lt = swapsum(l);
            if (h == 0) { *(LAS f32x2*)(smem + LDS_STAT + wid * 256 + l31 * 8) = (f32x2){m, lt}; }
        }
        WAITV(16);
        BARRIER();
        if (kh == 0) issueK(i + 2, par);
        else {
            if (i == 0) issueWo(0);
            else if (i == 1) { WAITV(0); packWo(0); asm volatile("s_waitcnt lgkmcnt(0)" ::: "memory"); issueWo(1); }
            else if (i == 2) { WAITV(0); packWo(1); }
        }
        f32x2 st;
        if (last) st = *(const LAS f32x2*)(smem + LDS_STAT + (wid ^ 4) * 256 + l31 * 8);
        {
            const lptr vp = smem + par + vb;
#pragma unroll
            for (int ks = 0; ks < 4; ++ks)
#pragma unroll
                for (int d = 0; d < 4; ++d) {
                    const s16x4 lo = ldstr(vp + 4096 * ks + 512 * d), hi = ldstr(vp + 4096 * ks + 2048 + 512 * d);
                    o[d] = MFMA32(cat(lo, hi), pf[ks], o[d]);
                }
        }
        if (!last) {
            WAITV(16);
            BARRIER();
            if (kh == 0) issueV(i + 2, par + 2 * SLOT);
        } else {
            const float M = fmaxf(m, st[0]);
            const float a0 = __builtin_amdgcn_exp2f((m - M) * CEXP), a1 = __builtin_amdgcn_exp2f((st[0] - M) * CEXP);
            const float sc = a0 / (lt * a0 + st[1] * a1);
#pragma unroll
            for (int d = 0; d < 4; ++d)
#pragma unroll
                for (int r = 0; r < 16; ++r) o[d][r] *= sc;
            BARRIER();
            const lptr xw = smem + LDS_X + wid * 8192 + lane * 16;
            const lptr xr = smem + LDS_X + (wid ^ 4) * 8192 + lane * 16;
            f32x16 keep0 = kh ? o[2] : o[0], keep1 = kh ? o[3] : o[1];
            f32x16 send0 = kh ? o[0] : o[2], send1 = kh ? o[1] : o[3];
#pragma unroll
            for (int c = 0; c < 4; ++c) {
                *(LAS f32x4*)(xw + 1024 * c) = (f32x4){send0[4 * c], send0[4 * c + 1], send0[4 * c + 2], send0[4 * c + 3]};
                *(LAS f32x4*)(xw + 4096 + 1024 * c) = (f32x4){send1[4 * c], send1[4 * c + 1], send1[4 * c + 2], send1[4 * c + 3]};
            }
            BARRIER();
#pragma unroll
            for (int c = 0; c < 4; ++c) {
                const f32x4 u0 = *(const LAS f32x4*)(xr + 1024 * c), u1 = *(const LAS f32x4*)(xr + 4096 + 1024 * c);
#pragma unroll
                for (int e = 0; e < 4; ++e) { keep0[4 * c + e] += u0[e]; keep1[4 * c + e] += u1[e]; }
            }
            const int head = 2 * hp + (i >> 2);
            char* ob = OF + ((size_t)((br * NB + b) * 8 + 2 * qb + (l31 >> 4)) * 128 + head * 4 + 2 * kh) * 1024 + (16 * h + (l31 & 15)) * 16;
            *(bf16x8*)(ob) = pack8(keep0, 0);
            *(bf16x8*)(ob + 512) = pack8(keep0, 1);
            *(bf16x8*)(ob + 1024) = pack8(keep1, 0);
            *(bf16x8*)(ob + 1536) = pack8(keep1, 1);
            if (i == 3) {
                BARRIER();
                if (kh == 0) issueV(i + 2, par + 2 * SLOT);
#pragma unroll
                for (int f = 0; f < 4; ++f) *(LAS bf16x8*)(qx_w + 1024 * f) = q2own[f];
                WAITV(28);
                BARRIER();
                bf16x8 qo[4];
#pragma unroll
                for (int f = 0; f < 4; ++f) qo[f] = lds128(qx_r + 1024 * f);
#pragma unroll
                for (int f = 0; f < 4; ++f) { qf[f] = kh ? qo[f] : q2own[f]; qf[4 + f] = kh ? q2own[f] : qo[f]; }
#pragma unroll
                for (int d = 0; d < 4; ++d)
#pragma unroll
                    for (int r = 0; r < 16; ++r) o[d][r] = 0.f;
                m = -__builtin_inff(); l = 0.f;
            }
        }
    }
    WAITV(0);
}

__global__ __launch_bounds__(512, 2) void k2_oproj(const char* __restrict__ OF, const char* __restrict__ WP, char* __restrict__ out)
{
    extern __shared__ __attribute__((aligned(16))) char smem_[];
    const lptr smem = (lptr)smem_;
    const int tid = threadIdx.x, lane = tid & 63;
    const int wid = __builtin_amdgcn_readfirstlane(tid >> 6);
    const int bid = blockIdx.x;
    const int xg = bid & 7, ii = bid >> 3;
    const int br = xg >> 2;
    const int b = 4 * ((xg >> 1) & 1) + (ii >> 3), qh = (ii >> 2) & 1, nt = 4 * (xg & 1) + (ii & 3);
    const rsrc_t so = mkrsrc(OF + ((size_t)((br * NB + b) * 8 + 4 * qh) * 128 + 16 * wid) * 1024, 4 * 131072);
    const rsrc_t swp = mkrsrc(WP + ((size_t)(br * 40 + 5 * nt) * 128 + 16 * wid) * 1024, 5 * 131072);
    const unsigned vo = lane * 16;
    constexpr int D = 4;
    bf16x8 of[D][4], wf[D][5];
    f32x4 acc[4][5];
#pragma unroll
    for (int a = 0; a < 4; ++a)
#pragma unroll
        for (int n = 0; n < 5; ++n) acc[a][n] = (f32x4){0.f, 0.f, 0.f, 0.f};
#pragma unroll
    for (int ks = 0; ks < 16 + D - 1; ++ks) {
        if (ks < 16) {
#pragma unroll
            for (int a = 0; a < 4; ++a) of[ks % D][a] = __builtin_bit_cast(bf16x8, __builtin_amdgcn_raw_buffer_load_b128(so, vo, a * 131072 + ks * 1024, 0));
#pragma unroll
            for (int n = 0; n < 5; ++n) wf[ks % D][n] = __builtin_bit_cast(bf16x8, __builtin_amdgcn_raw_buffer_load_b128(swp, vo, n * 131072 + ks * 1024, 0));
        }
        __builtin_amdgcn_sched_barrier(0);
        if (ks >= D - 1) {
            const int c = (ks - (D - 1)) % D;
#pragma unroll
            for (int a = 0; a < 4; ++a)
#pragma unroll
                for (int n = 0; n < 5; ++n) acc[a][n] = MFMA16(wf[c][n], of[c][a], acc[a][n]);
        }
        __builtin_amdgcn_sched_barrier(0);
    }
#pragma unroll
    for (int a = 0; a < 4; ++a)
#pragma unroll
        for (int n = 0; n < 5; ++n) *(LAS f32x4*)(smem + wid * 20480 + (a * 5 + n) * 1024 + lane * 16) = acc[a][n];
    __syncthreads();
    for (int it = tid; it < 1280; it += 512) {
        f32x4 s = *(const LAS f32x4*)(smem + it * 16);
#pragma unroll
        for (int w = 1; w < 8; ++w) { const f32x4 t = *(const LAS f32x4*)(smem + w * 20480 + it * 16); s += t; }
        const int fr = it >> 6, ln = it & 63, a = fr / 5, n = fr - 5 * a;
        const int q = 64 * qh + 16 * a + (ln & 15), col = br * DM + 80 * nt + 16 * n + 4 * (ln >> 4);
        u32x2 w = {cvtpk(s[0], s[1]), cvtpk(s[2], s[3])};
        *(u32x2*)(out + ((size_t)(b * SQ + q) * (2 * DM) + col) * 2) = w;
    }
}

extern "C" void kernel_launch(void* const* d_in, const int* in_sizes, int n_in, void* d_out, int out_size,
                              void* d_ws, size_t ws_size, hipStream_t stream) {
    char* OF = (char*)d_ws;
    char* WP = OF + OF_BYTES;
    (void)hipFuncSetAttribute((const void*)k1_attn, hipFuncAttributeMaxDynamicSharedMemorySize, LDS1_BYTES);
    (void)hipFuncSetAttribute((const void*)k2_oproj, hipFuncAttributeMaxDynamicSharedMemorySize, 163840);
    hipLaunchKernelGGL(k1_attn, dim3(256), dim3(512), LDS1_BYTES, stream,
                       (const char*)d_in[0], (const char*)d_in[1], (const char*)d_in[2], (const char*)d_in[3], (const char*)d_in[4],
                       (const char*)d_in[5], (const char*)d_in[6], (const char*)d_in[7], (const char*)d_in[8], (const char*)d_in[9],
                       OF, WP);
    hipLaunchKernelGGL(k2_oproj, dim3(256), dim3(512), 163840, stream, (const char*)OF, (const char*)WP, (char*)d_out);
}
```

```cpp
#define NT_X 0
#define NT_W 1
#include <hip/hip_runtime.h>
#include <hip/hip_bf16.h>
#include <stdint.h>

typedef short bf16x8 __attribute__((ext_vector_type(8)));
typedef short s16x4 __attribute__((ext_vector_type(4)));
typedef float f32x16 __attribute__((ext_vector_type(16)));
typedef float f32x4 __attribute__((ext_vector_type(4)));
typedef float f32x2 __attribute__((ext_vector_type(2)));
typedef __bf16 bf16x2_t __attribute__((ext_vector_type(2)));
typedef unsigned u32x4 __attribute__((ext_vector_type(4)));
typedef unsigned u32x2 __attribute__((ext_vector_type(2)));
#define LAS __attribute__((address_space(3)))
#define DI __device__ __forceinline__
typedef LAS char* lptr;
typedef __amdgpu_buffer_rsrc_t rsrc_t;

constexpr int NB = 8, SQ = 128, DM = 640, NH = 32, SK = 512, DH = 128;
constexpr float CEXP = 0.08838834764831845f * 1.4426950408889634f;
constexpr int SLOT = 16384;
constexpr int LDS_E = 8 * SLOT;
constexpr int LDS_X = 6 * SLOT;
constexpr int LDS_STAT = LDS_E + 24576;
constexpr int LDS1_BYTES = 10 * SLOT;
constexpr size_t OF_BYTES = (size_t)2 * NB * 8 * 128 * 1024;
constexpr size_t WP_BYTES = (size_t)2 * 40 * 128 * 1024;

#define STAMP(idx)
#define MFMA32(a, b, c) __builtin_amdgcn_mfma_f32_32x32x16_bf16((a), (b), (c), 0, 0, 0)
#define MFMA16(a, b, c) __builtin_amdgcn_mfma_f32_16x16x32_bf16((a), (b), (c), 0, 0, 0)
#define WAITV(n) asm volatile("s_waitcnt vmcnt(" #n ")" ::: "memory")
#define BARRIER() asm volatile("s_waitcnt lgkmcnt(0)\n\ts_barrier" ::: "memory")

DI unsigned cvtpk(float lo, float hi) {
    f32x2 v = {lo, hi};
    bf16x2_t b = __builtin_convertvector(v, bf16x2_t);
    return __builtin_bit_cast(unsigned, b);
}
DI bf16x8 pack8(const f32x16& x, int s) {
    u32x4 w = {cvtpk(x[8 * s + 0], x[8 * s + 1]), cvtpk(x[8 * s + 2], x[8 * s + 3]),
               cvtpk(x[8 * s + 4], x[8 * s + 5]), cvtpk(x[8 * s + 6], x[8 * s + 7])};
    return __builtin_bit_cast(bf16x8, w);
}
template <int NT> DI void dma(unsigned lds, unsigned voff, rsrc_t srd, unsigned soff) {
    if (NT) asm volatile("s_mov_b32 m0, %0\n\ts_nop 0\n\tbuffer_load_dwordx4 %1, %2, %3 offen nt lds"
                 :: "s"(lds), "v"(voff), "s"(srd), "s"(soff) : "memory");
    else asm volatile("s_mov_b32 m0, %0\n\ts_nop 0\n\tbuffer_load_dwordx4 %1, %2, %3 offen lds"
                 :: "s"(lds), "v"(voff), "s"(srd), "s"(soff) : "memory");
}
#define NT_KV 1
#define NT_WO 1
DI bf16x8 lds128(lptr p) { return *(const LAS bf16x8*)p; }
DI s16x4 ldstr(lptr p) {
    typedef short v4i16_t __attribute__((ext_vector_type(4)));
    return __builtin_amdgcn_ds_read_tr16_b64_v4i16((LAS v4i16_t*)p);
}
DI bf16x8 cat(s16x4 a, s16x4 b) { return __builtin_shufflevector(a, b, 0, 1, 2, 3, 4, 5, 6, 7); }
DI float swapmax(float v) {
    auto r = __builtin_amdgcn_permlane32_swap(__float_as_uint(v), __float_as_uint(v), false, false);
    return fmaxf(__uint_as_float(r[0]), __uint_as_float(r[1]));
}
DI float swapsum(float v) {
    auto r = __builtin_amdgcn_permlane32_swap(__float_as_uint(v), __float_as_uint(v), false, false);
    return __uint_as_float(r[0]) + __uint_as_float(r[1]);
}
DI rsrc_t mkrsrc(const void* p, unsigned bytes) {
    return __builtin_amdgcn_make_buffer_rsrc((void*)p, (short)0, (int)bytes, 0x00020000);
}

__global__ __launch_bounds__(512, 2) void k1_attn(
    const char* xA, const char* kA, const char* vA, const char* wqA, const char* woA,
    const char* xB, const char* kB, const char* vB, const char* wqB, const char* woB,
    char* OF, char* WP)
{
    extern __shared__ __attribute__((aligned(16))) char smem_[];
    const lptr smem = (lptr)smem_;
    const unsigned lds0 = (unsigned)(uintptr_t)smem;
    const int tid = threadIdx.x, lane = tid & 63;
    const int wid = __builtin_amdgcn_readfirstlane(tid >> 6);
    const int bid = blockIdx.x;
    const int xg = bid & 7, ii = bid >> 3;
    const int br = xg >> 2, hp = 4 * (xg & 3) + (ii & 3), b = ii >> 2;
    const char* x = br ? xB : xA;
    const char* kc = br ? kB : kA;
    const char* vc = br ? vB : vA;
    const char* wq = br ? wqB : wqA;
    const int qb = wid & 3, kh = wid >> 2, lw = wid & 3;
    const int l31 = lane & 31, h = lane >> 5;

    const rsrc_t sx = mkrsrc(x + (size_t)b * SQ * DM * 2, SQ * DM * 2);
    const rsrc_t sw = mkrsrc(wq + (size_t)hp * 512, DM * 8192 - hp * 512);
    const rsrc_t sk = mkrsrc(kc + (size_t)(b * NH + 2 * hp) * SK * DH * 2, 2 * SK * DH * 2);
    const rsrc_t sv = mkrsrc(vc + (size_t)(b * NH + 2 * hp) * SK * DH * 2, 2 * SK * DH * 2);

    const unsigned dsub = lane >> 5, dr = (lane >> 2) & 7, dsl = lane & 3;
    const unsigned xr0 = 16u * (dsl ^ ((dr >> 2) & 3)), xr1 = 16u * (dsl ^ ((2 + (dr >> 2)) & 3));
    const unsigned rs = kh ? 1280u : 256u;
    const unsigned va0 = dr * rs + dsub * 64 + xr0, va1 = dr * rs + dsub * 64 + xr1;
    const unsigned va2 = dr * (kh ? 8192u : 256u) + dsub * 64 + 16 * dsl;

    auto issueK = [&](int n, unsigned ldsoff) {
#pragma unroll
        for (int gg = 0; gg < 4; ++gg)
#pragma unroll
            for (int pp = 0; pp < 2; ++pp) {
                const unsigned g = 4 * lw + gg;
                dma<NT_KV>(lds0 + ldsoff + 2048 * g + 1024 * pp, (gg & 1) ? va1 : va0, sk, (unsigned)n * 32768u + 2048 * g + 128 * pp);
            }
    };
    auto issueV = [&](int n, unsigned ldsoff) {
#pragma unroll
        for (int gg = 0; gg < 4; ++gg)
#pragma unroll
            for (int pp = 0; pp < 2; ++pp) {
                const unsigned g = 4 * lw + gg;
                dma<NT_KV>(lds0 + ldsoff + 2048 * g + 1024 * pp, va2, sv, (unsigned)n * 32768u + 2048 * g + 128 * pp);
            }
    };
    auto issueX = [&](int i) {
#pragma unroll
        for (int gg = 0; gg < 4; ++gg) {
            const unsigned g = 4 * lw + gg;
            dma<NT_X>(lds0 + LDS_E + (i & 1) * SLOT + 1024 * g, (gg & 1) ? va1 : va0, sx, g * 8 * 1280 + 128 * i);
        }
    };
    auto issueW = [&](int j) {
#pragma unroll
        for (int cq = 0; cq < 4; ++cq)
            dma<NT_W>(lds0 + (4 + (j & 3)) * SLOT + 4096 * lw + 1024 * cq, va2, sw, (unsigned)(32 * j + 8 * lw) * 8192u + 128 * cq);
    };

    constexpr int STAMPK = 0;
    STAMP(0);
    if (kh == 0) { issueK(0, 0); issueV(0, 2 * SLOT); }
    else { issueX(0); issueW(0); issueW(1); issueX(1); issueW(2); issueW(3); }

    const int G1 = (lane >> 4) & 1, tq = (lane & 15) >> 2, tp = lane & 3;
    const int sig = ((tp & 1) << 1) | (tp >> 1);
    const unsigned wbase = 4096 * h + 64 * tq + 32 * G1 + 8 * sig + 1024 * kh;
    const unsigned rowq = 32 * qb + l31;
    const unsigned xsw = (rowq >> 2) & 3;
    const unsigned xb0 = 1024 * (rowq >> 3) + 64 * (rowq & 7) + 16 * ((0 + h) ^ xsw);
    const unsigned xb1 = 1024 * (rowq >> 3) + 64 * (rowq & 7) + 16 * ((2 + h) ^ xsw);

    f32x16 qa[2][2];
#pragma unroll
    for (int a = 0; a < 2; ++a)
#pragma unroll
        for (int e = 0; e < 2; ++e)
#pragma unroll
            for (int r = 0; r < 16; ++r) qa[a][e][r] = 0.f;

    auto qhalf = [&](int j) {
        const lptr wp = smem + (4 + (j & 3)) * SLOT + wbase;
        const lptr xp = smem + LDS_E + ((j >> 1) & 1) * SLOT + 512 * (j & 1);
#pragma unroll
        for (int s = 0; s < 2; ++s) {
            const bf16x8 xf = lds128(xp + (s ? xb1 : xb0));
#pragma unroll
            for (int hh = 0; hh < 2; ++hh)
#pragma unroll
                for (int e = 0; e < 2; ++e) {
                    const s16x4 lo = ldstr(wp + 8192 * s + 2048 * hh + 512 * e);
                    const s16x4 hi = ldstr(wp + 8192 * s + 2048 * hh + 512 * e + 256);
                    qa[hh][e] = MFMA32(cat(lo, hi), xf, qa[hh][e]);
                }
        }
    };
    if (kh) WAITV(16);
    BARRIER();
    qhalf(0);
    if (kh) WAITV(12);
    BARRIER();
    if (kh) issueW(4);
    qhalf(1);
#pragma unroll 1
    for (int i = 1; i < 9; ++i) {
        const int j = 2 * i;
        if (kh) WAITV(8);
        BARRIER();
        if (kh) { issueX(i + 1); issueW(j + 3); }
        qhalf(j);
        if (kh) WAITV(12);
        BARRIER();
        if (kh && i < 8) issueW(j + 4);
        qhalf(j + 1);
    }
    if (kh) WAITV(4);
    BARRIER();
    qhalf(18);
    if (kh) WAITV(0);
    BARRIER();
    qhalf(19);

    bf16x8 qf[8], q2own[4];
    bf16x8 qown[4];
#pragma unroll
    for (int e = 0; e < 2; ++e)
#pragma unroll
        for (int s = 0; s < 2; ++s) { qown[2 * e + s] = pack8(qa[0][e], s); q2own[2 * e + s] = pack8(qa[1][e], s); }
    BARRIER();
    STAMP(1);
    if (kh == 0) { issueK(1, 4 * SLOT); issueV(1, 6 * SLOT); }
    const lptr qx_w = smem + LDS_E + (qb * 8 + 4 * kh) * 1024 + lane * 16;
    const lptr qx_r = smem + LDS_E + (qb * 8 + 4 * (kh ^ 1)) * 1024 + lane * 16;
#pragma unroll
    for (int f = 0; f < 4; ++f) *(LAS bf16x8*)(qx_w + 1024 * f) = qown[f];
    WAITV(24);
    BARRIER();
    STAMP(2);
    {
        bf16x8 qo[4];
#pragma unroll
        for (int f = 0; f < 4; ++f) qo[f] = lds128(qx_r + 1024 * f);
#pragma unroll
        for (int f = 0; f < 4; ++f) { qf[f] = kh ? qo[f] : qown[f]; qf[4 + f] = kh ? qown[f] : qo[f]; }
    }

    const unsigned ksw = (l31 >> 2) & 3;
    const unsigned kb0 = kh * SLOT + 2048 * (l31 >> 3) + 64 * (l31 & 7) + 16 * ((0 + h) ^ ksw);
    const unsigned kb1 = kh * SLOT + 2048 * (l31 >> 3) + 64 * (l31 & 7) + 16 * ((2 + h) ^ ksw);
    const unsigned vb = 2 * SLOT + kh * SLOT + 64 * (4 * h + tq) + 32 * G1 + 8 * tp;

    f32x16 o[4];
#pragma unroll
    for (int d = 0; d < 4; ++d)
#pragma unroll
        for (int r = 0; r < 16; ++r) o[d][r] = 0.f;
    float m = -__builtin_inff(), l = 0.f;

    const char* wo = (bid >> 7) ? woB : woA;
    const rsrc_t swo = mkrsrc(wo + (size_t)(bid & 127) * 32 * 1280, 32 * 1280);
    auto issueWo = [&](int rnd) {
#pragma unroll
        for (int p = 0; p < 5; ++p) {
            const unsigned c = 64 * p + lane, row = c / 10, cc = c - row * 10;
            dma<NT_WO>(lds0 + LDS_E + lw * 6144 + 1024 * p, row * 1280 + cc * 16, swo, (unsigned)(320 * lw + 160 * rnd));
        }
    };
    auto packWo = [&](int rnd) {
        const int g = lane >> 4;
        const lptr p0 = smem + LDS_E + lw * 6144 + (16 * (g >> 1) + 4 * (g & 1) + tq) * 160 + 8 * tp;
        char* dst = WP + ((size_t)((bid >> 7) * 40 + 10 * lw + 5 * rnd) * 128 + (bid & 127)) * 1024 + lane * 16;
#pragma unroll
        for (int nb = 0; nb < 5; ++nb) {
            const s16x4 lo = ldstr(p0 + nb * 32), hi = ldstr(p0 + nb * 32 + 8 * 160);
            *(bf16x8*)(dst + (size_t)nb * 128 * 1024) = cat(lo, hi);
        }
    };

#pragma unroll 1
    for (int i = 0; i < 8; ++i) {
        const unsigned par = (i & 1) * 4 * SLOT;
        f32x16 p0, p1;
#pragma unroll
        for (int r = 0; r < 16; ++r) { p0[r] = 0.f; p1[r] = 0.f; }
        {
            const lptr kp = smem + par;
#pragma unroll
            for (int s = 0; s < 8; ++s) {
                const lptr a = kp + ((s & 1) ? kb1 : kb0) + 512 * (s >> 1);
                const bf16x8 k0 = lds128(a), k1 = lds128(a + 8192);
                p0 = MFMA32(k0, qf[s], p0);
                p1 = MFMA32(k1, qf[s], p1);
            }
        }
        float mx = p0[0];
#pragma unroll
        for (int r = 1; r < 16; ++r) mx = fmaxf(mx, p0[r]);
#pragma unroll
        for (int r = 0; r < 16; ++r) mx = fmaxf(mx, p1[r]);
        mx = swapmax(mx);
        const float mn = fmaxf(m, mx);
        const float alpha = __builtin_amdgcn_exp2f((m - mn) * CEXP);
        m = mn;
        const float nm = -mn * CEXP;
        float rs0 = 0.f, rs1 = 0.f;
#pragma unroll
        for (int r = 0; r < 16; ++r) {
            p0[r] = __builtin_amdgcn_exp2f(fmaf(p0[r], CEXP, nm)); rs0 += p0[r];
            p1[r] = __builtin_amdgcn_exp2f(fmaf(p1[r], CEXP, nm)); rs1 += p1[r];
        }
        l = l * alpha + (rs0 + rs1);
        bf16x8 pf[4];
        pf[0] = pack8(p0, 0); pf[1] = pack8(p0, 1); pf[2] = pack8(p1, 0); pf[3] = pack8(p1, 1);
        if (__any(alpha < 1.f)) {
#pragma unroll
            for (int d = 0; d < 4; ++d)
#pragma unroll
                for (int r = 0; r < 16; ++r) o[d][r] *= alpha;
        }
        const bool last = (i & 3) == 3;
        float lt = 0.f;
        if (last) {
            lt = swapsum(l);
            if (h == 0) { *(LAS f32x2*)(smem + LDS_STAT + wid * 256 + l31 * 8) = (f32x2){m, lt}; }
        }
        STAMP(3 + 4 * i);
        WAITV(16);
        BARRIER();
        STAMP(4 + 4 * i);
        if (kh == 0) issueK(i + 2, par);
        else {
            if (i == 0) issueWo(0);
            else if (i == 1) { WAITV(0); packWo(0); asm volatile("s_waitcnt lgkmcnt(0)" ::: "memory"); issueWo(1); }
            else if (i == 2) { WAITV(0); packWo(1); }
        }
        f32x2 st;
        if (last) st = *(const LAS f32x2*)(smem + LDS_STAT + (wid ^ 4) * 256 + l31 * 8);
        {
            const lptr vp = smem + par + vb;
#pragma unroll
            for (int ks = 0; ks < 4; ++ks)
#pragma unroll
                for (int d = 0; d < 4; ++d) {
                    const s16x4 lo = ldstr(vp + 4096 * ks + 512 * d), hi = ldstr(vp + 4096 * ks + 2048 + 512 * d);
                    o[d] = MFMA32(cat(lo, hi), pf[ks], o[d]);
                }
        }
        STAMP(5 + 4 * i);
        if (!last) {
            WAITV(16);
            BARRIER();
            STAMP(6 + 4 * i);
            if (kh == 0) issueV(i + 2, par + 2 * SLOT);
        } else {
            const float M = fmaxf(m, st[0]);
            const float a0 = __builtin_amdgcn_exp2f((m - M) * CEXP), a1 = __builtin_amdgcn_exp2f((st[0] - M) * CEXP);
            const float sc = a0 / (lt * a0 + st[1] * a1);
#pragma unroll
            for (int d = 0; d < 4; ++d)
#pragma unroll
                for (int r = 0; r < 16; ++r) o[d][r] *= sc;
            BARRIER();
            const lptr xw = smem + LDS_X + wid * 8192 + lane * 16;
            const lptr xr = smem + LDS_X + (wid ^ 4) * 8192 + lane * 16;
            f32x16 keep0 = kh ? o[2] : o[0], keep1 = kh ? o[3] : o[1];
            f32x16 send0 = kh ? o[0] : o[2], send1 = kh ? o[1] : o[3];
#pragma unroll
            for (int c = 0; c < 4; ++c) {
                *(LAS f32x4*)(xw + 1024 * c) = (f32x4){send0[4 * c], send0[4 * c + 1], send0[4 * c + 2], send0[4 * c + 3]};
                *(LAS f32x4*)(xw + 4096 + 1024 * c) = (f32x4){send1[4 * c], send1[4 * c + 1], send1[4 * c + 2], send1[4 * c + 3]};
            }
            BARRIER();
#pragma unroll
            for (int c = 0; c < 4; ++c) {
                const f32x4 u0 = *(const LAS f32x4*)(xr + 1024 * c), u1 = *(const LAS f32x4*)(xr + 4096 + 1024 * c);
#pragma unroll
                for (int e = 0; e < 4; ++e) { keep0[4 * c + e] += u0[e]; keep1[4 * c + e] += u1[e]; }
            }
            const int head = 2 * hp + (i >> 2);
            char* ob = OF + ((size_t)((br * NB + b) * 8 + 2 * qb + (l31 >> 4)) * 128 + head * 4 + 2 * kh) * 1024 + (16 * h + (l31 & 15)) * 16;
            *(bf16x8*)(ob) = pack8(keep0, 0);
            *(bf16x8*)(ob + 512) = pack8(keep0, 1);
            *(bf16x8*)(ob + 1024) = pack8(keep1, 0);
            *(bf16x8*)(ob + 1536) = pack8(keep1, 1);
            if (i == 3) {
                BARRIER();
                if (kh == 0) issueV(i + 2, par + 2 * SLOT);
#pragma unroll
                for (int f = 0; f < 4; ++f) *(LAS bf16x8*)(qx_w + 1024 * f) = q2own[f];
                WAITV(28);
                BARRIER();
                STAMP(6 + 4 * i);
                bf16x8 qo[4];
#pragma unroll
                for (int f = 0; f < 4; ++f) qo[f] = lds128(qx_r + 1024 * f);
#pragma unroll
                for (int f = 0; f < 4; ++f) { qf[f] = kh ? qo[f] : q2own[f]; qf[4 + f] = kh ? q2own[f] : qo[f]; }
#pragma unroll
                for (int d = 0; d < 4; ++d)
#pragma unroll
                    for (int r = 0; r < 16; ++r) o[d][r] = 0.f;
                m = -__builtin_inff(); l = 0.f;
            }
        }
    }
    WAITV(0);
    STAMP(35);
}

__global__ __launch_bounds__(512, 2) void k2_oproj(const char* __restrict__ OF, const char* __restrict__ WP, char* __restrict__ out)
{
    extern __shared__ __attribute__((aligned(16))) char smem_[];
    const lptr smem = (lptr)smem_;
    const int tid = threadIdx.x, lane = tid & 63;
    const int wid = __builtin_amdgcn_readfirstlane(tid >> 6);
    const int bid = blockIdx.x;
    const int xg = bid & 7, ii = bid >> 3;
    const int br = xg >> 2;
    const int b = 4 * ((xg >> 1) & 1) + (ii >> 3), qh = (ii >> 2) & 1, nt = 4 * (xg & 1) + (ii & 3);
    const rsrc_t so = mkrsrc(OF + ((size_t)((br * NB + b) * 8 + 4 * qh) * 128 + 16 * wid) * 1024, 4 * 131072);
    const rsrc_t swp = mkrsrc(WP + ((size_t)(br * 40 + 5 * nt) * 128 + 16 * wid) * 1024, 5 * 131072);
    const unsigned vo = lane * 16;
    constexpr int D = 4;
    bf16x8 of[D][4], wf[D][5];
    f32x4 acc[4][5];
#pragma unroll
    for (int a = 0; a < 4; ++a)
#pragma unroll
        for (int n = 0; n < 5; ++n) acc[a][n] = (f32x4){0.f, 0.f, 0.f, 0.f};
#pragma unroll
    for (int ks = 0; ks < 16 + D - 1; ++ks) {
        if (ks < 16) {
#pragma unroll
            for (int a = 0; a < 4; ++a) of[ks % D][a] = __builtin_bit_cast(bf16x8, __builtin_amdgcn_raw_buffer_load_b128(so, vo, a * 131072 + ks * 1024, 0));
#pragma unroll
            for (int n = 0; n < 5; ++n) wf[ks % D][n] = __builtin_bit_cast(bf16x8, __builtin_amdgcn_raw_buffer_load_b128(swp, vo, n * 131072 + ks * 1024, 0));
        }
        __builtin_amdgcn_sched_barrier(0);
        if (ks >= D - 1) {
            const int c = (ks - (D - 1)) % D;
#pragma unroll
            for (int a = 0; a < 4; ++a)
#pragma unroll
                for (int n = 0; n < 5; ++n) acc[a][n] = MFMA16(wf[c][n], of[c][a], acc[a][n]);
        }
        __builtin_amdgcn_sched_barrier(0);
    }
#pragma unroll
    for (int a = 0; a < 4; ++a)
#pragma unroll
        for (int n = 0; n < 5; ++n) *(LAS f32x4*)(smem + wid * 20480 + (a * 5 + n) * 1024 + lane * 16) = acc[a][n];
    __syncthreads();
    for (int it = tid; it < 1280; it += 512) {
        f32x4 s = *(const LAS f32x4*)(smem + it * 16);
#pragma unroll
        for (int w = 1; w < 8; ++w) { const f32x4 t = *(const LAS f32x4*)(smem + w * 20480 + it * 16); s += t; }
        const int fr = it >> 6, ln = it & 63, a = fr / 5, n = fr - 5 * a;
        const int q = 64 * qh + 16 * a + (ln & 15), col = br * DM + 80 * nt + 16 * n + 4 * (ln >> 4);
        u32x2 w = {cvtpk(s[0], s[1]), cvtpk(s[2], s[3])};
        *(u32x2*)(out + ((size_t)(b * SQ + q) * (2 * DM) + col) * 2) = w;
    }
}

extern "C" void kernel_launch(void* const* d_in, const int* in_sizes, int n_in, void* d_out, int out_size,
                              void* d_ws, size_t ws_size, hipStream_t stream) {
    char* OF = (char*)d_ws;
    char* WP = OF + OF_BYTES;
    (void)hipFuncSetAttribute((const void*)k1_attn, hipFuncAttributeMaxDynamicSharedMemorySize, LDS1_BYTES);
    (void)hipFuncSetAttribute((const void*)k2_oproj, hipFuncAttributeMaxDynamicSharedMemorySize, 163840);
    hipLaunchKernelGGL(k1_attn, dim3(256), dim3(512), LDS1_BYTES, stream,
                       (const char*)d_in[0], (const char*)d_in[1], (const char*)d_in[2], (const char*)d_in[3], (const char*)d_in[4],
                       (const char*)d_in[5], (const char*)d_in[6], (const char*)d_in[7], (const char*)d_in[8], (const char*)d_in[9],
                       OF, WP);
    hipLaunchKernelGGL(k2_oproj, dim3(256), dim3(512), 163840, stream, (const char*)OF, (const char*)WP, (char*)d_out);
}
```

```cpp
#include <hip/hip_runtime.h>
#include <hip/hip_bf16.h>
#include <stdint.h>

typedef short bf16x8 __attribute__((ext_vector_type(8)));
typedef short s16x4 __attribute__((ext_vector_type(4)));
typedef float f32x16 __attribute__((ext_vector_type(16)));
typedef float f32x4 __attribute__((ext_vector_type(4)));
typedef float f32x2 __attribute__((ext_vector_type(2)));
typedef __bf16 bf16x2_t __attribute__((ext_vector_type(2)));
typedef unsigned u32x4 __attribute__((ext_vector_type(4)));
typedef unsigned u32x2 __attribute__((ext_vector_type(2)));
typedef unsigned long long u64;
#define LAS __attribute__((address_space(3)))
#define GAS __attribute__((address_space(1)))
#define DI __device__ __forceinline__
typedef LAS char* lptr;
typedef __amdgpu_buffer_rsrc_t rsrc_t;

constexpr int NB = 8, SQ = 128, DM = 640, NH = 32, SK = 512, DH = 128;
constexpr float CEXP = 0.08838834764831845f * 1.4426950408889634f;
constexpr int SLOT = 16384;
constexpr int LDS_T = 6 * SLOT;
constexpr int WT = 20480;
constexpr int LDS_BYTES = 10 * SLOT;
constexpr size_t WS_GEN = 0, WS_FLAG = 8192, WS_OF = 1 << 20;
constexpr size_t OF_BYTES = (size_t)2 * NB * NH * 32768;
constexpr size_t WS_PB = WS_OF + OF_BYTES;
constexpr unsigned PB_WG = 4 * 8 * 3 * 1024;
constexpr unsigned SPIN_CAP = 1u << 16;

#define MFMA32(a, b, c) __builtin_amdgcn_mfma_f32_32x32x16_bf16((a), (b), (c), 0, 0, 0)
#define WAITV(n) asm volatile("s_waitcnt vmcnt(" #n ")" ::: "memory")
#define BAR() asm volatile("s_waitcnt lgkmcnt(0)\n\ts_barrier" ::: "memory")
#define SB() __builtin_amdgcn_sched_barrier(0)

#define STAMP(idx)

DI unsigned cvtpk(float lo, float hi) {
    f32x2 v = {lo, hi};
    bf16x2_t b = __builtin_convertvector(v, bf16x2_t);
    return __builtin_bit_cast(unsigned, b);
}
DI bf16x8 pack8(const f32x16& x, int s) {
    u32x4 w = {cvtpk(x[8 * s + 0], x[8 * s + 1]), cvtpk(x[8 * s + 2], x[8 * s + 3]),
               cvtpk(x[8 * s + 4], x[8 * s + 5]), cvtpk(x[8 * s + 6], x[8 * s + 7])};
    return __builtin_bit_cast(bf16x8, w);
}
template <int NT> DI void dma(unsigned lds, unsigned voff, rsrc_t srd, unsigned soff) {
    if (NT) asm volatile("s_mov_b32 m0, %0\n\ts_nop 3\n\tbuffer_load_dwordx4 %1, %2, %3 offen nt lds"
                 :: "s"(lds), "v"(voff), "s"(srd), "s"(soff) : "memory");
    else asm volatile("s_mov_b32 m0, %0\n\ts_nop 3\n\tbuffer_load_dwordx4 %1, %2, %3 offen lds"
                 :: "s"(lds), "v"(voff), "s"(srd), "s"(soff) : "memory");
}
DI void ld_sc1(bf16x8& d, unsigned voff, rsrc_t srd, unsigned soff) {
    asm volatile("s_nop 4\n\tbuffer_load_dwordx4 %0, %1, %2, %3 offen sc1" : "=v"(d) : "v"(voff), "s"(srd), "s"(soff) : "memory");
}
#define WAITV_F(n, f) asm volatile("s_waitcnt vmcnt(" #n ")" : "+v"((f)[0]), "+v"((f)[1]), "+v"((f)[2]), "+v"((f)[3]) :: "memory")
DI bf16x8 lds128(lptr p) { return *(const LAS bf16x8*)p; }
DI s16x4 ldstr(lptr p) {
    typedef short v4i16_t __attribute__((ext_vector_type(4)));
    return __builtin_amdgcn_ds_read_tr16_b64_v4i16((LAS v4i16_t*)p);
}
DI bf16x8 cat(s16x4 a, s16x4 b) { return __builtin_shufflevector(a, b, 0, 1, 2, 3, 4, 5, 6, 7); }
DI float swapmax(float v) {
    auto r = __builtin_amdgcn_permlane32_swap(__float_as_uint(v), __float_as_uint(v), false, false);
    return fmaxf(__uint_as_float(r[0]), __uint_as_float(r[1]));
}
DI float swapsum(float v) {
    auto r = __builtin_amdgcn_permlane32_swap(__float_as_uint(v), __float_as_uint(v), false, false);
    return __uint_as_float(r[0]) + __uint_as_float(r[1]);
}
DI rsrc_t mkrsrc(const void* p, unsigned bytes) {
    return __builtin_amdgcn_make_buffer_rsrc((void*)p, (short)0, (int)bytes, 0x00020000);
}
DI void zero16(f32x16& v) {
#pragma unroll
    for (int r = 0; r < 16; ++r) v[r] = 0.f;
}
DI void wait_flags(const GAS u64* p, u64 tag) {
#pragma unroll 1
    for (unsigned it = 0; it < SPIN_CAP; ++it) {
        const u64 v = __hip_atomic_load(p, __ATOMIC_RELAXED, __HIP_MEMORY_SCOPE_AGENT);
        if (__all(v == tag)) break;
        __builtin_amdgcn_s_sleep(1);
    }
    asm volatile("" ::: "memory");
}

DI void qblock(f32x16* acc, lptr xp0, lptr xp1, lptr wp) {
    bf16x8 xf[2], wf[2][4];
    auto load = [&](int s, int bsel) {
        xf[bsel] = lds128(((s & 1) ? xp1 : xp0) + 512 * (s >> 1));
#pragma unroll
        for (int d = 0; d < 4; ++d) wf[bsel][d] = cat(ldstr(wp + 4096 * s + 512 * d), ldstr(wp + 4096 * s + 512 * d + 256));
    };
    load(0, 0);
#pragma unroll
    for (int s = 0; s < 4; ++s) {
        if (s + 1 < 4) load(s + 1, (s + 1) & 1);
        SB();
#pragma unroll
        for (int d = 0; d < 4; ++d) acc[d] = MFMA32(wf[s & 1][d], xf[s & 1], acc[d]);
        SB();
    }
}
DI void oblock(f32x16* acc, const bf16x8* of, lptr wp) {
    bf16x8 wf[2][5];
    auto load = [&](int ks, int bsel) {
#pragma unroll
        for (int n = 0; n < 5; ++n) wf[bsel][n] = cat(ldstr(wp + 5120 * ks + 512 * n), ldstr(wp + 5120 * ks + 2560 + 512 * n));
    };
    load(0, 0);
#pragma unroll
    for (int ks = 0; ks < 4; ++ks) {
        if (ks + 1 < 4) load(ks + 1, (ks + 1) & 1);
        SB();
#pragma unroll
        for (int n = 0; n < 5; ++n) acc[n] = MFMA32(wf[ks & 1][n], of[ks], acc[n]);
        SB();
    }
}
template <bool PV, bool QK>
DI void mphase(f32x16* o, const bf16x8* pf, f32x16& s0, f32x16& s1, const bf16x8* qf, lptr vp, lptr kp0, lptr kp1) {
    constexpr int GA = PV ? 0 : 4, GB = QK ? 8 : 4;
    bf16x8 fr[3][4];
    auto load = [&](int g, bf16x8* f) {
        if (g < 4) {
#pragma unroll
            for (int d = 0; d < 4; ++d) f[d] = cat(ldstr(vp + 4096 * g + 512 * d), ldstr(vp + 4096 * g + 2048 + 512 * d));
        } else {
            f[0] = lds128(kp0 + 512 * (g - 4)); f[1] = lds128(kp0 + 512 * (g - 4) + 8192);
            f[2] = lds128(kp1 + 512 * (g - 4)); f[3] = lds128(kp1 + 512 * (g - 4) + 8192);
        }
    };
    load(GA, fr[GA % 3]); load(GA + 1, fr[(GA + 1) % 3]);
    if (QK) { zero16(s0); zero16(s1); }
#pragma unroll
    for (int g = GA; g < GB; ++g) {
        if (g + 2 < GB) load(g + 2, fr[(g + 2) % 3]);
        SB();
        const bf16x8* f = fr[g % 3];
        if (g < 4) {
#pragma unroll
            for (int d = 0; d < 4; ++d) o[d] = MFMA32(f[d], pf[g], o[d]);
        } else {
            s0 = MFMA32(f[0], qf[2 * (g - 4)], s0); s1 = MFMA32(f[1], qf[2 * (g - 4)], s1);
            s0 = MFMA32(f[2], qf[2 * (g - 4) + 1], s0); s1 = MFMA32(f[3], qf[2 * (g - 4) + 1], s1);
        }
        SB();
    }
}
DI void softmax(f32x16* o, bf16x8* pf, f32x16& p0, f32x16& p1, float& m, float& l) {
    float mx = p0[0];
#pragma unroll
    for (int r = 1; r < 16; ++r) mx = fmaxf(mx, p0[r]);
#pragma unroll
    for (int r = 0; r < 16; ++r) mx = fmaxf(mx, p1[r]);
    mx = swapmax(mx);
    const float mn = fmaxf(m, mx);
    const float alpha = __builtin_amdgcn_exp2f((m - mn) * CEXP);
    m = mn;
    const float nm = -mn * CEXP;
    float rs0 = 0.f, rs1 = 0.f;
#pragma unroll
    for (int r = 0; r < 16; ++r) {
        p0[r] = __builtin_amdgcn_exp2f(fmaf(p0[r], CEXP, nm)); rs0 += p0[r];
        p1[r] = __builtin_amdgcn_exp2f(fmaf(p1[r], CEXP, nm)); rs1 += p1[r];
    }
    l = l * alpha + (rs0 + rs1);
    pf[0] = pack8(p0, 0); pf[1] = pack8(p0, 1); pf[2] = pack8(p1, 0); pf[3] = pack8(p1, 1);
    if (__any(alpha < 1.f)) {
#pragma unroll
        for (int d = 0; d < 4; ++d)
#pragma unroll
            for (int r = 0; r < 16; ++r) o[d][r] *= alpha;
    }
}

__global__ __launch_bounds__(512, 2) void fused_kernel(
    const char* xA, const char* kA, const char* vA, const char* wqA, const char* woA,
    const char* xB, const char* kB, const char* vB, const char* wqB, const char* woB,
    char* ws, char* out)
{
    extern __shared__ __attribute__((aligned(16))) char smem_[];
    const lptr smem = (lptr)smem_;
    const unsigned lds0 = (unsigned)(uintptr_t)smem;
    const int tid = threadIdx.x, lane = tid & 63;
    const int wid = __builtin_amdgcn_readfirstlane(tid >> 6);
    const int bid = blockIdx.x;
    const int xg = bid & 7, ii = bid >> 3;
    const int br = xg >> 2, xq = xg & 3, b = ii >> 2, jj = ii & 3;
    const int h1 = 8 * xq + 2 * jj;
    const char* x = br ? xB : xA;
    const char* kc = br ? kB : kA;
    const char* vc = br ? vB : vA;
    const char* wq = br ? wqB : wqA;
    const char* wo = br ? woB : woA;
    const int qb = wid & 3, grp = wid >> 2, lw = wid & 3;
    const int l31 = lane & 31, h = lane >> 5;

    u64 gen;
    {
        const char* gp = ws + WS_GEN + (br * NB + b) * 256;
        asm volatile("global_load_dwordx2 %0, %1, off sc1" : "=v"(gen) : "v"(gp) : "memory");
    }

    const rsrc_t sx = mkrsrc(x + (size_t)b * SQ * DM * 2, SQ * DM * 2);
    const rsrc_t sw = mkrsrc(wq + (size_t)h1 * 256, DM * 8192 - h1 * 256);
    const rsrc_t sk = mkrsrc(kc + (size_t)(b * NH + h1) * SK * DH * 2, 2 * SK * DH * 2);
    const rsrc_t sv = mkrsrc(vc + (size_t)(b * NH + h1) * SK * DH * 2, 2 * SK * DH * 2);
    const rsrc_t swo = mkrsrc(wo + (size_t)(8 * xq * 128) * 1280 + 320 * jj, 1024 * 1280 - 320 * jj);
    const rsrc_t sof = mkrsrc(ws + WS_OF + (size_t)((br * NB + b) * NH + 8 * xq) * 32768, 8 * 32768);

    const unsigned dsub = lane >> 5, dr = (lane >> 2) & 7, dsl = lane & 3;
    const unsigned xr0 = 16u * (dsl ^ ((dr >> 2) & 3)), xr1 = 16u * (dsl ^ ((2 + (dr >> 2)) & 3));
    const unsigned vk0 = dr * 256 + dsub * 64 + xr0, vk1 = dr * 256 + dsub * 64 + xr1, vv = dr * 256 + dsub * 64 + 16 * dsl;

    auto issueK = [&](int t, unsigned pair) {
#pragma unroll
        for (int gg = 0; gg < 2; ++gg)
#pragma unroll
            for (int pp = 0; pp < 2; ++pp) {
                const unsigned g = 2 * lw + gg;
                dma<1>(lds0 + pair + 2048 * g + 1024 * pp, gg ? vk1 : vk0, sk, (unsigned)t * 16384u + 2048 * g + 128 * pp);
            }
    };
    auto issueV = [&](int t, unsigned pair) {
#pragma unroll
        for (int gg = 0; gg < 2; ++gg)
#pragma unroll
            for (int pp = 0; pp < 2; ++pp) {
                const unsigned g = 2 * lw + gg;
                dma<1>(lds0 + pair + SLOT + 2048 * g + 1024 * pp, vv, sv, (unsigned)t * 16384u + 2048 * g + 128 * pp);
            }
    };

    const int G1 = (lane >> 4) & 1, tq = (lane & 15) >> 2, tp = lane & 3;
    const int sig = ((tp & 1) << 1) | (tp >> 1);
    const unsigned wqb = SLOT + 2048 * h + 64 * tq + 32 * G1 + 8 * sig;
    const unsigned rowq = 32 * qb + l31;
    const unsigned xsw = (rowq >> 2) & 3;
    const unsigned xb0 = 1024 * (rowq >> 3) + 64 * (rowq & 7) + 16 * ((0 + h) ^ xsw);
    const unsigned xb1 = 1024 * (rowq >> 3) + 64 * (rowq & 7) + 16 * ((2 + h) ^ xsw);
    const unsigned ksw = (l31 >> 2) & 3;
    const unsigned kb0 = 2048 * (l31 >> 3) + 64 * (l31 & 7) + 16 * ((0 + h) ^ ksw);
    const unsigned kb1 = 2048 * (l31 >> 3) + 64 * (l31 & 7) + 16 * ((2 + h) ^ ksw);
    const unsigned trb = 64 * (4 * h + tq) + 32 * G1 + 8 * tp;
    const unsigned vo16 = lane * 16;

    auto flagp = [&](int bb, int stage, int w) { return (GAS u64*)(ws + WS_FLAG + (size_t)((bb * 3 + stage) * 8 + w) * 64); };
    auto partner = [&](int dj) { return (bid & ~24) | (((jj + dj) & 3) << 3); };

    f32x16 acc[5];
    bf16x8 qf[8], pf[4];
    f32x16 s0, s1;
    float m, l;
    u64 tag;

    auto epilogue = [&](int head_local) {
        const float inv = 1.f / swapsum(l);
#pragma unroll
        for (int d = 0; d < 4; ++d) {
#pragma unroll
            for (int r = 0; r < 16; ++r) acc[d][r] *= inv;
            qf[2 * d] = pack8(acc[d], 0); qf[2 * d + 1] = pack8(acc[d], 1);
        }
#pragma unroll
        for (int f = 0; f < 8; ++f)
            __builtin_amdgcn_raw_buffer_store_b128(__builtin_bit_cast(u32x4, qf[f]), sof, vo16, head_local * 32768 + qb * 8192 + f * 1024, 16);
    };

    if (grp == 0) {
        STAMP(0);
#pragma unroll
        for (int d = 0; d < 4; ++d) zero16(acc[d]);
        {
            unsigned st = 4 * SLOT;
#pragma unroll 1
            for (int i = 0; i < 10; ++i) {
                BAR();
                qblock(acc, smem + st + xb0, smem + st + xb1, smem + st + wqb);
                st = (st == 8 * SLOT) ? 4 * SLOT : st + 2 * SLOT;
            }
        }
#pragma unroll
        for (int d = 0; d < 4; ++d) { qf[2 * d] = pack8(acc[d], 0); qf[2 * d + 1] = pack8(acc[d], 1); zero16(acc[d]); }
        m = -__builtin_inff(); l = 0.f;
        STAMP(1);
        BAR();
        mphase<false, true>(acc, pf, s0, s1, qf, smem, smem + kb0, smem + kb1);
        BAR();
        softmax(acc, pf, s0, s1, m, l);
        {
            unsigned pv = 0, pk = 2 * SLOT;
#pragma unroll 1
            for (int t = 1; t < 8; ++t) {
                STAMP(2 * t);
                BAR();
                mphase<true, true>(acc, pf, s0, s1, qf, smem + pv + SLOT + trb, smem + pk + kb0, smem + pk + kb1);
                STAMP(2 * t + 1);
                BAR();
                softmax(acc, pf, s0, s1, m, l);
                pv = pk; pk = (pk == 4 * SLOT) ? 0 : pk + 2 * SLOT;
            }
            STAMP(16);
            BAR();
            mphase<true, false>(acc, pf, s0, s1, qf, smem + pv + SLOT + trb, smem, smem);
        }
        asm volatile("s_waitcnt vmcnt(0)" : "+v"(gen) :: "memory");
        tag = gen + 1;
        STAMP(17);
        BAR();
        const unsigned vwA = dr * 1280 + dsub * 64 + 16 * dsl, vwB = dr * 1280 + 16 * dsl + (dsub ? 8 * 1280 : 256);
        auto issueW = [&](int hl, int half, int buf) {
            const unsigned so = (unsigned)(hl * 128 + 64 * half + 16 * lw) * 1280u, ld = lds0 + buf * WT + 5 * lw * 1024;
            dma<0>(ld, vwA, swo, so); dma<0>(ld + 1024, vwA, swo, so + 128); dma<0>(ld + 2048, vwB, swo, so);
            dma<0>(ld + 3072, vwA, swo, so + 8 * 1280 + 64); dma<0>(ld + 4096, vwA, swo, so + 8 * 1280 + 192);
        };
        auto hl1 = [&](int dj) { return 2 * ((jj + dj) & 3); };
        bf16x8 ofa[4], ofb[4];
        auto issueO = [&](bf16x8* f, int hl, int half) {
#pragma unroll
            for (int k = 0; k < 4; ++k) ld_sc1(f[k], vo16, sof, hl * 32768 + qb * 8192 + (4 * half + k) * 1024);
        };
        epilogue(2 * jj);
        issueW(hl1(0), 0, 5); issueW(hl1(0), 1, 6);
        issueV(10, 2 * SLOT); issueK(11, 4 * SLOT);
#pragma unroll
        for (int n = 0; n < 5; ++n) zero16(acc[n]);
        WAITV(18);
        if (lane == 0) __hip_atomic_store(flagp(bid, 0, qb), tag, __ATOMIC_RELAXED, __HIP_MEMORY_SCOPE_AGENT);
        STAMP(18);
        BAR();
        issueW(hl1(1), 0, 7);
        WAITV(18);
        BAR();
        issueV(11, 4 * SLOT); issueK(12, 0);
        oblock(acc, qf, smem + 5 * WT + trb);
        BAR();
        wait_flags(flagp(partner(1 + (lane % 3)), 0, qb), tag);
        issueO(ofa, hl1(1), 0); issueW(hl1(1), 1, 5);
        WAITV(22);
        BAR();
        issueV(12, 0); issueK(13, 2 * SLOT);
        oblock(acc, qf + 4, smem + 6 * WT + trb);
        BAR();
        issueO(ofb, hl1(1), 1); issueW(hl1(2), 0, 6);
        WAITV_F(22, ofa);
        BAR();
        issueV(13, 2 * SLOT); issueK(14, 4 * SLOT);
        oblock(acc, ofa, smem + 7 * WT + trb);
        BAR();
        issueO(ofa, hl1(2), 0); issueW(hl1(2), 1, 7);
        WAITV_F(22, ofb);
        BAR();
        issueV(14, 4 * SLOT); issueK(15, 0);
        oblock(acc, ofb, smem + 5 * WT + trb);
        BAR();
        issueO(ofb, hl1(2), 1); issueW(hl1(3), 0, 5);
        WAITV_F(22, ofa);
        BAR();
        issueV(15, 0);
        oblock(acc, ofa, smem + 6 * WT + trb);
        BAR();
        issueO(ofa, hl1(3), 0); issueW(hl1(3), 1, 6);
        WAITV_F(18, ofb);
        BAR();
        issueW(hl1(0) + 1, 0, 2); issueW(hl1(0) + 1, 1, 3);
        oblock(acc, ofb, smem + 7 * WT + trb);
        BAR();
        issueO(ofb, hl1(3), 1); issueW(hl1(1) + 1, 0, 7);
        WAITV_F(24, ofa);
        BAR();
        issueW(hl1(1) + 1, 1, 4);
        oblock(acc, ofa, smem + 5 * WT + trb);
        WAITV_F(10, ofb);
        BAR();
        issueW(hl1(2) + 1, 0, 5);
        oblock(acc, ofb, smem + 6 * WT + trb);
        STAMP(19);
        BAR();
        issueW(hl1(3) + 1, 0, 0); issueW(hl1(2) + 1, 1, 6); issueW(hl1(3) + 1, 1, 1);
        WAITV(0);
        BAR();
        wait_flags(flagp(partner(1 + (lane & 1)), 1, qb), tag);
        STAMP(20);
        bf16x8 of2[16];
#pragma unroll
        for (int k = 0; k < 16; ++k)
            of2[k] = __builtin_bit_cast(bf16x8, __builtin_amdgcn_raw_buffer_load_b128(sof, vo16, (hl1(1 + (k >> 3)) + 1) * 32768 + qb * 8192 + (k & 7) * 1024, 16));
        oblock(acc, of2, smem + 7 * WT + trb);
        oblock(acc, of2 + 4, smem + 4 * WT + trb);
        oblock(acc, of2 + 8, smem + 5 * WT + trb);
        oblock(acc, of2 + 12, smem + 6 * WT + trb);
        STAMP(21);
    } else {
        STAMP(0);
        const unsigned vx0 = dr * 1280 + dsub * 64 + xr0, vx1 = dr * 1280 + dsub * 64 + xr1, vw = dr * 8192 + dsub * 64 + 16 * dsl;
        auto issueQ = [&](int i, int head, unsigned st) {
#pragma unroll
            for (int gg = 0; gg < 4; ++gg) {
                const unsigned g = 4 * lw + gg;
                dma<0>(lds0 + st + 1024 * g, (gg & 1) ? vx1 : vx0, sx, g * 8 * 1280 + 128 * i);
            }
#pragma unroll
            for (int gg = 0; gg < 2; ++gg)
#pragma unroll
                for (int pp = 0; pp < 2; ++pp) {
                    const unsigned g = 2 * lw + gg;
                    dma<0>(lds0 + st + SLOT + 2048 * g + 1024 * pp, vw, sw, (unsigned)(64 * i + 8 * g) * 8192u + 256 * head + 128 * pp);
                }
        };
        constexpr unsigned T0 = 6 * SLOT, T1 = 8 * SLOT, R45 = 4 * SLOT;
        issueQ(0, 0, R45); issueQ(1, 0, T0); issueQ(2, 0, T1);
        WAITV(16); BAR();
        WAITV(8);  BAR(); issueQ(3, 0, R45); issueK(0, 0);
        WAITV(12); BAR(); issueQ(4, 0, T0);  issueV(0, 0);
        WAITV(16); BAR(); issueQ(5, 0, T1);  issueK(1, 2 * SLOT);
        WAITV(16); BAR(); issueQ(6, 0, R45); issueV(1, 2 * SLOT);
        WAITV(16); BAR(); issueQ(7, 0, T0);
        WAITV(12); BAR(); issueQ(8, 0, T1);
        WAITV(8);  BAR(); issueQ(9, 0, R45);
        WAITV(8);  BAR(); issueQ(0, 1, T0);
        WAITV(8);  BAR(); issueQ(1, 1, T1);
#pragma unroll
        for (int d = 0; d < 4; ++d) zero16(acc[d]);
        asm volatile("s_waitcnt vmcnt(8)" : "+v"(gen) :: "memory");
        tag = gen + 1;
        STAMP(1);
        BAR();
        issueK(2, 4 * SLOT); issueV(2, 4 * SLOT);
        qblock(acc, smem + T0 + xb0, smem + T0 + xb1, smem + T0 + wqb);
        WAITV(8);
        BAR();
        issueQ(2, 1, T0); issueK(3, 0);
        qblock(acc, smem + T1 + xb0, smem + T1 + xb1, smem + T1 + wqb);
        BAR();
        issueQ(3, 1, T1);
        WAITV(12);
        BAR();
        issueV(3, 0); issueK(4, 2 * SLOT);
        qblock(acc, smem + T0 + xb0, smem + T0 + xb1, smem + T0 + wqb);
        {
            unsigned pv = 2 * SLOT, pk = 4 * SLOT;
#pragma unroll 1
            for (int mm = 2; mm < 8; ++mm) {
                const unsigned tf = (mm & 1) ? T1 : T0, tc = (mm & 1) ? T0 : T1;
                BAR();
                issueQ(mm + 2, 1, tf);
                WAITV(16);
                BAR();
                issueV(mm + 2, pv); issueK(mm + 3, pk);
                qblock(acc, smem + tc + xb0, smem + tc + xb1, smem + tc + wqb);
                pv = pk; pk = (pk == 4 * SLOT) ? 0 : pk + 2 * SLOT;
            }
        }
        WAITV(8);
        STAMP(16);
        BAR();
        qblock(acc, smem + T1 + xb0, smem + T1 + xb1, smem + T1 + wqb);
#pragma unroll
        for (int d = 0; d < 4; ++d) { qf[2 * d] = pack8(acc[d], 0); qf[2 * d + 1] = pack8(acc[d], 1); zero16(acc[d]); }
        m = -__builtin_inff(); l = 0.f;
        mphase<false, true>(acc, pf, s0, s1, qf, smem, smem + 4 * SLOT + kb0, smem + 4 * SLOT + kb1);
        STAMP(17);
        BAR();
        softmax(acc, pf, s0, s1, m, l);
        WAITV(0);
        {
            unsigned pv = 4 * SLOT, pk = 0;
#pragma unroll 1
            for (int t = 9; t < 16; ++t) {
                STAMP(2 * t);
                BAR();
                mphase<true, true>(acc, pf, s0, s1, qf, smem + pv + SLOT + trb, smem + pk + kb0, smem + pk + kb1);
                STAMP(2 * t + 1);
                BAR();
                softmax(acc, pf, s0, s1, m, l);
                pv = pk; pk = (pk == 4 * SLOT) ? 0 : pk + 2 * SLOT;
            }
            STAMP(32);
            BAR();
            mphase<true, false>(acc, pf, s0, s1, qf, smem + pv + SLOT + trb, smem, smem);
        }
        STAMP(33);
        BAR();
        epilogue(2 * jj + 1);
#pragma unroll
        for (int n = 0; n < 5; ++n) zero16(acc[n]);
        oblock(acc, qf, smem + 2 * WT + trb);
        WAITV(0);
        if (lane == 0) __hip_atomic_store(flagp(bid, 1, qb), tag, __ATOMIC_RELAXED, __HIP_MEMORY_SCOPE_AGENT);
        STAMP(34);
        oblock(acc, qf + 4, smem + 3 * WT + trb);
        BAR();
        wait_flags(flagp(partner(3), 1, qb), tag);
        STAMP(35);
        bf16x8 of2[8];
#pragma unroll
        for (int k = 0; k < 8; ++k)
            of2[k] = __builtin_bit_cast(bf16x8, __builtin_amdgcn_raw_buffer_load_b128(sof, vo16, (2 * ((jj + 3) & 3) + 1) * 32768 + qb * 8192 + k * 1024, 16));
        oblock(acc, of2, smem + 0 * WT + trb);
        oblock(acc, of2 + 4, smem + 1 * WT + trb);
        STAMP(36);
    }
    BAR();
    {
        const lptr xw = smem + wid * 12288 + vo16;
        const lptr xr = smem + (wid ^ 4) * 12288 + vo16;
        f32x16 keep[3];
        if (grp == 0) {
#pragma unroll
            for (int n = 0; n < 2; ++n)
#pragma unroll
                for (int g = 0; g < 4; ++g) *(LAS f32x4*)(xw + (n * 4 + g) * 1024) = (f32x4){acc[3 + n][4 * g], acc[3 + n][4 * g + 1], acc[3 + n][4 * g + 2], acc[3 + n][4 * g + 3]};
            keep[0] = acc[0]; keep[1] = acc[1]; keep[2] = acc[2];
        } else {
#pragma unroll
            for (int n = 0; n < 3; ++n)
#pragma unroll
                for (int g = 0; g < 4; ++g) *(LAS f32x4*)(xw + (n * 4 + g) * 1024) = (f32x4){acc[n][4 * g], acc[n][4 * g + 1], acc[n][4 * g + 2], acc[n][4 * g + 3]};
            keep[0] = acc[3]; keep[1] = acc[4]; keep[2] = acc[4];
        }
        BAR();
        const int nk = grp ? 2 : 3;
        const rsrc_t spb = mkrsrc(ws + WS_PB, 256 * PB_WG);
        f32x4 mine[3];
#pragma unroll
        for (int n = 0; n < 3; ++n) {
            if (n < nk) {
#pragma unroll
                for (int g = 0; g < 4; ++g) {
                    const f32x4 u = *(const LAS f32x4*)(xr + (n * 4 + g) * 1024);
                    const f32x4 v = {keep[n][4 * g] + u[0], keep[n][4 * g + 1] + u[1], keep[n][4 * g + 2] + u[2], keep[n][4 * g + 3] + u[3]};
                    if (g == xq) mine[n] = v;
                    else __builtin_amdgcn_raw_buffer_store_b128(__builtin_bit_cast(u32x4, v), spb, vo16,
                             (unsigned)((bid & ~3) | g) * PB_WG + ((xq * 8 + wid) * 3 + n) * 1024, 16);
                }
            }
        }
        WAITV(0);
        if (lane == 0) __hip_atomic_store(flagp(bid, 2, wid), tag, __ATOMIC_RELAXED, __HIP_MEMORY_SCOPE_AGENT);
        STAMP(40);
        wait_flags(flagp((bid & ~3) | ((xq + 1 + (lane % 3)) & 3), 2, wid), tag);
        STAMP(41);
#pragma unroll
        for (int n = 0; n < 3; ++n) {
            if (n < nk) {
                f32x4 u[3];
#pragma unroll
                for (int s = 0; s < 3; ++s)
                    u[s] = __builtin_bit_cast(f32x4, __builtin_amdgcn_raw_buffer_load_b128(spb, vo16,
                               (unsigned)bid * PB_WG + ((((xq + 1 + s) & 3) * 8 + wid) * 3 + n) * 1024, 16));
                const f32x4 v = (mine[n] + u[0]) + (u[1] + u[2]);
                const u32x2 w = {cvtpk(v[0], v[1]), cvtpk(v[2], v[3])};
                const int col = br * DM + 160 * jj + 32 * (grp ? 3 + n : n) + 8 * xq + 4 * h;
                *(u32x2*)(out + ((size_t)(b * SQ + rowq) * (2 * DM) + col) * 2) = w;
            }
        }
        if (xq == 0 && jj == 0 && wid == 0 && lane == 0)
            __hip_atomic_store((GAS u64*)(ws + WS_GEN + (br * NB + b) * 256), tag, __ATOMIC_RELAXED, __HIP_MEMORY_SCOPE_AGENT);
        STAMP(42);
    }
}

extern "C" void kernel_launch(void* const* d_in, const int* in_sizes, int n_in, void* d_out, int out_size,
                              void* d_ws, size_t ws_size, hipStream_t stream) {
    (void)hipFuncSetAttribute((const void*)fused_kernel, hipFuncAttributeMaxDynamicSharedMemorySize, LDS_BYTES);
    hipLaunchKernelGGL(fused_kernel, dim3(256), dim3(512), LDS_BYTES, stream,
                       (const char*)d_in[0], (const char*)d_in[1], (const char*)d_in[2], (const char*)d_in[3], (const char*)d_in[4],
                       (const char*)d_in[5], (const char*)d_in[6], (const char*)d_in[7], (const char*)d_in[8], (const char*)d_in[9],
                       (char*)d_ws, (char*)d_out);
}
```

```cpp
#include <hip/hip_runtime.h>
#include <hip/hip_bf16.h>
#include <stdint.h>

typedef short bf16x8 __attribute__((ext_vector_type(8)));
typedef short s16x4 __attribute__((ext_vector_type(4)));
typedef float f32x16 __attribute__((ext_vector_type(16)));
typedef float f32x4 __attribute__((ext_vector_type(4)));
typedef float f32x2 __attribute__((ext_vector_type(2)));
typedef __bf16 bf16x2_t __attribute__((ext_vector_type(2)));
typedef unsigned u32x4 __attribute__((ext_vector_type(4)));
typedef unsigned u32x2 __attribute__((ext_vector_type(2)));
typedef unsigned long long u64;
#define LAS __attribute__((address_space(3)))
#define GAS __attribute__((address_space(1)))
#define DI __device__ __forceinline__
typedef LAS char* lptr;
typedef __amdgpu_buffer_rsrc_t rsrc_t;

constexpr int NB = 8, SQ = 128, DM = 640, NH = 32, SK = 512, DH = 128;
constexpr float CEXP = 0.08838834764831845f * 1.4426950408889634f;
constexpr int SLOT = 16384;
constexpr int LDS_T = 6 * SLOT;
constexpr int WT = 20480;
constexpr int LDS_BYTES = 10 * SLOT;
constexpr size_t WS_GEN = 0, WS_FLAG = 8192, WS_OF = 1 << 20;
constexpr size_t OF_BYTES = (size_t)2 * NB * NH * 32768;
constexpr size_t WS_PB = WS_OF + OF_BYTES;
constexpr unsigned PB_WG = 4 * 8 * 3 * 1024;
constexpr unsigned SPIN_CAP = 1u << 16;

#define MFMA32(a, b, c) __builtin_amdgcn_mfma_f32_32x32x16_bf16((a), (b), (c), 0, 0, 0)
#define WAITV(n) asm volatile("s_waitcnt vmcnt(" #n ")" ::: "memory")
#define SB() __builtin_amdgcn_sched_barrier(0)
#define BAR() do { SB(); asm volatile("s_waitcnt lgkmcnt(0)\n\ts_barrier" ::: "memory"); SB(); } while (0)

#define STAMP(idx)

DI unsigned cvtpk(float lo, float hi) {
    f32x2 v = {lo, hi};
    bf16x2_t b = __builtin_convertvector(v, bf16x2_t);
    return __builtin_bit_cast(unsigned, b);
}
DI bf16x8 pack8(const f32x16& x, int s) {
    u32x4 w = {cvtpk(x[8 * s + 0], x[8 * s + 1]), cvtpk(x[8 * s + 2], x[8 * s + 3]),
               cvtpk(x[8 * s + 4], x[8 * s + 5]), cvtpk(x[8 * s + 6], x[8 * s + 7])};
    return __builtin_bit_cast(bf16x8, w);
}
template <int NT> DI void dma(unsigned lds, unsigned voff, rsrc_t srd, unsigned soff) {
    if (NT) asm volatile("s_mov_b32 m0, %0\n\ts_nop 3\n\tbuffer_load_dwordx4 %1, %2, %3 offen nt lds"
                 :: "s"(lds), "v"(voff), "s"(srd), "s"(soff) : "memory");
    else asm volatile("s_mov_b32 m0, %0\n\ts_nop 3\n\tbuffer_load_dwordx4 %1, %2, %3 offen lds"
                 :: "s"(lds), "v"(voff), "s"(srd), "s"(soff) : "memory");
}
DI void ld_sc1(bf16x8& d, unsigned voff, rsrc_t srd, unsigned soff) {
    asm volatile("s_nop 4\n\tbuffer_load_dwordx4 %0, %1, %2, %3 offen sc1" : "+v"(d) : "v"(voff), "s"(srd), "s"(soff) : "memory");
}
#define WAITV_F(n, f) asm volatile("s_waitcnt vmcnt(" #n ")" : "+v"((f)[0]), "+v"((f)[1]), "+v"((f)[2]), "+v"((f)[3]) :: "memory")
DI bf16x8 lds128(lptr p) { return *(const LAS bf16x8*)p; }
DI s16x4 ldstr(lptr p) {
    typedef short v4i16_t __attribute__((ext_vector_type(4)));
    return __builtin_amdgcn_ds_read_tr16_b64_v4i16((LAS v4i16_t*)p);
}
DI bf16x8 cat(s16x4 a, s16x4 b) { return __builtin_shufflevector(a, b, 0, 1, 2, 3, 4, 5, 6, 7); }
DI float swapmax(float v) {
    auto r = __builtin_amdgcn_permlane32_swap(__float_as_uint(v), __float_as_uint(v), false, false);
    return fmaxf(__uint_as_float(r[0]), __uint_as_float(r[1]));
}
DI float swapsum(float v) {
    auto r = __builtin_amdgcn_permlane32_swap(__float_as_uint(v), __float_as_uint(v), false, false);
    return __uint_as_float(r[0]) + __uint_as_float(r[1]);
}
DI rsrc_t mkrsrc(const void* p, unsigned bytes) {
    return __builtin_amdgcn_make_buffer_rsrc((void*)p, (short)0, (int)bytes, 0x00020000);
}
DI void zero16(f32x16& v) {
#pragma unroll
    for (int r = 0; r < 16; ++r) v[r] = 0.f;
}
DI void wait_flags(const GAS u64* p, u64 tag) {
#pragma unroll 1
    for (unsigned it = 0; it < SPIN_CAP; ++it) {
        const u64 v = __hip_atomic_load(p, __ATOMIC_RELAXED, __HIP_MEMORY_SCOPE_AGENT);
        if (__all(v == tag)) break;
        __builtin_amdgcn_s_sleep(1);
    }
    asm volatile("" ::: "memory");
}

DI float max3f(float a, float b, float c) { float r; asm("v_max3_f32 %0, %1, %2, %3" : "=v"(r) : "v"(a), "v"(b), "v"(c)); return r; }

template <class H> DI void qblock(f32x16* acc, lptr xp0, lptr xp1, lptr wp, H hook) {
    bf16x8 xf[3], wf[3][4];
    auto load = [&](int s) {
        xf[s % 3] = lds128(((s & 1) ? xp1 : xp0) + 512 * (s >> 1));
#pragma unroll
        for (int d = 0; d < 4; ++d) wf[s % 3][d] = cat(ldstr(wp + 4096 * s + 512 * d), ldstr(wp + 4096 * s + 512 * d + 256));
    };
    load(0); SB(); load(1);
#pragma unroll
    for (int s = 0; s < 4; ++s) {
        SB();
#pragma unroll
        for (int d = 0; d < 4; ++d) {
            acc[d] = MFMA32(wf[s % 3][d], xf[s % 3], acc[d]);
            SB();
            if (d == 0 && s + 2 < 4) load(s + 2);
            if (d & 1) hook(2 * s + (d >> 1));
            SB();
        }
    }
}
template <class H> DI void oblock(f32x16* acc, const bf16x8* of, lptr wp, H hook) {
    bf16x8 wf[3][5];
    auto load = [&](int ks) {
#pragma unroll
        for (int n = 0; n < 5; ++n) wf[ks % 3][n] = cat(ldstr(wp + 5120 * ks + 512 * n), ldstr(wp + 5120 * ks + 2560 + 512 * n));
    };
    load(0); SB(); load(1);
#pragma unroll
    for (int ks = 0; ks < 4; ++ks) {
        SB();
#pragma unroll
        for (int n = 0; n < 5; ++n) {
            acc[n] = MFMA32(wf[ks % 3][n], of[ks], acc[n]);
            SB();
            if (n == 0 && ks + 2 < 4) load(ks + 2);
            if (n == 1 || n == 3) hook(2 * ks + (n >> 1));
            SB();
        }
    }
}
struct NoHook { DI void operator()(int) const {} };

DI void aload(int g, bf16x8* f, lptr vp, lptr kp0, lptr kp1) {
    if (g < 4) {
#pragma unroll
        for (int d = 0; d < 4; ++d) f[d] = cat(ldstr(vp + 4096 * g + 512 * d), ldstr(vp + 4096 * g + 2048 + 512 * d));
    } else {
        f[0] = lds128(kp0 + 512 * (g - 4)); f[1] = lds128(kp0 + 512 * (g - 4) + 8192);
        f[2] = lds128(kp1 + 512 * (g - 4)); f[3] = lds128(kp1 + 512 * (g - 4) + 8192);
    }
}
DI void qkpart(f32x16& s0, f32x16& s1, const bf16x8* qf, bf16x8 (*fr)[4], lptr kp0, lptr kp1) {
    zero16(s0); zero16(s1);
#pragma unroll
    for (int g = 4; g < 8; ++g) {
        const bf16x8* f = fr[g % 3];
        SB();
        s0 = MFMA32(f[0], qf[2 * (g - 4)], s0);
        SB();
        if (g + 2 < 8) aload(g + 2, fr[(g + 2) % 3], kp0, kp0, kp1);
        SB();
        s1 = MFMA32(f[1], qf[2 * (g - 4)], s1);
        s0 = MFMA32(f[2], qf[2 * (g - 4) + 1], s0); s1 = MFMA32(f[3], qf[2 * (g - 4) + 1], s1);
        SB();
    }
}
DI void pvpart(f32x16* o, const bf16x8* pf, bf16x8 (*fr)[4], lptr vp, lptr kp0, lptr kp1) {
#pragma unroll
    for (int g = 0; g < 4; ++g) {
        const bf16x8* f = fr[g % 3];
        SB();
        o[0] = MFMA32(f[0], pf[g], o[0]);
        SB();
        aload(g + 2, fr[(g + 2) % 3], vp, kp0, kp1);
        SB();
#pragma unroll
        for (int d = 1; d < 4; ++d) o[d] = MFMA32(f[d], pf[g], o[d]);
        SB();
    }
}
DI void softmax(f32x16* o, bf16x8* pf, f32x16& p0, f32x16& p1, float& m, float& l, bf16x8 (*fr)[4], lptr vp) {
    float c0 = max3f(p0[0], p0[1], p0[2]), c1 = max3f(p0[8], p0[9], p0[10]), c2 = max3f(p1[0], p1[1], p1[2]), c3 = max3f(p1[8], p1[9], p1[10]);
    c0 = max3f(c0, p0[3], p0[4]); c1 = max3f(c1, p0[11], p0[12]); c2 = max3f(c2, p1[3], p1[4]); c3 = max3f(c3, p1[11], p1[12]);
    c0 = max3f(c0, p0[5], p0[6]); c1 = max3f(c1, p0[13], p0[14]); c2 = max3f(c2, p1[5], p1[6]); c3 = max3f(c3, p1[13], p1[14]);
    c0 = max3f(c0, p0[7], p0[15]); c2 = max3f(c2, p1[7], p1[15]);
    c0 = max3f(c0, c1, m); c0 = max3f(c0, c2, c3);
    auto r_ = __builtin_amdgcn_permlane32_swap(__float_as_uint(c0), __float_as_uint(c0), false, false);
    const float mn = max3f(__uint_as_float(r_[0]), __uint_as_float(r_[1]), m);
    const float alpha = __builtin_amdgcn_exp2f((m - mn) * CEXP);
    m = mn;
    const float nm = -mn * CEXP;
    SB();
#pragma unroll
    for (int r = 0; r < 16; ++r) { p0[r] = fmaf(p0[r], CEXP, nm); p1[r] = fmaf(p1[r], CEXP, nm); }
    SB();
#pragma unroll
    for (int r = 0; r < 16; ++r) { p0[r] = __builtin_amdgcn_exp2f(p0[r]); p1[r] = __builtin_amdgcn_exp2f(p1[r]); }
    SB();
    f32x2 a0 = {p0[0], p0[1]}, a1 = {p0[2], p0[3]}, a2 = {p1[0], p1[1]}, a3 = {p1[2], p1[3]};
#pragma unroll
    for (int r = 4; r < 16; r += 4) {
        a0 += (f32x2){p0[r], p0[r + 1]}; a1 += (f32x2){p0[r + 2], p0[r + 3]};
        a2 += (f32x2){p1[r], p1[r + 1]}; a3 += (f32x2){p1[r + 2], p1[r + 3]};
    }
    pf[0] = pack8(p0, 0); pf[1] = pack8(p0, 1); pf[2] = pack8(p1, 0); pf[3] = pack8(p1, 1);
    a0 = (a0 + a1) + (a2 + a3);
    l = l * alpha + (a0[0] + a0[1]);
    SB();
    aload(0, fr[0], vp, vp, vp); aload(1, fr[1], vp, vp, vp);
    SB();
    if (__any(alpha < 1.f)) {
#pragma unroll
        for (int d = 0; d < 4; ++d)
#pragma unroll
            for (int r = 0; r < 16; ++r) o[d][r] *= alpha;
    }
}

__global__ __launch_bounds__(512, 2) void fused_kernel(
    const char* xA, const char* kA, const char* vA, const char* wqA, const char* woA,
    const char* xB, const char* kB, const char* vB, const char* wqB, const char* woB,
    char* ws, char* out)
{
    extern __shared__ __attribute__((aligned(16))) char smem_[];
    const lptr smem = (lptr)smem_;
    const unsigned lds0 = (unsigned)(uintptr_t)smem;
    const int tid = threadIdx.x, lane = tid & 63;
    const int wid = __builtin_amdgcn_readfirstlane(tid >> 6);
    const int bid = blockIdx.x;
    const int xg = bid & 7, ii = bid >> 3;
    const int br = xg >> 2, xq = xg & 3, b = ii >> 2, jj = ii & 3;
    const int h1 = 8 * xq + 2 * jj;
    const char* x = br ? xB : xA;
    const char* kc = br ? kB : kA;
    const char* vc = br ? vB : vA;
    const char* wq = br ? wqB : wqA;
    const char* wo = br ? woB : woA;
    const int qb = wid & 3, grp = wid >> 2, lw = wid & 3;
    const int l31 = lane & 31, h = lane >> 5;
    STAMP(0);

    u64 gen;
    {
        const char* gp = ws + WS_GEN + (br * NB + b) * 256;
        asm volatile("global_load_dwordx2 %0, %1, off sc1" : "=v"(gen) : "v"(gp) : "memory");
    }

    const rsrc_t sk = mkrsrc(kc + (size_t)(b * NH + h1) * SK * DH * 2, 2 * SK * DH * 2);
    const rsrc_t sv = mkrsrc(vc + (size_t)(b * NH + h1) * SK * DH * 2, 2 * SK * DH * 2);
    const rsrc_t sof = mkrsrc(ws + WS_OF + (size_t)((br * NB + b) * NH + 8 * xq) * 32768, 8 * 32768);

    const unsigned dsub = lane >> 5, dr = (lane >> 2) & 7, dsl = lane & 3;
    const unsigned xr0 = 16u * (dsl ^ ((dr >> 2) & 3)), xr1 = 16u * (dsl ^ ((2 + (dr >> 2)) & 3));
    const unsigned vk0 = dr * 256 + dsub * 64 + xr0, vk1 = dr * 256 + dsub * 64 + xr1, vv = dr * 256 + dsub * 64 + 16 * dsl;

    auto pieceK = [&](int t, unsigned pair, int i) {
        dma<1>(lds0 + pair + 4096 * lw + 1024 * i, (i >> 1) ? vk1 : vk0, sk, (unsigned)t * 16384u + 4096 * lw + 2048 * (i >> 1) + 128 * (i & 1));
    };
    auto pieceV = [&](int t, unsigned pair, int i) {
        dma<1>(lds0 + pair + SLOT + 4096 * lw + 1024 * i, vv, sv, (unsigned)t * 16384u + 4096 * lw + 2048 * (i >> 1) + 128 * (i & 1));
    };
    auto nextpair = [](unsigned p) { return (p == 4u * SLOT) ? 0u : p + 2u * SLOT; };

    const int G1 = (lane >> 4) & 1, tq = (lane & 15) >> 2, tp = lane & 3;
    const int sig = ((tp & 1) << 1) | (tp >> 1);
    const unsigned wqb = SLOT + 2048 * h + 64 * tq + 32 * G1 + 8 * sig;
    const unsigned rowq = 32 * qb + l31;
    const unsigned xsw = (rowq >> 2) & 3;
    const unsigned xb0 = 1024 * (rowq >> 3) + 64 * (rowq & 7) + 16 * ((0 + h) ^ xsw);
    const unsigned xb1 = 1024 * (rowq >> 3) + 64 * (rowq & 7) + 16 * ((2 + h) ^ xsw);
    const unsigned ksw = (l31 >> 2) & 3;
    const unsigned kb0 = 2048 * (l31 >> 3) + 64 * (l31 & 7) + 16 * ((0 + h) ^ ksw);
    const unsigned kb1 = 2048 * (l31 >> 3) + 64 * (l31 & 7) + 16 * ((2 + h) ^ ksw);
    const unsigned trb = 64 * (4 * h + tq) + 32 * G1 + 8 * tp;
    const unsigned vo16 = lane * 16;

    auto flagp = [&](int bb, int stage, int w) { return (GAS u64*)(ws + WS_FLAG + (size_t)((bb * 3 + stage) * 8 + w) * 64); };
    auto partner = [&](int dj) { return (bid & ~24) | (((jj + dj) & 3) << 3); };
    auto hl1 = [&](int dj) { return 2 * ((jj + dj) & 3); };

    f32x16 acc[5];
    bf16x8 qf[8], pf[4], fr[3][4];
    f32x16 s0, s1;
#pragma unroll
    for (int d = 0; d < 4; ++d) zero16(acc[d]);

    if (grp == 0) {
#pragma unroll
        for (int i = 0; i < 4; ++i) pieceK(0, 0, i);
#pragma unroll
        for (int i = 0; i < 4; ++i) pieceV(0, 0, i);
#pragma unroll
        for (int i = 0; i < 4; ++i) pieceK(1, 2 * SLOT, i);
        unsigned st = 4 * SLOT;
#pragma unroll 1
        for (int i = 0; i < 10; ++i) {
            BAR();
            qblock(acc, smem + st + xb0, smem + st + xb1, smem + st + wqb, NoHook());
            st = (st == 8 * SLOT) ? 4 * SLOT : st + 2 * SLOT;
        }
        WAITV(0);
        STAMP(1);
        BAR();
    } else {
        const rsrc_t sx = mkrsrc(x + (size_t)b * SQ * DM * 2, SQ * DM * 2);
        const rsrc_t sw = mkrsrc(wq + (size_t)h1 * 256, DM * 8192 - h1 * 256);
        const unsigned vx0 = dr * 1280 + dsub * 64 + xr0, vx1 = dr * 1280 + dsub * 64 + xr1, vw = dr * 8192 + dsub * 64 + 16 * dsl;
        auto issueQ = [&](int u, unsigned st) {
            const int hd = u >= 10, i = u - 10 * hd;
#pragma unroll
            for (int gg = 0; gg < 4; ++gg)
                dma<0>(lds0 + st + 4096 * lw + 1024 * gg, (gg & 1) ? vx1 : vx0, sx, (4 * lw + gg) * 8 * 1280 + 128 * i);
#pragma unroll
            for (int gg = 0; gg < 4; ++gg)
                dma<0>(lds0 + st + SLOT + 4096 * lw + 1024 * gg, vw, sw, (unsigned)(64 * i + 16 * lw + 8 * (gg >> 1)) * 8192u + 256 * hd + 128 * (gg & 1));
        };
        constexpr unsigned T0 = 6 * SLOT;
        unsigned st = 4 * SLOT;
#pragma unroll 1
        for (int u = 0; u < 12; ++u) {
            if (u >= 3) { WAITV(8); BAR(); }
            issueQ(u, st);
            st = (st == 8 * SLOT) ? 4 * SLOT : st + 2 * SLOT;
            if (u == 2) { WAITV(16); BAR(); }
        }
        STAMP(1);
        unsigned pv = 2 * SLOT;
#pragma unroll 1
        for (int c = 0; c < 10; ++c) {
            const unsigned tc = T0 + (c & 1) * 2 * SLOT, tn = T0 + ((c + 1) & 1) * 2 * SLOT;
            if (c >= 2 && c <= 8) { BAR(); issueQ(c + 11, tn); WAITV(16); } else WAITV(8);
            BAR();
            if (c == 1) issueQ(12, tn);
            const unsigned pk = nextpair(pv);
            const bool kv = c < 9;
            qblock(acc, smem + tc + xb0, smem + tc + xb1, smem + tc + wqb,
                   [&](int i) { if (kv) { if (i < 4) pieceV(c + 1, pv, i); else pieceK(c + 2, pk, i - 4); } });
            pv = pk;
        }
    }
    asm volatile("" : "+v"(gen));
    const u64 tag = gen + 1;

#pragma unroll
    for (int d = 0; d < 4; ++d) { qf[2 * d] = pack8(acc[d], 0); qf[2 * d + 1] = pack8(acc[d], 1); zero16(acc[d]); }
    float m = -__builtin_inff(), l = 0.f;
    {
        unsigned pk = grp ? 4 * SLOT : 0;
        aload(4, fr[1], smem, smem + pk + kb0, smem + pk + kb1); aload(5, fr[2], smem, smem + pk + kb0, smem + pk + kb1);
        const GAS u64* fp = flagp(partner(1 + (lane % 3)), 0, qb);
#pragma unroll 1
        for (int t = 0; t < 8; ++t) {
            const bool watch = grp && t == 3;
            u64 fv = tag;
            if (watch) fv = __hip_atomic_load(fp, __ATOMIC_RELAXED, __HIP_MEMORY_SCOPE_AGENT);
            qkpart(s0, s1, qf, fr, smem + pk + kb0, smem + pk + kb1);
            if (!__all(fv == tag)) wait_flags(fp, tag);
            STAMP(2 + 2 * t);
            BAR();
            softmax(acc, pf, s0, s1, m, l, fr, smem + pk + SLOT + trb);
            WAITV(0);
            STAMP(3 + 2 * t);
            BAR();
            const unsigned pvv = pk;
            pk = nextpair(pk);
            pvpart(acc, pf, fr, smem + pvv + SLOT + trb, smem + pk + kb0, smem + pk + kb1);
        }
    }
    STAMP(18);
    BAR();

    {
        const float inv = 1.f / swapsum(l);
#pragma unroll
        for (int d = 0; d < 4; ++d) {
#pragma unroll
            for (int r = 0; r < 16; ++r) acc[d][r] *= inv;
            qf[2 * d] = pack8(acc[d], 0); qf[2 * d + 1] = pack8(acc[d], 1);
        }
#pragma unroll
        for (int f = 0; f < 8; ++f)
            __builtin_amdgcn_raw_buffer_store_b128(__builtin_bit_cast(u32x4, qf[f]), sof, vo16, (2 * jj + grp) * 32768 + qb * 8192 + f * 1024, 16);
#pragma unroll
        for (int n = 0; n < 5; ++n) zero16(acc[n]);
    }
    const rsrc_t swo = mkrsrc(wo + (size_t)(8 * xq * 128) * 1280 + 320 * jj, 1024 * 1280 - 320 * jj);
    const unsigned vwA = dr * 1280 + dsub * 64 + 16 * dsl, vwB = dr * 1280 + 16 * dsl + (dsub ? 8 * 1280 : 256);
    auto issueW = [&](int hl, int half, int buf) {
        const unsigned so = (unsigned)(hl * 128 + 64 * half + 16 * lw) * 1280u, ld = lds0 + buf * WT + 5 * lw * 1024;
        dma<0>(ld, vwA, swo, so); dma<0>(ld + 1024, vwA, swo, so + 128); dma<0>(ld + 2048, vwB, swo, so);
        dma<0>(ld + 3072, vwA, swo, so + 8 * 1280 + 64); dma<0>(ld + 4096, vwA, swo, so + 8 * 1280 + 192);
    };
    bf16x8 cur[4], nxt[4];
#pragma unroll
    for (int k = 0; k < 4; ++k) { cur[k] = qf[k]; nxt[k] = qf[4 + k]; }
    unsigned pv = 0;
    if (grp == 0) {
        issueW(hl1(0), 0, 5); issueW(hl1(0), 1, 6);
#pragma unroll
        for (int i = 0; i < 4; ++i) pieceV(10, 2 * SLOT, i);
#pragma unroll
        for (int i = 0; i < 4; ++i) pieceK(11, 4 * SLOT, i);
        BAR();
        issueW(hl1(1), 0, 7);
        BAR();
#pragma unroll
        for (int i = 0; i < 4; ++i) pieceV(11, 4 * SLOT, i);
#pragma unroll
        for (int i = 0; i < 4; ++i) pieceK(12, 0, i);
    }
#pragma unroll 1
    for (int k = 0; k < (grp ? 2 : 8); ++k) {
        const unsigned pk = nextpair(pv);
        if (grp == 0) {
            if (k <= 5) {
                BAR();
                if (k >= 1) issueW(hl1((k + 2) >> 1), k & 1, 5 + (k + 2) % 3);
                WAITV(13);
                if (k == 0 && lane == 0) __hip_atomic_store(flagp(bid, 0, qb), tag, __ATOMIC_RELAXED, __HIP_MEMORY_SCOPE_AGENT);
            } else WAITV(5);
        }
        asm volatile("" : "+v"(nxt[0]), "+v"(nxt[1]), "+v"(nxt[2]), "+v"(nxt[3]) :: "memory");
        if (k >= 1) {
#pragma unroll
            for (int i = 0; i < 4; ++i) cur[i] = nxt[i];
        }
        if (grp == 0) {
            BAR();
            if (k < 7) {
                const unsigned so = hl1((k + 1) >> 1) * 32768 + qb * 8192 + 4096 * ((k + 1) & 1);
#pragma unroll
                for (int i = 0; i < 4; ++i) ld_sc1(nxt[i], vo16, sof, so + i * 1024);
            }
            if (k == 4) { issueW(hl1(0) + 1, 0, 2); issueW(hl1(0) + 1, 1, 3); }
            if (k == 5 || k == 6) issueW(hl1(1) + 1, k - 5, k == 5 ? 4 : 7);
        }
        const bool kv = grp == 0 && k <= 3;
        const int buf = grp ? 2 + k : 5 + k % 3;
        oblock(acc, cur, smem + buf * WT + trb, [&](int i) { if (kv) { if (i < 4) pieceV(12 + k, pv, i); else pieceK(13 + k, pk, i - 4); } });
        pv = pk;
    }
    STAMP(19);
    BAR();
    {
        const lptr xw = smem + (grp ? 32768 + lw * 12288 : lw * 8192) + vo16;
        const lptr xr = smem + (grp ? lw * 8192 : 32768 + lw * 12288) + vo16;
        f32x16 k0, k1, k2;
        if (grp == 0) {
            issueW(hl1(2) + 1, 0, 5); issueW(hl1(2) + 1, 1, 6);
#pragma unroll
            for (int n = 0; n < 2; ++n)
#pragma unroll
                for (int g = 0; g < 4; ++g) *(LAS f32x4*)(xw + (n * 4 + g) * 1024) = (f32x4){acc[3 + n][4 * g], acc[3 + n][4 * g + 1], acc[3 + n][4 * g + 2], acc[3 + n][4 * g + 3]};
            k0 = acc[0]; k1 = acc[1]; k2 = acc[2];
        } else {
#pragma unroll
            for (int n = 0; n < 3; ++n)
#pragma unroll
                for (int g = 0; g < 4; ++g) *(LAS f32x4*)(xw + (n * 4 + g) * 1024) = (f32x4){acc[n][4 * g], acc[n][4 * g + 1], acc[n][4 * g + 2], acc[n][4 * g + 3]};
            k0 = acc[3]; k1 = acc[4]; k2 = acc[4];
        }
        BAR();
#pragma unroll
        for (int g = 0; g < 4; ++g) {
            const f32x4 u0 = *(const LAS f32x4*)(xr + g * 1024), u1 = *(const LAS f32x4*)(xr + (4 + g) * 1024);
#pragma unroll
            for (int e = 0; e < 4; ++e) { k0[4 * g + e] += u0[e]; k1[4 * g + e] += u1[e]; }
        }
        if (grp == 0) {
#pragma unroll
            for (int g = 0; g < 4; ++g) {
                const f32x4 u2 = *(const LAS f32x4*)(xr + (8 + g) * 1024);
#pragma unroll
                for (int e = 0; e < 4; ++e) k2[4 * g + e] += u2[e];
            }
        }
        BAR();
        if (grp == 0) { issueW(hl1(3) + 1, 0, 0); issueW(hl1(3) + 1, 1, 1); }
        else {
            WAITV(0);
            if (lane == 0) __hip_atomic_store(flagp(bid, 1, qb), tag, __ATOMIC_RELAXED, __HIP_MEMORY_SCOPE_AGENT);
        }
        STAMP(20);
        wait_flags(flagp(partner(1 + (lane % 3)), 1, qb), tag);
        WAITV(0);
        STAMP(21);
        bf16x8 of2[24];
#pragma unroll
        for (int k = 0; k < 24; ++k)
            of2[k] = __builtin_bit_cast(bf16x8, __builtin_amdgcn_raw_buffer_load_b128(sof, vo16, (hl1(1 + (k >> 3)) + 1) * 32768 + qb * 8192 + (k & 7) * 1024, 16));
        BAR();
        const rsrc_t spb = mkrsrc(ws + WS_PB, 256 * PB_WG);
        const int nk = grp ? 2 : 3;
        f32x4 m0 = {0.f, 0.f, 0.f, 0.f}, m1 = m0, m2 = m0;
#pragma unroll 1
        for (int n = 0; n < nk; ++n) {
            const lptr wp = smem + trb + 512 * (grp ? 3 + n : n);
            bf16x8 wf[2][4];
            auto load = [&](int tl) {
                constexpr int bufs[6] = {4, 7, 5, 6, 0, 1};
#pragma unroll
                for (int ks = 0; ks < 4; ++ks) wf[tl & 1][ks] = cat(ldstr(wp + bufs[tl] * WT + 5120 * ks), ldstr(wp + bufs[tl] * WT + 5120 * ks + 2560));
            };
            load(0);
#pragma unroll
            for (int tl = 0; tl < 6; ++tl) {
                if (tl + 1 < 6) load(tl + 1);
                SB();
#pragma unroll
                for (int ks = 0; ks < 4; ++ks) k0 = MFMA32(wf[tl & 1][ks], of2[4 * tl + ks], k0);
                SB();
            }
            m2 = m1; m1 = m0;
#pragma unroll
            for (int g = 0; g < 4; ++g) {
                const f32x4 v = {k0[4 * g], k0[4 * g + 1], k0[4 * g + 2], k0[4 * g + 3]};
                if (g == xq) m0 = v;
                else __builtin_amdgcn_raw_buffer_store_b128(__builtin_bit_cast(u32x4, v), spb, vo16,
                         (unsigned)((bid & ~3) | g) * PB_WG + ((xq * 8 + wid) * 3 + n) * 1024, 16);
            }
            k0 = k1; k1 = k2;
        }
        STAMP(22);
        WAITV(0);
        if (lane == 0) __hip_atomic_store(flagp(bid, 2, wid), tag, __ATOMIC_RELAXED, __HIP_MEMORY_SCOPE_AGENT);
        STAMP(23);
        wait_flags(flagp((bid & ~3) | ((xq + 1 + (lane % 3)) & 3), 2, wid), tag);
        STAMP(24);
#pragma unroll
        for (int i = 0; i < 3; ++i) {
            if (i < nk) {
                const int n = nk - 1 - i;
                f32x4 u[3];
#pragma unroll
                for (int s = 0; s < 3; ++s)
                    u[s] = __builtin_bit_cast(f32x4, __builtin_amdgcn_raw_buffer_load_b128(spb, vo16,
                               (unsigned)bid * PB_WG + ((((xq + 1 + s) & 3) * 8 + wid) * 3 + n) * 1024, 16));
                const f32x4 mi = i == 0 ? m0 : (i == 1 ? m1 : m2);
                const f32x4 v = (mi + u[0]) + (u[1] + u[2]);
                const u32x2 w = {cvtpk(v[0], v[1]), cvtpk(v[2], v[3])};
                const int col = br * DM + 160 * jj + 32 * (grp ? 3 + n : n) + 8 * xq + 4 * h;
                *(u32x2*)(out + ((size_t)(b * SQ + rowq) * (2 * DM) + col) * 2) = w;
            }
        }
        if (xq == 0 && jj == 0 && wid == 0 && lane == 0)
            __hip_atomic_store((GAS u64*)(ws + WS_GEN + (br * NB + b) * 256), tag, __ATOMIC_RELAXED, __HIP_MEMORY_SCOPE_AGENT);
        STAMP(25);
    }
}

extern "C" void kernel_launch(void* const* d_in, const int* in_sizes, int n_in, void* d_out, int out_size,
                              void* d_ws, size_t ws_size, hipStream_t stream) {
    (void)hipFuncSetAttribute((const void*)fused_kernel, hipFuncAttributeMaxDynamicSharedMemorySize, LDS_BYTES);
    hipLaunchKernelGGL(fused_kernel, dim3(256), dim3(512), LDS_BYTES, stream,
                       (const char*)d_in[0], (const char*)d_in[1], (const char*)d_in[2], (const char*)d_in[3], (const char*)d_in[4],
                       (const char*)d_in[5], (const char*)d_in[6], (const char*)d_in[7], (const char*)d_in[8], (const char*)d_in[9],
                       (char*)d_ws, (char*)d_out);
}
```

```cpp
#include <hip/hip_runtime.h>
#include <hip/hip_bf16.h>
#include <stdint.h>
#include <type_traits>

typedef short bf16x8 __attribute__((ext_vector_type(8)));
typedef short s16x4 __attribute__((ext_vector_type(4)));
typedef float f32x16 __attribute__((ext_vector_type(16)));
typedef float f32x4 __attribute__((ext_vector_type(4)));
typedef float f32x2 __attribute__((ext_vector_type(2)));
typedef __bf16 bf16x2_t __attribute__((ext_vector_type(2)));
typedef unsigned u32x4 __attribute__((ext_vector_type(4)));
typedef unsigned u32x2 __attribute__((ext_vector_type(2)));
typedef unsigned long long u64;
#define LAS __attribute__((address_space(3)))
#define GAS __attribute__((address_space(1)))
#define DI __device__ __forceinline__
typedef LAS char* lptr;
typedef __amdgpu_buffer_rsrc_t rsrc_t;

constexpr int NB = 8, SQ = 128, DM = 640, NH = 32, SK = 512, DH = 128;
constexpr float CEXP = 0.08838834764831845f * 1.4426950408889634f;
constexpr int SLOT = 16384;
constexpr int LDS_T = 6 * SLOT;
constexpr int WT = 20480;
constexpr int LDS_BYTES = 10 * SLOT;
constexpr size_t WS_GEN = 0, WS_FLAG = 8192, WS_OF = 1 << 20;
constexpr size_t OF_BYTES = (size_t)2 * NB * NH * 32768;
constexpr size_t WS_PB = WS_OF + OF_BYTES;
constexpr unsigned PB_WG = 4 * 8 * 3 * 1024;
#define NT_W 0
#define NT_X 0
#define NT_WO 0
constexpr unsigned SPIN_CAP = 1u << 16;

#define MFMA32(a, b, c) __builtin_amdgcn_mfma_f32_32x32x16_bf16((a), (b), (c), 0, 0, 0)
#define WAITV(n) asm volatile("s_waitcnt vmcnt(" #n ")" ::: "memory")
#define SB() __builtin_amdgcn_sched_barrier(0)
#define BARX() do { SB(); asm volatile("s_barrier" ::: "memory"); SB(); } while (0)
#define BAR() do { SB(); asm volatile("s_waitcnt lgkmcnt(0)\n\ts_barrier" ::: "memory"); SB(); } while (0)

#define STAMP(idx)

DI unsigned cvtpk(float lo, float hi) {
    f32x2 v = {lo, hi};
    bf16x2_t b = __builtin_convertvector(v, bf16x2_t);
    return __builtin_bit_cast(unsigned, b);
}
DI bf16x8 pack8(const f32x16& x, int s) {
    u32x4 w = {cvtpk(x[8 * s + 0], x[8 * s + 1]), cvtpk(x[8 * s + 2], x[8 * s + 3]),
               cvtpk(x[8 * s + 4], x[8 * s + 5]), cvtpk(x[8 * s + 6], x[8 * s + 7])};
    return __builtin_bit_cast(bf16x8, w);
}
template <int NT> DI void dma(unsigned lds, unsigned voff, rsrc_t srd, unsigned soff) {
    if (NT) asm volatile("s_mov_b32 m0, %0\n\ts_nop 3\n\tbuffer_load_dwordx4 %1, %2, %3 offen nt lds"
                 :: "s"(lds), "v"(voff), "s"(srd), "s"(soff) : "memory");
    else asm volatile("s_mov_b32 m0, %0\n\ts_nop 3\n\tbuffer_load_dwordx4 %1, %2, %3 offen lds"
                 :: "s"(lds), "v"(voff), "s"(srd), "s"(soff) : "memory");
}
DI void ld_sc1(bf16x8& d, unsigned voff, rsrc_t srd, unsigned soff) {
    asm volatile("s_nop 4\n\tbuffer_load_dwordx4 %0, %1, %2, %3 offen sc1" : "+v"(d) : "v"(voff), "s"(srd), "s"(soff) : "memory");
}
#define WAITV_F(n, f) asm volatile("s_waitcnt vmcnt(" #n ")" : "+v"((f)[0]), "+v"((f)[1]), "+v"((f)[2]), "+v"((f)[3]) :: "memory")
DI bf16x8 lds128(lptr p) { return *(const LAS bf16x8*)p; }
DI s16x4 ldstr(lptr p) {
    typedef short v4i16_t __attribute__((ext_vector_type(4)));
    return __builtin_amdgcn_ds_read_tr16_b64_v4i16((LAS v4i16_t*)p);
}
DI bf16x8 cat(s16x4 a, s16x4 b) { return __builtin_shufflevector(a, b, 0, 1, 2, 3, 4, 5, 6, 7); }
DI float swapmax(float v) {
    auto r = __builtin_amdgcn_permlane32_swap(__float_as_uint(v), __float_as_uint(v), false, false);
    return fmaxf(__uint_as_float(r[0]), __uint_as_float(r[1]));
}
DI float swapsum(float v) {
    auto r = __builtin_amdgcn_permlane32_swap(__float_as_uint(v), __float_as_uint(v), false, false);
    return __uint_as_float(r[0]) + __uint_as_float(r[1]);
}
DI rsrc_t mkrsrc(const void* p, unsigned bytes) {
    return __builtin_amdgcn_make_buffer_rsrc((void*)p, (short)0, (int)bytes, 0x00020000);
}
DI void zero16(f32x16& v) {
#pragma unroll
    for (int r = 0; r < 16; ++r) v[r] = 0.f;
}
DI void wait_flags(const GAS u64* p, u64 tag) {
#pragma unroll 1
    for (unsigned it = 0; it < SPIN_CAP; ++it) {
        const u64 v = __hip_atomic_load(p, __ATOMIC_RELAXED, __HIP_MEMORY_SCOPE_AGENT);
        if (__all(v == tag)) break;
        __builtin_amdgcn_s_sleep(1);
    }
    asm volatile("" ::: "memory");
}

DI float max3f(float a, float b, float c) { float r; asm("v_max3_f32 %0, %1, %2, %3" : "=v"(r) : "v"(a), "v"(b), "v"(c)); return r; }

DI lptr opaque(lptr p) { unsigned a = (unsigned)(uintptr_t)p; asm volatile("" : "+v"(a)); return (lptr)(uintptr_t)a; }
struct NoHook { DI void operator()(int) const {} };
constexpr int LA = 6;

struct QBase { lptr x0, x1, w; };
DI void qload(int i, bf16x8* wf, bf16x8* xf, const QBase& q) {
    const int s = i >> 2, d = i & 3;
    if (d == 0) xf[s] = lds128(((s & 1) ? q.x1 : q.x0) + 512 * (s >> 1));
    wf[i & 7] = cat(ldstr(q.w + 4096 * s + 512 * d), ldstr(q.w + 4096 * s + 512 * d + 256));
}
DI void qpre(bf16x8* wf, bf16x8* xf, const QBase& q) {
#pragma unroll
    for (int i = 0; i < LA; ++i) qload(i, wf, xf, q);
}
template <bool NEXT, class H> DI void qsteps(f32x16* acc, bf16x8* wf, bf16x8* xf, const QBase& q, const QBase& qn, H hook) {
#pragma unroll
    for (int i = 0; i < 16; ++i) {
        SB();
        acc[i & 3] = MFMA32(wf[i & 7], xf[i >> 2], acc[i & 3]);
        SB();
        if (i + LA < 16) qload(i + LA, wf, xf, q); else if (NEXT) qload(i + LA - 16, wf, xf, qn);
        if (i & 1) hook(i >> 1);
    }
    SB();
}
template <class H> DI void oblock(f32x16* acc, const bf16x8* of, lptr wp_, H hook) {
    const lptr wp = opaque(wp_);
    bf16x8 wf[8];
    auto load = [&](int i) { wf[i & 7] = cat(ldstr(wp + 5120 * (i / 5) + 512 * (i % 5)), ldstr(wp + 5120 * (i / 5) + 2560 + 512 * (i % 5))); };
#pragma unroll
    for (int i = 0; i < LA; ++i) load(i);
#pragma unroll
    for (int i = 0; i < 20; ++i) {
        SB();
        acc[i % 5] = MFMA32(wf[i & 7], of[i / 5], acc[i % 5]);
        SB();
        if (i + LA < 20) load(i + LA);
        if ((i & 1) && i < 16) hook(i >> 1);
    }
    SB();
}
DI void aload(int i, bf16x8* fr, lptr vp, lptr kp0, lptr kp1) {
    if (i < 16) fr[i & 7] = cat(ldstr(vp + 4096 * (i >> 2) + 512 * (i & 3)), ldstr(vp + 4096 * (i >> 2) + 2048 + 512 * (i & 3)));
    else { const int s = (i - 16) >> 1; fr[i & 7] = lds128(((s & 1) ? kp1 : kp0) + 512 * (s >> 1) + 8192 * (i & 1)); }
}
DI void qkpart(f32x16& s0, f32x16& s1, const f32x16& negm, const bf16x8* qf, bf16x8* fr, lptr kp0, lptr kp1) {
    s0 = negm; s1 = negm;
#pragma unroll
    for (int i = 16; i < 32; ++i) {
        SB();
        if (i & 1) s1 = MFMA32(fr[i & 7], qf[(i - 16) >> 1], s1); else s0 = MFMA32(fr[i & 7], qf[(i - 16) >> 1], s0);
        SB();
        if (i + LA < 32) aload(i + LA, fr, kp0, kp0, kp1);
    }
    SB();
}
DI void pvpart(f32x16* o, const bf16x8* pf, bf16x8* fr, lptr vp, lptr kp0, lptr kp1) {
#pragma unroll
    for (int i = 0; i < 16; ++i) {
        SB();
        o[i & 3] = MFMA32(fr[i & 7], pf[i >> 2], o[i & 3]);
        SB();
        aload(i + LA, fr, vp, kp0, kp1);
    }
    SB();
}
DI float fadd_s(float a, float b) { float r; asm("v_add_f32_e32 %0, %1, %2" : "=v"(r) : "v"(a), "v"(b)); return r; }
DI float fmul_s(float a, float b) { float r; asm("v_mul_f32_e32 %0, %1, %2" : "=v"(r) : "v"(a), "v"(b)); return r; }
#define MPAD4(a) asm volatile("s_nop 7\n\ts_nop 7" : "+v"((a)[0]), "+v"((a)[1]), "+v"((a)[2]), "+v"((a)[3]))
#define PIN(x) asm volatile("" : "+v"(x))
DI float fsub_s(float a, float b) { float r; asm("v_sub_f32_e32 %0, %1, %2" : "=v"(r) : "v"(a), "v"(b)); return r; }
#define SM_THR 8.0f
DI void softmax(f32x16* o, bf16x8* pf, f32x16& p0, f32x16& p1, f32x16& negm, float& l, bool first, bf16x8* fr, lptr vp) {
#pragma unroll
    for (int i = 0; i < LA; ++i) aload(i, fr, vp, vp, vp);
    PIN(p0); PIN(p1);
    SB();
    float c0 = max3f(p0[0], p0[1], p0[2]), c1 = max3f(p0[8], p0[9], p0[10]), c2 = max3f(p1[0], p1[1], p1[2]), c3 = max3f(p1[8], p1[9], p1[10]);
    c0 = max3f(c0, p0[3], p0[4]); c1 = max3f(c1, p0[11], p0[12]); c2 = max3f(c2, p1[3], p1[4]); c3 = max3f(c3, p1[11], p1[12]);
    c0 = max3f(c0, p0[5], p0[6]); c1 = max3f(c1, p0[13], p0[14]); c2 = max3f(c2, p1[5], p1[6]); c3 = max3f(c3, p1[13], p1[14]);
    c0 = max3f(c0, p0[7], p0[15]); c2 = max3f(c2, p1[7], p1[15]);
    c0 = max3f(c0, c1, c2); c0 = max3f(c0, c3, c3);
    auto r_ = __builtin_amdgcn_permlane32_swap(__float_as_uint(c0), __float_as_uint(c0), false, false);
    const float mx = max3f(__uint_as_float(r_[0]), __uint_as_float(r_[1]), __uint_as_float(r_[1]));
    if (first || __any(!(mx <= SM_THR))) {
        const float d = first ? mx : fmaxf(mx, 0.f);
#pragma unroll
        for (int r = 0; r < 16; ++r) { p0[r] = fsub_s(p0[r], d); p1[r] = fsub_s(p1[r], d); negm[r] = fsub_s(negm[r], d); }
        if (!first) {
            const float f = __builtin_amdgcn_exp2f(-d);
            l *= f;
#pragma unroll
            for (int dd = 0; dd < 4; ++dd)
#pragma unroll
                for (int r = 0; r < 16; ++r) o[dd][r] = fmul_s(o[dd][r], f);
        }
    }
    SB();
#pragma unroll
    for (int r = 0; r < 16; ++r) { p0[r] = __builtin_amdgcn_exp2f(p0[r]); p1[r] = __builtin_amdgcn_exp2f(p1[r]); }
    PIN(p0); PIN(p1);
    SB();
    float a0 = fadd_s(p0[0], l), a1 = fadd_s(p0[1], p0[2]), a2 = fadd_s(p1[0], p1[1]), a3 = fadd_s(p1[2], p1[3]);
    a0 = fadd_s(a0, p0[3]);
#pragma unroll
    for (int r = 4; r < 16; r += 2) {
        a0 = fadd_s(a0, p0[r]); a1 = fadd_s(a1, p0[r + 1]); a2 = fadd_s(a2, p1[r]); a3 = fadd_s(a3, p1[r + 1]);
    }
    pf[0] = pack8(p0, 0); pf[1] = pack8(p0, 1); pf[2] = pack8(p1, 0); pf[3] = pack8(p1, 1);
    l = fadd_s(fadd_s(a0, a1), fadd_s(a2, a3));
    PIN(pf[0]); PIN(pf[1]); PIN(pf[2]); PIN(pf[3]); PIN(l);
    SB();
}

__global__ __launch_bounds__(512, 2) void fused_kernel(
    const char* xA, const char* kA, const char* vA, const char* wqA, const char* woA,
    const char* xB, const char* kB, const char* vB, const char* wqB, const char* woB,
    char* ws, char* out)
{
    extern __shared__ __attribute__((aligned(16))) char smem_[];
    const lptr smem = (lptr)smem_;
    const unsigned lds0 = (unsigned)(uintptr_t)smem;
    const int tid = threadIdx.x, lane = tid & 63;
    const int wid = __builtin_amdgcn_readfirstlane(tid >> 6);
    const int bid = blockIdx.x;
    const int xg = bid & 7, ii = bid >> 3;
    const int br = xg >> 2, xq = xg & 3, b = ii >> 2, jj = ii & 3;
    const int h1 = 8 * xq + 2 * jj;
    const char* x = br ? xB : xA;
    const char* kc = br ? kB : kA;
    const char* vc = br ? vB : vA;
    const char* wq = br ? wqB : wqA;
    const char* wo = br ? woB : woA;
    const int qb = wid & 3, grp = wid >> 2, lw = wid & 3;
    const int l31 = lane & 31, h = lane >> 5;
    STAMP(0);

    u64 gen;
    {
        const char* gp = ws + WS_GEN + (br * NB + b) * 256;
        asm volatile("global_load_dwordx2 %0, %1, off sc1" : "=v"(gen) : "v"(gp) : "memory");
    }

    const rsrc_t sk = mkrsrc(kc + (size_t)(b * NH + h1) * SK * DH * 2, 2 * SK * DH * 2);
    const rsrc_t sv = mkrsrc(vc + (size_t)(b * NH + h1) * SK * DH * 2, 2 * SK * DH * 2);
    const rsrc_t sof = mkrsrc(ws + WS_OF + (size_t)((br * NB + b) * NH + 8 * xq) * 32768, 8 * 32768);

    const unsigned dsub = lane >> 5, dr = (lane >> 2) & 7, dsl = lane & 3;
    const unsigned xr0 = 16u * (dsl ^ ((dr >> 2) & 3)), xr1 = 16u * (dsl ^ ((2 + (dr >> 2)) & 3));
    const unsigned vk0 = dr * 256 + dsub * 64 + xr0, vk1 = dr * 256 + dsub * 64 + xr1, vv = dr * 256 + dsub * 64 + 16 * dsl;

    auto pieceK = [&](int t, unsigned pair, int i) {
        dma<1>(lds0 + pair + 4096 * lw + 1024 * i, (i >> 1) ? vk1 : vk0, sk, (unsigned)t * 16384u + 4096 * lw + 2048 * (i >> 1) + 128 * (i & 1));
    };
    auto pieceV = [&](int t, unsigned pair, int i) {
        dma<1>(lds0 + pair + SLOT + 4096 * lw + 1024 * i, vv, sv, (unsigned)t * 16384u + 4096 * lw + 2048 * (i >> 1) + 128 * (i & 1));
    };
    auto nextpair = [](unsigned p) { return (p == 4u * SLOT) ? 0u : p + 2u * SLOT; };

    const int G1 = (lane >> 4) & 1, tq = (lane & 15) >> 2, tp = lane & 3;
    const int sig = ((tp & 1) << 1) | (tp >> 1);
    const unsigned wqb = SLOT + 2048 * h + 64 * tq + 32 * G1 + 8 * sig;
    const unsigned rowq = 32 * qb + l31;
    const unsigned xsw = (rowq >> 2) & 3;
    const unsigned xb0 = 1024 * (rowq >> 3) + 64 * (rowq & 7) + 16 * ((0 + h) ^ xsw);
    const unsigned xb1 = 1024 * (rowq >> 3) + 64 * (rowq & 7) + 16 * ((2 + h) ^ xsw);
    const unsigned ksw = (l31 >> 2) & 3;
    const unsigned kb0 = 2048 * (l31 >> 3) + 64 * (l31 & 7) + 16 * ((0 + h) ^ ksw);
    const unsigned kb1 = 2048 * (l31 >> 3) + 64 * (l31 & 7) + 16 * ((2 + h) ^ ksw);
    const unsigned trb = 64 * (4 * h + tq) + 32 * G1 + 8 * tp;
    const unsigned vo16 = lane * 16;

    auto flagp = [&](int bb, int stage, int w) { return (GAS u64*)(ws + WS_FLAG + (size_t)((bb * 3 + stage) * 8 + w) * 64); };
    auto partner = [&](int dj) { return (bid & ~24) | (((jj + dj) & 3) << 3); };
    auto hl1 = [&](int dj) { return 2 * ((jj + dj) & 3); };

    f32x16 acc[5];
    bf16x8 qf[8], pf[4], fr[8];
    f32x16 s0, s1;
#pragma unroll
    for (int d = 0; d < 4; ++d) zero16(acc[d]);

    auto nextstage = [](unsigned st) { return st == 8u * SLOT ? 4u * SLOT : (st == 4u * SLOT ? 6u * SLOT : (st == 6u * SLOT ? 2u * SLOT : 8u * SLOT)); };
    auto qbase = [&](unsigned st) { return QBase{opaque(smem + st + xb0), opaque(smem + st + xb1), opaque(smem + st + wqb)}; };
    if (grp == 0) {
        unsigned st = 8 * SLOT;
        BARX();
        QBase qc = qbase(st);
        qpre(fr, pf, qc);
#pragma unroll 1
        for (int i = 0; i < 10; ++i) {
            st = nextstage(st);
            const QBase qn = qbase(st);
            qsteps<true>(acc, fr, pf, qc, qn, NoHook());
            qc = qn;
            if (i == 9) WAITV(0);
            BARX();
        }
    } else {
        const rsrc_t sx = mkrsrc(x + (size_t)b * SQ * DM * 2, SQ * DM * 2);
        const rsrc_t sw = mkrsrc(wq + (size_t)h1 * 256, DM * 8192 - h1 * 256);
        const unsigned vx0 = dr * 1280 + dsub * 64 + xr0, vx1 = dr * 1280 + dsub * 64 + xr1, vw = dr * 8192 + dsub * 64 + 16 * dsl;
        const int rot = b + 3 * jj;
        auto issueQ = [&](int i0, int hd, unsigned st) {
            int i = i0 + rot; i -= (i >= 20) ? 20 : ((i >= 10) ? 10 : 0);
#pragma unroll
            for (int gg = 0; gg < 4; ++gg)
                dma<NT_W>(lds0 + st + SLOT + 4096 * lw + 1024 * gg, vw, sw, (unsigned)(64 * i + 16 * lw + 8 * (gg >> 1)) * 8192u + 256 * hd + 128 * (gg & 1));
#pragma unroll
            for (int gg = 0; gg < 4; ++gg)
                dma<NT_X>(lds0 + st + 4096 * lw + 1024 * gg, (gg & 1) ? vx1 : vx0, sx, (4 * lw + gg) * 8 * 1280 + 128 * i);
        };
        constexpr unsigned T0 = 6 * SLOT;
        unsigned st = 8 * SLOT;
#pragma unroll 1
        for (int u = 0; u < 13; ++u) {
            if (u >= 3) {
                if (u == 7 || u == 8 || u == 12) WAITV(16); else WAITV(8);
                BARX();
            }
            if (u != 11) issueQ(u < 10 ? u : (u - 10) >> 1, u >= 10, st);
            if (u == 6 || u == 11) {
                const int t = u == 11;
#pragma unroll
                for (int i = 0; i < 4; ++i) pieceK(t, t * 2 * SLOT, i);
#pragma unroll
                for (int i = 0; i < 4; ++i) pieceV(t, t * 2 * SLOT, i);
            }
            st = nextstage(st);
        }
        unsigned pv = 4 * SLOT;
#pragma unroll 1
        for (int c = 0; c < 10; ++c) {
            const unsigned tc = T0 + (c & 1) * 2 * SLOT, tn = T0 + ((c + 1) & 1) * 2 * SLOT;
            const unsigned pk = nextpair(pv);
            if (c >= 2 && c <= 8) {
                BARX();
                issueQ(c + 1, 1, tn);
#pragma unroll
                for (int i = 0; i < 4; ++i) pieceV(c, pv, i);
#pragma unroll
                for (int i = 0; i < 4; ++i) pieceK(c + 1, pk, i);
                pv = pk;
                WAITV(16);
            } else if (c == 0) WAITV(16); else if (c == 1) WAITV(4); else WAITV(8);
            BARX();
            if (c == 1) issueQ(2, 1, tn);
            const QBase qc = qbase(tc);
            qpre(fr, pf, qc);
            qsteps<false>(acc, fr, pf, qc, qc, NoHook());
            if (c == 0) {
#pragma unroll
                for (int i = 0; i < 4; ++i) pieceK(2, 4 * SLOT, i);
            }
        }
    }
    STAMP(1);
    asm volatile("" : "+v"(gen));
    const u64 tag = gen + 1;

    MPAD4(acc);
    float cexp = CEXP; PIN(cexp);
#pragma unroll
    for (int d = 0; d < 4; ++d) {
#pragma unroll
        for (int r = 0; r < 16; ++r) acc[d][r] = fmul_s(acc[d][r], cexp);
        qf[2 * d] = pack8(acc[d], 0); qf[2 * d + 1] = pack8(acc[d], 1); zero16(acc[d]);
    }
    float l = 0.f;
    f32x16 negm; zero16(negm);
    __builtin_amdgcn_s_setprio(2);
    {
        unsigned pk = grp ? 4 * SLOT : 0;
        #pragma unroll
        for (int i = 16; i < 16 + LA; ++i) aload(i, fr, smem, smem + pk + kb0, smem + pk + kb1);
        const GAS u64* fp = flagp(partner(1 + (lane % 3)), 0, qb);
        u64 fv = 0;
#pragma unroll 1
        for (int t = 0; t < 8; ++t) {
            qkpart(s0, s1, negm, qf, fr, smem + pk + kb0, smem + pk + kb1);
            if (t == 0) WAITV(0);
            STAMP(2 + 2 * t);
            BAR();
            const bool watch = grp && t == 2;
            if (watch) asm volatile("global_load_dwordx2 %0, %1, off sc1" : "+v"(fv) : "v"(fp) : "memory");
            softmax(acc, pf, s0, s1, negm, l, t == 0, fr, smem + pk + SLOT + trb);
            if (watch) {
                WAITV(0);
                PIN(fv);
                if (!__all(fv == tag)) wait_flags(fp, tag);
            }
            STAMP(3 + 2 * t);
            BAR();
            const unsigned pvv = pk;
            pk = nextpair(pk);
            pvpart(acc, pf, fr, smem + pvv + SLOT + trb, smem + pk + kb0, smem + pk + kb1);
        }
    }
    __builtin_amdgcn_s_setprio(0);
    if (grp == 0) {
#pragma unroll
        for (int i = 0; i < 4; ++i) pieceV(9, 0, i);
#pragma unroll
        for (int i = 0; i < 4; ++i) pieceK(10, 2 * SLOT, i);
    }
    STAMP(18);
    BAR();

    {
        const float inv = 1.f / swapsum(l);
        MPAD4(acc);
#pragma unroll
        for (int d = 0; d < 4; ++d) {
#pragma unroll
            for (int r = 0; r < 16; ++r) acc[d][r] = fmul_s(acc[d][r], inv);
            qf[2 * d] = pack8(acc[d], 0); qf[2 * d + 1] = pack8(acc[d], 1);
        }
#pragma unroll
        for (int f = 0; f < 8; ++f)
            __builtin_amdgcn_raw_buffer_store_b128(__builtin_bit_cast(u32x4, qf[f]), sof, vo16, (2 * jj + grp) * 32768 + qb * 8192 + f * 1024, 16);
#pragma unroll
        for (int n = 0; n < 5; ++n) zero16(acc[n]);
    }
    const rsrc_t swo = mkrsrc(wo + (size_t)(8 * xq * 128) * 1280 + 320 * jj, 1024 * 1280 - 320 * jj);
    const unsigned vwA = dr * 1280 + dsub * 64 + 16 * dsl, vwB = dr * 1280 + 16 * dsl + (dsub ? 8 * 1280 : 256);
    auto issueW = [&](int hl, int half, int buf) {
        const unsigned so = (unsigned)(hl * 128 + 64 * half + 16 * lw) * 1280u, ld = lds0 + buf * WT + 5 * lw * 1024;
        dma<NT_WO>(ld, vwA, swo, so); dma<NT_WO>(ld + 1024, vwA, swo, so + 128); dma<NT_WO>(ld + 2048, vwB, swo, so);
        dma<NT_WO>(ld + 3072, vwA, swo, so + 8 * 1280 + 64); dma<NT_WO>(ld + 4096, vwA, swo, so + 8 * 1280 + 192);
    };
    bf16x8 cur[4], nxt[4];
#pragma unroll
    for (int k = 0; k < 4; ++k) { cur[k] = qf[k]; nxt[k] = qf[4 + k]; }
    auto issueO = [&](bf16x8* f, int blk) {
        const unsigned so = hl1(blk >> 1) * 32768 + qb * 8192 + 4096 * (blk & 1);
#pragma unroll
        for (int i = 0; i < 4; ++i) ld_sc1(f[i], vo16, sof, so + i * 1024);
    };
    if (grp == 0) {
        issueW(hl1(0), 0, 5);
        BAR();
        issueW(hl1(0), 1, 6);
#pragma unroll
        for (int i = 0; i < 4; ++i) pieceV(10, 2 * SLOT, i);
#pragma unroll
        for (int i = 0; i < 4; ++i) pieceK(11, 4 * SLOT, i);
        WAITV(26);
        BAR();
        issueW(hl1(1), 0, 7);
        WAITV(23);
        if (lane == 0) __hip_atomic_store(flagp(bid, 0, qb), tag, __ATOMIC_RELAXED, __HIP_MEMORY_SCOPE_AGENT);
        unsigned pv = 4 * SLOT;
#pragma unroll 1
        for (int k = 0; k < 6; ++k) {
            BAR();
            if (k >= 1) {
                WAITV(13);
                asm volatile("" : "+v"(nxt[0]), "+v"(nxt[1]), "+v"(nxt[2]), "+v"(nxt[3]) :: "memory");
#pragma unroll
                for (int i = 0; i < 4; ++i) cur[i] = nxt[i];
                asm volatile("" : "+v"(cur[0]), "+v"(cur[1]), "+v"(cur[2]), "+v"(cur[3]) :: "memory");
                issueO(nxt, k + 1);
                issueW(hl1((k + 2) >> 1), k & 1, 5 + (k + 2) % 3);
            }
            const unsigned pk = nextpair(pv);
            if (k <= 4) {
#pragma unroll
                for (int i = 0; i < 4; ++i) pieceV(11 + k, pv, i);
#pragma unroll
                for (int i = 0; i < 4; ++i) pieceK(12 + k, pk, i);
            } else { issueW(hl1(0) + 1, 0, 2); issueW(hl1(0) + 1, 1, 3); }
            pv = pk;
            if (k == 0) WAITV(13); else if (k == 5) WAITV(19); else WAITV(17);
            BAR();
            oblock(acc, cur, smem + (5 + k % 3) * WT + trb, NoHook());
        }
        issueO(cur, 7);
        issueW(hl1(1) + 1, 0, 4);
        WAITV(9);
        BAR();
    }
    asm volatile("" : "+v"(nxt[0]), "+v"(nxt[1]), "+v"(nxt[2]), "+v"(nxt[3]) :: "memory");
    oblock(acc, nxt, smem + (grp ? 3 : 5) * WT + trb, NoHook());
    if (grp == 0) {
        issueW(hl1(1) + 1, 1, 7);
        WAITV(10);
        BAR();
    }
    asm volatile("" : "+v"(cur[0]), "+v"(cur[1]), "+v"(cur[2]), "+v"(cur[3]) :: "memory");
    oblock(acc, cur, smem + (grp ? 2 : 6) * WT + trb, NoHook());
    if (grp) {
        WAITV(0);
        if (lane == 0) __hip_atomic_store(flagp(bid, 1, qb), tag, __ATOMIC_RELAXED, __HIP_MEMORY_SCOPE_AGENT);
    }
    STAMP(19);
    BAR();
    {
        const lptr xw = smem + (grp ? 32768 + lw * 12288 : lw * 8192) + vo16;
        const lptr xr = smem + (grp ? lw * 8192 : 32768 + lw * 12288) + vo16;
        f32x16 k0, k1, k2;
        if (grp == 0) {
#pragma unroll
            for (int n = 0; n < 2; ++n)
#pragma unroll
                for (int g = 0; g < 4; ++g) *(LAS f32x4*)(xw + (n * 4 + g) * 1024) = (f32x4){acc[3 + n][4 * g], acc[3 + n][4 * g + 1], acc[3 + n][4 * g + 2], acc[3 + n][4 * g + 3]};
            issueW(hl1(2) + 1, 0, 5); issueW(hl1(2) + 1, 1, 6);
            k0 = acc[0]; k1 = acc[1]; k2 = acc[2];
        } else {
#pragma unroll
            for (int n = 0; n < 3; ++n)
#pragma unroll
                for (int g = 0; g < 4; ++g) *(LAS f32x4*)(xw + (n * 4 + g) * 1024) = (f32x4){acc[n][4 * g], acc[n][4 * g + 1], acc[n][4 * g + 2], acc[n][4 * g + 3]};
            k0 = acc[3]; k1 = acc[4]; k2 = acc[4];
        }
        BAR();
#pragma unroll
        for (int g = 0; g < 4; ++g) {
            const f32x4 u0 = *(const LAS f32x4*)(xr + g * 1024), u1 = *(const LAS f32x4*)(xr + (4 + g) * 1024);
#pragma unroll
            for (int e = 0; e < 4; ++e) { k0[4 * g + e] += u0[e]; k1[4 * g + e] += u1[e]; }
        }
        if (grp == 0) {
#pragma unroll
            for (int g = 0; g < 4; ++g) {
                const f32x4 u2 = *(const LAS f32x4*)(xr + (8 + g) * 1024);
#pragma unroll
                for (int e = 0; e < 4; ++e) k2[4 * g + e] += u2[e];
            }
        }
        BAR();
        if (grp) { issueW(hl1(3) + 1, 0, 0); issueW(hl1(3) + 1, 1, 1); }
        STAMP(20);
        wait_flags(flagp(partner(1 + (lane % 3)), 1, qb), tag);
        WAITV(0);
        STAMP(21);
        bf16x8 of2[24];
#pragma unroll
        for (int k = 0; k < 24; ++k)
            of2[k] = __builtin_bit_cast(bf16x8, __builtin_amdgcn_raw_buffer_load_b128(sof, vo16, (hl1(1 + (k >> 3)) + 1) * 32768 + qb * 8192 + (k & 7) * 1024, 16));
        BAR();
        {
            const lptr wp = smem + trb + (grp ? 3 * 512 : 0);
            bf16x8 wf[8];
            auto woff = [](int k) { constexpr int bufs[6] = {4, 7, 5, 6, 0, 1}; return bufs[k >> 2] * WT + 5120 * (k & 3); };
            auto run = [&](auto NKC) {
                constexpr int NK = decltype(NKC)::value;
                auto load = [&](int i) { wf[i & 7] = cat(ldstr(wp + woff(i / NK) + 512 * (i % NK)), ldstr(wp + woff(i / NK) + 2560 + 512 * (i % NK))); };
#pragma unroll
                for (int i = 0; i < LA; ++i) load(i);
#pragma unroll
                for (int i = 0; i < 24 * NK; ++i) {
                    SB();
                    if (i % NK == 0) k0 = MFMA32(wf[i & 7], of2[i / NK], k0);
                    else if (i % NK == 1) k1 = MFMA32(wf[i & 7], of2[i / NK], k1);
                    else k2 = MFMA32(wf[i & 7], of2[i / NK], k2);
                    SB();
                    if (i + LA < 24 * NK) load(i + LA);
                }
                SB();
            };
            if (grp) run(std::integral_constant<int, 2>()); else run(std::integral_constant<int, 3>());
        }
        STAMP(22);
        const rsrc_t spb = mkrsrc(ws + WS_PB, 256 * PB_WG);
        f32x4 m0, m1, m2;
#pragma unroll
        for (int g = 0; g < 4; ++g) {
            const f32x4 v0 = {k0[4 * g], k0[4 * g + 1], k0[4 * g + 2], k0[4 * g + 3]};
            const f32x4 v1 = {k1[4 * g], k1[4 * g + 1], k1[4 * g + 2], k1[4 * g + 3]};
            const f32x4 v2 = {k2[4 * g], k2[4 * g + 1], k2[4 * g + 2], k2[4 * g + 3]};
            if (g == xq) { m0 = v0; m1 = v1; m2 = v2; }
            else {
                const unsigned so = (unsigned)((bid & ~3) | g) * PB_WG + (xq * 8 + wid) * 3072;
                __builtin_amdgcn_raw_buffer_store_b128(__builtin_bit_cast(u32x4, v0), spb, vo16, so, 16);
                __builtin_amdgcn_raw_buffer_store_b128(__builtin_bit_cast(u32x4, v1), spb, vo16, so + 1024, 16);
                if (grp == 0) __builtin_amdgcn_raw_buffer_store_b128(__builtin_bit_cast(u32x4, v2), spb, vo16, so + 2048, 16);
            }
        }
        WAITV(0);
        if (lane == 0) __hip_atomic_store(flagp(bid, 2, wid), tag, __ATOMIC_RELAXED, __HIP_MEMORY_SCOPE_AGENT);
        STAMP(23);
        wait_flags(flagp((bid & ~3) | ((xq + 1 + (lane % 3)) & 3), 2, wid), tag);
        STAMP(24);
        {
            f32x4 u[3][3];
#pragma unroll
            for (int s = 0; s < 3; ++s)
#pragma unroll
                for (int n = 0; n < 3; ++n)
                    u[s][n] = __builtin_bit_cast(f32x4, __builtin_amdgcn_raw_buffer_load_b128(spb, vo16,
                               (n == 2 && grp) ? 0x7ff00000u : (unsigned)bid * PB_WG + (((xq + 1 + s) & 3) * 8 + wid) * 3072 + n * 1024, 16));
            char* op = out + ((size_t)(b * SQ + rowq) * (2 * DM) + br * DM + 160 * jj + (grp ? 96 : 0) + 8 * xq + 4 * h) * 2;
#pragma unroll
            for (int n = 0; n < 3; ++n) {
                const f32x4 mi = n == 0 ? m0 : (n == 1 ? m1 : m2);
                const f32x4 v = (mi + u[0][n]) + (u[1][n] + u[2][n]);
                const u32x2 w = {cvtpk(v[0], v[1]), cvtpk(v[2], v[3])};
                if (n < 2 || grp == 0) *(u32x2*)(op + 64 * n) = w;
            }
        }
        if (xq == 0 && jj == 0 && wid == 0 && lane == 0)
            __hip_atomic_store((GAS u64*)(ws + WS_GEN + (br * NB + b) * 256), tag, __ATOMIC_RELAXED, __HIP_MEMORY_SCOPE_AGENT);
        STAMP(25);
    }
}

extern "C" void kernel_launch(void* const* d_in, const int* in_sizes, int n_in, void* d_out, int out_size,
                              void* d_ws, size_t ws_size, hipStream_t stream) {
    (void)hipFuncSetAttribute((const void*)fused_kernel, hipFuncAttributeMaxDynamicSharedMemorySize, LDS_BYTES);
    hipLaunchKernelGGL(fused_kernel, dim3(256), dim3(512), LDS_BYTES, stream,
                       (const char*)d_in[0], (const char*)d_in[1], (const char*)d_in[2], (const char*)d_in[3], (const char*)d_in[4],
                       (const char*)d_in[5], (const char*)d_in[6], (const char*)d_in[7], (const char*)d_in[8], (const char*)d_in[9],
                       (char*)d_ws, (char*)d_out);
}
```

```cpp
#include <hip/hip_runtime.h>
#include <hip/hip_bf16.h>
#include <stdint.h>
#include <type_traits>

typedef short bf16x8 __attribute__((ext_vector_type(8)));
typedef short s16x4 __attribute__((ext_vector_type(4)));
typedef float f32x16 __attribute__((ext_vector_type(16)));
typedef float f32x4 __attribute__((ext_vector_type(4)));
typedef float f32x2 __attribute__((ext_vector_type(2)));
typedef __bf16 bf16x2_t __attribute__((ext_vector_type(2)));
typedef unsigned u32x4 __attribute__((ext_vector_type(4)));
typedef unsigned u32x2 __attribute__((ext_vector_type(2)));
typedef unsigned long long u64;
#define LAS __attribute__((address_space(3)))
#define GAS __attribute__((address_space(1)))
#define DI __device__ __forceinline__
typedef LAS char* lptr;
typedef __amdgpu_buffer_rsrc_t rsrc_t;

constexpr int NB = 8, SQ = 128, DM = 640, NH = 32, SK = 512, DH = 128;
constexpr float CEXP = 0.08838834764831845f * 1.4426950408889634f;
constexpr int SLOT = 16384;
constexpr int LDS_T = 6 * SLOT;
constexpr int WT = 20480;
constexpr int LDS_BYTES = 10 * SLOT;
constexpr size_t WS_GEN = 0, WS_FLAG = 8192, WS_OF = 1 << 20;
constexpr size_t OF_BYTES = (size_t)2 * NB * NH * 32768;
constexpr size_t WS_PB = WS_OF + OF_BYTES;
constexpr unsigned PB_WG = 4 * 8 * 3 * 1024;
#define NT_W 0
#define NT_X 0
#define NT_WO 0
constexpr unsigned SPIN_CAP = 1u << 16;

#define MFMA32(a, b, c) __builtin_amdgcn_mfma_f32_32x32x16_bf16((a), (b), (c), 0, 0, 0)
#define WAITV(n) asm volatile("s_waitcnt vmcnt(" #n ")" ::: "memory")
#define SB() __builtin_amdgcn_sched_barrier(0)
#define BARX() do { SB(); asm volatile("s_barrier" ::: "memory"); SB(); } while (0)
#define BAR() do { SB(); asm volatile("s_waitcnt lgkmcnt(0)\n\ts_barrier" ::: "memory"); SB(); } while (0)

#define STAMP(idx)

DI unsigned cvtpk(float lo, float hi) {
    f32x2 v = {lo, hi};
    bf16x2_t b = __builtin_convertvector(v, bf16x2_t);
    return __builtin_bit_cast(unsigned, b);
}
DI bf16x8 pack8(const f32x16& x, int s) {
    u32x4 w = {cvtpk(x[8 * s + 0], x[8 * s + 1]), cvtpk(x[8 * s + 2], x[8 * s + 3]),
               cvtpk(x[8 * s + 4], x[8 * s + 5]), cvtpk(x[8 * s + 6], x[8 * s + 7])};
    return __builtin_bit_cast(bf16x8, w);
}
template <int NT> DI void dma(unsigned lds, unsigned voff, rsrc_t srd, unsigned soff) {
    if (NT) asm volatile("s_mov_b32 m0, %0\n\ts_nop 3\n\tbuffer_load_dwordx4 %1, %2, %3 offen nt lds"
                 :: "s"(lds), "v"(voff), "s"(srd), "s"(soff) : "memory");
    else asm volatile("s_mov_b32 m0, %0\n\ts_nop 3\n\tbuffer_load_dwordx4 %1, %2, %3 offen lds"
                 :: "s"(lds), "v"(voff), "s"(srd), "s"(soff) : "memory");
}
DI void ld_sc1(bf16x8& d, unsigned voff, rsrc_t srd, unsigned soff) {
    asm volatile("s_nop 4\n\tbuffer_load_dwordx4 %0, %1, %2, %3 offen sc1" : "+v"(d) : "v"(voff), "s"(srd), "s"(soff) : "memory");
}
#define WAITV_F(n, f) asm volatile("s_waitcnt vmcnt(" #n ")" : "+v"((f)[0]), "+v"((f)[1]), "+v"((f)[2]), "+v"((f)[3]) :: "memory")
DI bf16x8 lds128(lptr p) { return *(const LAS bf16x8*)p; }
DI s16x4 ldstr(lptr p) {
    typedef short v4i16_t __attribute__((ext_vector_type(4)));
    return __builtin_amdgcn_ds_read_tr16_b64_v4i16((LAS v4i16_t*)p);
}
DI bf16x8 cat(s16x4 a, s16x4 b) { return __builtin_shufflevector(a, b, 0, 1, 2, 3, 4, 5, 6, 7); }
DI float swapmax(float v) {
    auto r = __builtin_amdgcn_permlane32_swap(__float_as_uint(v), __float_as_uint(v), false, false);
    return fmaxf(__uint_as_float(r[0]), __uint_as_float(r[1]));
}
DI float swapsum(float v) {
    auto r = __builtin_amdgcn_permlane32_swap(__float_as_uint(v), __float_as_uint(v), false, false);
    return __uint_as_float(r[0]) + __uint_as_float(r[1]);
}
DI rsrc_t mkrsrc(const void* p, unsigned bytes) {
    return __builtin_amdgcn_make_buffer_rsrc((void*)p, (short)0, (int)bytes, 0x00020000);
}
DI void zero16(f32x16& v) {
#pragma unroll
    for (int r = 0; r < 16; ++r) v[r] = 0.f;
}
DI void wait_flags(const GAS u64* p, u64 tag) {
#pragma unroll 1
    for (unsigned it = 0; it < SPIN_CAP; ++it) {
        const u64 v = __hip_atomic_load(p, __ATOMIC_RELAXED, __HIP_MEMORY_SCOPE_AGENT);
        if (__all(v == tag)) break;
        __builtin_amdgcn_s_sleep(1);
    }
    asm volatile("" ::: "memory");
}

DI float max3f(float a, float b, float c) { float r; asm("v_max3_f32 %0, %1, %2, %3" : "=v"(r) : "v"(a), "v"(b), "v"(c)); return r; }
DI float fadd_s(float a, float b) { float r; asm("v_add_f32_e32 %0, %1, %2" : "=v"(r) : "v"(a), "v"(b)); return r; }
DI float fmul_s(float a, float b) { float r; asm("v_mul_f32_e32 %0, %1, %2" : "=v"(r) : "v"(a), "v"(b)); return r; }
DI float fsub_s(float a, float b) { float r; asm("v_sub_f32_e32 %0, %1, %2" : "=v"(r) : "v"(a), "v"(b)); return r; }
#define MPAD4(a) asm volatile("s_nop 7\n\ts_nop 7" : "+v"((a)[0]), "+v"((a)[1]), "+v"((a)[2]), "+v"((a)[3]))
#define PIN(x) asm volatile("" : "+v"(x))
#define PIN4(f) asm volatile("" : "+v"((f)[0]), "+v"((f)[1]), "+v"((f)[2]), "+v"((f)[3]) :: "memory")
DI lptr opaque(lptr p) { unsigned a = (unsigned)(uintptr_t)p; asm volatile("" : "+v"(a)); return (lptr)(uintptr_t)a; }
constexpr int LA = 6;
#define SM_THR 8.0f

struct QBase { lptr x0, x1, w; };
DI void qload(int i, bf16x8* wf, bf16x8* xf, const QBase& q) {
    const int s = i >> 2, d = i & 3;
    if (d == 0) xf[s] = lds128(((s & 1) ? q.x1 : q.x0) + 512 * (s >> 1));
    wf[i & 7] = cat(ldstr(q.w + 4096 * s + 512 * d), ldstr(q.w + 4096 * s + 512 * d + 256));
}
DI void qpre(bf16x8* wf, bf16x8* xf, const QBase& q) {
#pragma unroll
    for (int i = 0; i < LA; ++i) qload(i, wf, xf, q);
}
template <bool NEXT> DI void qsteps(f32x16* acc, bf16x8* wf, bf16x8* xf, const QBase& q, const QBase& qn) {
#pragma unroll
    for (int i = 0; i < 16; ++i) {
        SB();
        acc[i & 3] = MFMA32(wf[i & 7], xf[i >> 2], acc[i & 3]);
        SB();
        if (NEXT && i + LA == 16) BARX();
        if (i + LA < 16) qload(i + LA, wf, xf, q); else if (NEXT) qload(i + LA - 16, wf, xf, qn);
    }
    SB();
}
DI void oblock(f32x16* acc, const bf16x8* of, lptr wp_) {
    const lptr wp = opaque(wp_);
    bf16x8 wf[8];
    auto load = [&](int i) { wf[i & 7] = cat(ldstr(wp + 5120 * (i / 5) + 512 * (i % 5)), ldstr(wp + 5120 * (i / 5) + 2560 + 512 * (i % 5))); };
#pragma unroll
    for (int i = 0; i < LA; ++i) load(i);
#pragma unroll
    for (int i = 0; i < 20; ++i) {
        SB();
        acc[i % 5] = MFMA32(wf[i & 7], of[i / 5], acc[i % 5]);
        SB();
        if (i + LA < 20) load(i + LA);
    }
    SB();
}
DI void aload(int i, bf16x8* fr, lptr vp, lptr kp0, lptr kp1) {
    if (i < 16) fr[i & 7] = cat(ldstr(vp + 4096 * (i >> 2) + 512 * (i & 3)), ldstr(vp + 4096 * (i >> 2) + 2048 + 512 * (i & 3)));
    else { const int s = i & 7; fr[i & 7] = lds128(((s & 1) ? kp1 : kp0) + 512 * (s >> 1) + 8192 * ((i - 16) >> 3)); }
}

__global__ __launch_bounds__(512, 2) void fused_kernel(
    const char* xA, const char* kA, const char* vA, const char* wqA, const char* woA,
    const char* xB, const char* kB, const char* vB, const char* wqB, const char* woB,
    char* ws, char* out)
{
    extern __shared__ __attribute__((aligned(16))) char smem_[];
    const lptr smem = (lptr)smem_;
    const unsigned lds0 = (unsigned)(uintptr_t)smem;
    const int tid = threadIdx.x, lane = tid & 63;
    const int wid = __builtin_amdgcn_readfirstlane(tid >> 6);
    const int bid = blockIdx.x;
    const int xg = bid & 7, ii = bid >> 3;
    const int br = xg >> 2, xq = xg & 3, b = ii >> 2, jj = ii & 3;
    const int h1 = 8 * xq + 2 * jj;
    const char* x = br ? xB : xA;
    const char* kc = br ? kB : kA;
    const char* vc = br ? vB : vA;
    const char* wq = br ? wqB : wqA;
    const char* wo = br ? woB : woA;
    const int qb = wid & 3, grp = wid >> 2, lw = wid & 3;
    const int l31 = lane & 31, h = lane >> 5;
    STAMP(0);

    u64 gen;
    {
        const char* gp = ws + WS_GEN + (br * NB + b) * 256;
        asm volatile("global_load_dwordx2 %0, %1, off sc1" : "=v"(gen) : "v"(gp) : "memory");
    }

    const rsrc_t sk = mkrsrc(kc + (size_t)(b * NH + h1) * SK * DH * 2, 2 * SK * DH * 2);
    const rsrc_t sv = mkrsrc(vc + (size_t)(b * NH + h1) * SK * DH * 2, 2 * SK * DH * 2);
    const rsrc_t sof = mkrsrc(ws + WS_OF + (size_t)((br * NB + b) * NH + 8 * xq) * 32768, 8 * 32768);

    const unsigned dsub = lane >> 5, dr = (lane >> 2) & 7, dsl = lane & 3;
    const unsigned xr0 = 16u * (dsl ^ ((dr >> 2) & 3)), xr1 = 16u * (dsl ^ ((2 + (dr >> 2)) & 3));
    const unsigned vk0 = dr * 256 + dsub * 64 + xr0, vk1 = dr * 256 + dsub * 64 + xr1, vv = dr * 256 + dsub * 64 + 16 * dsl;

    auto issueK = [&](int t, unsigned pair) {
#pragma unroll
        for (int i = 0; i < 4; ++i)
            dma<1>(lds0 + pair + 4096 * lw + 1024 * i, (i >> 1) ? vk1 : vk0, sk, (unsigned)t * 16384u + 4096 * lw + 2048 * (i >> 1) + 128 * (i & 1));
    };
    auto issueV = [&](int t, unsigned pair) {
#pragma unroll
        for (int i = 0; i < 4; ++i)
            dma<1>(lds0 + pair + SLOT + 4096 * lw + 1024 * i, vv, sv, (unsigned)t * 16384u + 4096 * lw + 2048 * (i >> 1) + 128 * (i & 1));
    };
    auto nextpair = [](unsigned p) { return (p == 4u * SLOT) ? 0u : p + 2u * SLOT; };

    const int G1 = (lane >> 4) & 1, tq = (lane & 15) >> 2, tp = lane & 3;
    const int sig = ((tp & 1) << 1) | (tp >> 1);
    const unsigned wqb = SLOT + 2048 * h + 64 * tq + 32 * G1 + 8 * sig;
    const unsigned rowq = 32 * qb + l31;
    const unsigned xsw = (rowq >> 2) & 3;
    const unsigned xb0 = 1024 * (rowq >> 3) + 64 * (rowq & 7) + 16 * ((0 + h) ^ xsw);
    const unsigned xb1 = 1024 * (rowq >> 3) + 64 * (rowq & 7) + 16 * ((2 + h) ^ xsw);
    const unsigned ksw = (l31 >> 2) & 3;
    const unsigned kb0 = 2048 * (l31 >> 3) + 64 * (l31 & 7) + 16 * ((0 + h) ^ ksw);
    const unsigned kb1 = 2048 * (l31 >> 3) + 64 * (l31 & 7) + 16 * ((2 + h) ^ ksw);
    const unsigned trb = 64 * (4 * h + tq) + 32 * G1 + 8 * tp;
    const unsigned vo16 = lane * 16;

    auto flagp = [&](int bb, int stage, int w) { return (GAS u64*)(ws + WS_FLAG + (size_t)((bb * 3 + stage) * 8 + w) * 64); };
    auto partner = [&](int dj) { return (bid & ~24) | (((jj + dj) & 3) << 3); };
    auto hl1 = [&](int dj) { return 2 * ((jj + dj) & 3); };

    f32x16 acc[5];
    bf16x8 qf[8], pf[4], fr[8];
    f32x16 s0, s1;
#pragma unroll
    for (int d = 0; d < 4; ++d) zero16(acc[d]);

    auto nextstage = [](unsigned st) { return st == 8u * SLOT ? 4u * SLOT : (st == 4u * SLOT ? 6u * SLOT : (st == 6u * SLOT ? 2u * SLOT : 8u * SLOT)); };
    auto qbase = [&](unsigned st) { return QBase{opaque(smem + st + xb0), opaque(smem + st + xb1), opaque(smem + st + wqb)}; };
    if (grp == 0) {
        unsigned st = 8 * SLOT;
        BARX();
        QBase qc = qbase(st);
        qpre(fr, pf, qc);
#pragma unroll 1
        for (int i = 0; i < 10; ++i) {
            st = nextstage(st);
            const QBase qn = qbase(st);
            qsteps<true>(acc, fr, pf, qc, qn);
            qc = qn;
        }
        WAITV(0);
    } else {
        const rsrc_t sx = mkrsrc(x + (size_t)b * SQ * DM * 2, SQ * DM * 2);
        const rsrc_t sw = mkrsrc(wq + (size_t)h1 * 256, DM * 8192 - h1 * 256);
        const unsigned vx0 = dr * 1280 + dsub * 64 + xr0, vx1 = dr * 1280 + dsub * 64 + xr1, vw = dr * 8192 + dsub * 64 + 16 * dsl;
        const int rot = b + 3 * jj;
        auto issueQ = [&](int i0, int hd, unsigned st) {
            int i = i0 + rot; i -= (i >= 20) ? 20 : ((i >= 10) ? 10 : 0);
#pragma unroll
            for (int gg = 0; gg < 4; ++gg)
                dma<NT_W>(lds0 + st + SLOT + 4096 * lw + 1024 * gg, vw, sw, (unsigned)(64 * i + 16 * lw + 8 * (gg >> 1)) * 8192u + 256 * hd + 128 * (gg & 1));
#pragma unroll
            for (int gg = 0; gg < 4; ++gg)
                dma<NT_X>(lds0 + st + 4096 * lw + 1024 * gg, (gg & 1) ? vx1 : vx0, sx, (4 * lw + gg) * 8 * 1280 + 128 * i);
        };
        constexpr unsigned T0 = 6 * SLOT;
        unsigned st = 8 * SLOT;
#pragma unroll 1
        for (int u = 0; u < 13; ++u) {
            if (u >= 2) {
                if (u == 7 || u == 8 || u == 12) WAITV(16); else WAITV(8);
                BARX();
            }
            if (u != 11) issueQ(u < 10 ? u : (u - 10) >> 1, u >= 10, st);
            if (u == 6 || u == 11) { const int t = u == 11; issueK(t, t * 2 * SLOT); issueV(t, t * 2 * SLOT); }
            st = nextstage(st);
        }
        unsigned pv = 2 * SLOT;
#pragma unroll 1
        for (int c = 0; c < 10; ++c) {
            const unsigned tc = T0 + (c & 1) * 2 * SLOT, tn = T0 + ((c + 1) & 1) * 2 * SLOT;
            const unsigned pk = nextpair(pv);
            if (c == 0) WAITV(16); else if (c == 1) WAITV(4); else WAITV(8);
            BARX();
            if (c >= 1 && c <= 8) { issueQ(c + 1, 1, tn); issueV(c + 1, pv); }
            if (c <= 8) issueK(c + 2, pk);
            pv = pk;
            const QBase qc = qbase(tc);
            qpre(fr, pf, qc);
            qsteps<false>(acc, fr, pf, qc, qc);
        }
    }
    STAMP(1);
    asm volatile("" : "+v"(gen));
    const u64 tag = gen + 1;

    __builtin_amdgcn_s_setprio(2);
    MPAD4(acc);
    {
        float cexp = CEXP; PIN(cexp);
#pragma unroll
        for (int d = 0; d < 4; ++d) {
#pragma unroll
            for (int r = 0; r < 16; ++r) acc[d][r] = fmul_s(acc[d][r], cexp);
            qf[2 * d] = pack8(acc[d], 0); qf[2 * d + 1] = pack8(acc[d], 1); zero16(acc[d]);
        }
    }
    if (grp == 0) BARX();
    float la = 0.f, lb = 0.f;
    {
        f32x16 negm; zero16(negm); zero16(s1);
        unsigned pk = grp ? 4 * SLOT : 0;
        lptr kp0 = opaque(smem + pk + kb0), kp1 = opaque(smem + pk + kb1);
#pragma unroll
        for (int i = 16; i < 16 + LA; ++i) aload(i, fr, kp0, kp0, kp1);
        const GAS u64* fp = flagp(partner(1 + (lane % 3)), 0, qb);
        u64 fv = 0;
#pragma unroll 1
        for (int t = 0; t < 8; ++t) {
            const bool watch = grp && t == 2;
            if (watch) asm volatile("global_load_dwordx2 %0, %1, off sc1" : "+v"(fv) : "v"(fp) : "memory");
            float c0, c1;
#pragma unroll
            for (int j = 0; j < 16; ++j) {
                const int i = 16 + j;
                SB();
                if (j < 8) s0 = MFMA32(fr[i & 7], qf[j], j == 0 ? negm : s0); else s1 = MFMA32(fr[i & 7], qf[j - 8], j == 8 ? negm : s1);
                SB();
                if (i + LA < 32) aload(i + LA, fr, kp0, kp0, kp1);
                if (j < 8) { la = fadd_s(la, s1[2 * j]); lb = fadd_s(lb, s1[2 * j + 1]); PIN(la); PIN(lb); }
                if (j == 11) { c0 = max3f(s0[0], s0[1], s0[2]); c1 = max3f(s0[3], s0[4], s0[5]); PIN(c0); PIN(c1); }
                if (j == 12) { c0 = max3f(c0, s0[6], s0[7]); c1 = max3f(c1, s0[8], s0[9]); PIN(c0); PIN(c1); }
                if (j == 13) { c0 = max3f(c0, s0[10], s0[11]); c1 = max3f(c1, s0[12], s0[13]); PIN(c0); PIN(c1); }
                if (j == 14) { c0 = max3f(c0, s0[14], s0[15]); PIN(c0); }
            }
            if (t <= 2) {
                WAITV(0);
                if (watch) { PIN(fv); if (!__all(fv == tag)) wait_flags(fp, tag); }
            }
            STAMP(2 + 2 * t);
            BARX();
            const lptr vp = opaque(smem + pk + SLOT + trb);
            pk = nextpair(pk);
            kp0 = opaque(smem + pk + kb0); kp1 = opaque(smem + pk + kb1);
#pragma unroll
            for (int i = 0; i < LA; ++i) aload(i, fr, vp, vp, vp);
            PIN(s1);
            SB();
            {
                float c2 = max3f(s1[0], s1[1], s1[2]), c3 = max3f(s1[3], s1[4], s1[5]);
                c0 = max3f(c0, s1[6], s1[7]); c1 = max3f(c1, s1[8], s1[9]); c2 = max3f(c2, s1[10], s1[11]); c3 = max3f(c3, s1[12], s1[13]);
                c0 = max3f(c0, s1[14], s1[15]); c1 = max3f(c1, c2, c3);
                c0 = max3f(c0, c1, c1);
                auto r_ = __builtin_amdgcn_permlane32_swap(__float_as_uint(c0), __float_as_uint(c0), false, false);
                const float mx = max3f(__uint_as_float(r_[0]), __uint_as_float(r_[1]), __uint_as_float(r_[1]));
                const bool first = t == 0;
                if (first || __any(!(mx <= SM_THR))) {
                    const float d = first ? mx : fmaxf(mx, 0.f);
#pragma unroll
                    for (int r = 0; r < 16; ++r) { s0[r] = fsub_s(s0[r], d); s1[r] = fsub_s(s1[r], d); negm[r] = fsub_s(negm[r], d); }
                    if (!first) {
                        const float f = __builtin_amdgcn_exp2f(-d);
                        la = fmul_s(la, f); lb = fmul_s(lb, f);
#pragma unroll
                        for (int dd = 0; dd < 4; ++dd)
#pragma unroll
                            for (int r = 0; r < 16; ++r) acc[dd][r] = fmul_s(acc[dd][r], f);
                    }
                }
            }
            SB();
#pragma unroll
            for (int r = 0; r < 8; ++r) s0[r] = __builtin_amdgcn_exp2f(s0[r]);
            PIN(s0);
            pf[0] = pack8(s0, 0);
            PIN(pf[0]);
            u32x4 pw[4];
#pragma unroll
            for (int i = 0; i < 16; ++i) {
                SB();
                acc[i & 3] = MFMA32(fr[i & 7], (i >> 2) == 0 ? pf[0] : __builtin_bit_cast(bf16x8, pw[i >> 2]), acc[i & 3]);
                SB();
                if (i < 12) {
                    const int e = 8 + 2 * i;
                    if (e < 16) { s0[e] = __builtin_amdgcn_exp2f(s0[e]); s0[e + 1] = __builtin_amdgcn_exp2f(s0[e + 1]); PIN(s0); }
                    else { s1[e - 16] = __builtin_amdgcn_exp2f(s1[e - 16]); s1[e - 15] = __builtin_amdgcn_exp2f(s1[e - 15]); PIN(s1); }
                }
                aload(i + LA, fr, vp, kp0, kp1);
                if (i & 1) { lb = fadd_s(lb, s0[i]); PIN(lb); } else { la = fadd_s(la, s0[i]); PIN(la); }
                if (i < 12) {
                    const int e = 8 + 2 * i;
                    pw[1 + (i >> 2)][i & 3] = e < 16 ? cvtpk(s0[e], s0[e + 1]) : cvtpk(s1[e - 16], s1[e - 15]);
                    PIN(pw[1 + (i >> 2)][i & 3]);
                }
            }
            SB();
            STAMP(3 + 2 * t);
        }
#pragma unroll
        for (int j = 0; j < 8; ++j) { la = fadd_s(la, s1[2 * j]); lb = fadd_s(lb, s1[2 * j + 1]); }
    }
    __builtin_amdgcn_s_setprio(0);
    if (grp == 0) BARX();
    STAMP(18);

    {
        const float inv = 1.f / swapsum(la + lb);
        MPAD4(acc);
#pragma unroll
        for (int d = 0; d < 4; ++d) {
#pragma unroll
            for (int r = 0; r < 16; ++r) acc[d][r] = fmul_s(acc[d][r], inv);
            qf[2 * d] = pack8(acc[d], 0); qf[2 * d + 1] = pack8(acc[d], 1);
        }
#pragma unroll
        for (int f = 0; f < 8; ++f)
            __builtin_amdgcn_raw_buffer_store_b128(__builtin_bit_cast(u32x4, qf[f]), sof, vo16, (2 * jj + grp) * 32768 + qb * 8192 + f * 1024, 16);
#pragma unroll
        for (int n = 0; n < 5; ++n) zero16(acc[n]);
    }
    const rsrc_t swo = mkrsrc(wo + (size_t)(8 * xq * 128) * 1280 + 320 * jj, 1024 * 1280 - 320 * jj);
    const unsigned vwA = dr * 1280 + dsub * 64 + 16 * dsl, vwB = dr * 1280 + 16 * dsl + (dsub ? 8 * 1280 : 256);
    auto issueW = [&](int hl, int half, int buf) {
        const unsigned so = (unsigned)(hl * 128 + 64 * half + 16 * lw) * 1280u, ld = lds0 + buf * WT + 5 * lw * 1024;
        dma<NT_WO>(ld, vwA, swo, so); dma<NT_WO>(ld + 1024, vwA, swo, so + 128); dma<NT_WO>(ld + 2048, vwB, swo, so);
        dma<NT_WO>(ld + 3072, vwA, swo, so + 8 * 1280 + 64); dma<NT_WO>(ld + 4096, vwA, swo, so + 8 * 1280 + 192);
    };
    bf16x8 cur[4], nxt[4];
#pragma unroll
    for (int k = 0; k < 4; ++k) { cur[k] = qf[k]; nxt[k] = qf[4 + k]; }
    auto issueO = [&](bf16x8* f, int blk) {
        const unsigned so = hl1(blk >> 1) * 32768 + qb * 8192 + 4096 * (blk & 1);
#pragma unroll
        for (int i = 0; i < 4; ++i) ld_sc1(f[i], vo16, sof, so + i * 1024);
    };
    if (grp == 0) {
        issueW(hl1(0), 0, 5);
        BARX();
        issueW(hl1(0), 1, 6); issueW(hl1(1), 0, 7);
        issueV(10, 2 * SLOT); issueK(11, 4 * SLOT);
        WAITV(18);
        if (lane == 0) __hip_atomic_store(flagp(bid, 0, qb), tag, __ATOMIC_RELAXED, __HIP_MEMORY_SCOPE_AGENT);
        unsigned pv = 4 * SLOT;
#pragma unroll 1
        for (int k = 0; k < 6; ++k) {
            BARX();
            if (k >= 1) {
                PIN4(nxt);
#pragma unroll
                for (int i = 0; i < 4; ++i) cur[i] = nxt[i];
                PIN4(cur);
                issueO(nxt, k + 1);
                issueW(hl1((k + 2) >> 1), k & 1, 5 + (k + 2) % 3);
            }
            const unsigned pk = nextpair(pv);
            if (k <= 4) { issueV(11 + k, pv); issueK(12 + k, pk); }
            else { issueW(hl1(0) + 1, 0, 2); issueW(hl1(0) + 1, 1, 3); }
            pv = pk;
            oblock(acc, cur, smem + (5 + k % 3) * WT + trb);
            if (k == 0) WAITV(8); else if (k == 5) WAITV(0); else WAITV(13);
        }
        BARX();
        issueO(cur, 7);
        issueW(hl1(1) + 1, 0, 4); issueW(hl1(1) + 1, 1, 7);
    }
    PIN4(nxt);
    oblock(acc, nxt, smem + (grp ? 3 : 5) * WT + trb);
    if (grp == 0) WAITV(10);
    PIN4(cur);
    oblock(acc, cur, smem + (grp ? 2 : 6) * WT + trb);
    if (grp) {
        WAITV(0);
        if (lane == 0) __hip_atomic_store(flagp(bid, 1, qb), tag, __ATOMIC_RELAXED, __HIP_MEMORY_SCOPE_AGENT);
    }
    STAMP(19);
    BAR();
    {
        const lptr xw = smem + (grp ? 32768 + lw * 12288 : lw * 8192) + vo16;
        const lptr xr = smem + (grp ? lw * 8192 : 32768 + lw * 12288) + vo16;
        f32x16 k0, k1, k2;
        if (grp == 0) {
#pragma unroll
            for (int n = 0; n < 2; ++n)
#pragma unroll
                for (int g = 0; g < 4; ++g) *(LAS f32x4*)(xw + (n * 4 + g) * 1024) = (f32x4){acc[3 + n][4 * g], acc[3 + n][4 * g + 1], acc[3 + n][4 * g + 2], acc[3 + n][4 * g + 3]};
            issueW(hl1(2) + 1, 0, 5); issueW(hl1(2) + 1, 1, 6);
            k0 = acc[0]; k1 = acc[1]; k2 = acc[2];
        } else {
#pragma unroll
            for (int n = 0; n < 3; ++n)
#pragma unroll
                for (int g = 0; g < 4; ++g) *(LAS f32x4*)(xw + (n * 4 + g) * 1024) = (f32x4){acc[n][4 * g], acc[n][4 * g + 1], acc[n][4 * g + 2], acc[n][4 * g + 3]};
            k0 = acc[3]; k1 = acc[4]; k2 = acc[4];
        }
        BAR();
#pragma unroll
        for (int g = 0; g < 4; ++g) {
            const f32x4 u0 = *(const LAS f32x4*)(xr + g * 1024), u1 = *(const LAS f32x4*)(xr + (4 + g) * 1024);
#pragma unroll
            for (int e = 0; e < 4; ++e) { k0[4 * g + e] += u0[e]; k1[4 * g + e] += u1[e]; }
        }
        if (grp == 0) {
#pragma unroll
            for (int g = 0; g < 4; ++g) {
                const f32x4 u2 = *(const LAS f32x4*)(xr + (8 + g) * 1024);
#pragma unroll
                for (int e = 0; e < 4; ++e) k2[4 * g + e] += u2[e];
            }
        }
        BAR();
        if (grp) { issueW(hl1(3) + 1, 0, 0); issueW(hl1(3) + 1, 1, 1); }
        STAMP(20);
        wait_flags(flagp(partner(1 + (lane % 3)), 1, qb), tag);
        WAITV(0);
        STAMP(21);
        bf16x8 of2[24];
#pragma unroll
        for (int k = 0; k < 24; ++k)
            of2[k] = __builtin_bit_cast(bf16x8, __builtin_amdgcn_raw_buffer_load_b128(sof, vo16, (hl1(1 + (k >> 3)) + 1) * 32768 + qb * 8192 + (k & 7) * 1024, 16));
        BAR();
        {
            const lptr wp = smem + trb + (grp ? 3 * 512 : 0);
            bf16x8 wf[8];
            auto woff = [](int k) { constexpr int bufs[6] = {4, 7, 5, 6, 0, 1}; return bufs[k >> 2] * WT + 5120 * (k & 3); };
            auto run = [&](auto NKC) {
                constexpr int NK = decltype(NKC)::value;
                auto load = [&](int i) { wf[i & 7] = cat(ldstr(wp + woff(i / NK) + 512 * (i % NK)), ldstr(wp + woff(i / NK) + 2560 + 512 * (i % NK))); };
#pragma unroll
                for (int i = 0; i < LA; ++i) load(i);
#pragma unroll
                for (int i = 0; i < 24 * NK; ++i) {
                    SB();
                    if (i % NK == 0) k0 = MFMA32(wf[i & 7], of2[i / NK], k0);
                    else if (i % NK == 1) k1 = MFMA32(wf[i & 7], of2[i / NK], k1);
                    else k2 = MFMA32(wf[i & 7], of2[i / NK], k2);
                    SB();
                    if (i + LA < 24 * NK) load(i + LA);
                }
                SB();
            };
            if (grp) run(std::integral_constant<int, 2>()); else run(std::integral_constant<int, 3>());
        }
        STAMP(22);
        const rsrc_t spb = mkrsrc(ws + WS_PB, 256 * PB_WG);
        f32x4 m0, m1, m2;
#pragma unroll
        for (int g = 0; g < 4; ++g) {
            const f32x4 v0 = {k0[4 * g], k0[4 * g + 1], k0[4 * g + 2], k0[4 * g + 3]};
            const f32x4 v1 = {k1[4 * g], k1[4 * g + 1], k1[4 * g + 2], k1[4 * g + 3]};
            const f32x4 v2 = {k2[4 * g], k2[4 * g + 1], k2[4 * g + 2], k2[4 * g + 3]};
            if (g == xq) { m0 = v0; m1 = v1; m2 = v2; }
            else {
                const unsigned so = (unsigned)((bid & ~3) | g) * PB_WG + (xq * 8 + wid) * 3072;
                __builtin_amdgcn_raw_buffer_store_b128(__builtin_bit_cast(u32x4, v0), spb, vo16, so, 16);
                __builtin_amdgcn_raw_buffer_store_b128(__builtin_bit_cast(u32x4, v1), spb, vo16, so + 1024, 16);
                if (grp == 0) __builtin_amdgcn_raw_buffer_store_b128(__builtin_bit_cast(u32x4, v2), spb, vo16, so + 2048, 16);
            }
        }
        WAITV(0);
        if (lane == 0) __hip_atomic_store(flagp(bid, 2, wid), tag, __ATOMIC_RELAXED, __HIP_MEMORY_SCOPE_AGENT);
        STAMP(23);
        wait_flags(flagp((bid & ~3) | ((xq + 1 + (lane % 3)) & 3), 2, wid), tag);
        STAMP(24);
        {
            f32x4 u[3][3];
#pragma unroll
            for (int s = 0; s < 3; ++s)
#pragma unroll
                for (int n = 0; n < 3; ++n)
                    u[s][n] = __builtin_bit_cast(f32x4, __builtin_amdgcn_raw_buffer_load_b128(spb, vo16,
                               (n == 2 && grp) ? 0x7ff00000u : (unsigned)bid * PB_WG + (((xq + 1 + s) & 3) * 8 + wid) * 3072 + n * 1024, 16));
            char* op = out + ((size_t)(b * SQ + rowq) * (2 * DM) + br * DM + 160 * jj + (grp ? 96 : 0) + 8 * xq + 4 * h) * 2;
#pragma unroll
            for (int n = 0; n < 3; ++n) {
                const f32x4 mi = n == 0 ? m0 : (n == 1 ? m1 : m2);
                const f32x4 v = (mi + u[0][n]) + (u[1][n] + u[2][n]);
                const u32x2 w = {cvtpk(v[0], v[1]), cvtpk(v[2], v[3])};
                if (n < 2 || grp == 0) *(u32x2*)(op + 64 * n) = w;
            }
        }
        if (xq == 0 && jj == 0 && wid == 0 && lane == 0)
            __hip_atomic_store((GAS u64*)(ws + WS_GEN + (br * NB + b) * 256), tag, __ATOMIC_RELAXED, __HIP_MEMORY_SCOPE_AGENT);
        STAMP(25);
    }
}

extern "C" void kernel_launch(void* const* d_in, const int* in_sizes, int n_in, void* d_out, int out_size,
                              void* d_ws, size_t ws_size, hipStream_t stream) {
    (void)hipFuncSetAttribute((const void*)fused_kernel, hipFuncAttributeMaxDynamicSharedMemorySize, LDS_BYTES);
    hipLaunchKernelGGL(fused_kernel, dim3(256), dim3(512), LDS_BYTES, stream,
                       (const char*)d_in[0], (const char*)d_in[1], (const char*)d_in[2], (const char*)d_in[3], (const char*)d_in[4],
                       (const char*)d_in[5], (const char*)d_in[6], (const char*)d_in[7], (const char*)d_in[8], (const char*)d_in[9],
                       (char*)d_ws, (char*)d_out);
}
```

```cpp
#include <hip/hip_runtime.h>
#include <hip/hip_bf16.h>
#include <stdint.h>
#include <type_traits>

typedef short bf16x8 __attribute__((ext_vector_type(8)));
typedef short s16x4 __attribute__((ext_vector_type(4)));
typedef float f32x16 __attribute__((ext_vector_type(16)));
typedef float f32x4 __attribute__((ext_vector_type(4)));
typedef float f32x2 __attribute__((ext_vector_type(2)));
typedef __bf16 bf16x2_t __attribute__((ext_vector_type(2)));
typedef unsigned u32x4 __attribute__((ext_vector_type(4)));
typedef unsigned u32x2 __attribute__((ext_vector_type(2)));
typedef unsigned long long u64;
#define LAS __attribute__((address_space(3)))
#define GAS __attribute__((address_space(1)))
#define DI __device__ __forceinline__
typedef LAS char* lptr;
typedef __amdgpu_buffer_rsrc_t rsrc_t;

constexpr int NB = 8, SQ = 128, DM = 640, NH = 32, SK = 512, DH = 128;
constexpr float CEXP = 0.08838834764831845f * 1.4426950408889634f;
constexpr int SLOT = 16384;
constexpr int LDS_T = 6 * SLOT;
constexpr int WT = 20480;
constexpr int LDS_BYTES = 10 * SLOT;
constexpr size_t WS_GEN = 0, WS_FLAG = 8192, WS_OF = 1 << 20;
constexpr size_t OF_BYTES = (size_t)2 * NB * NH * 32768;
constexpr size_t WS_PB = WS_OF + OF_BYTES;
constexpr unsigned PB_WG = 4 * 8 * 3 * 1024;
#define NT_W 0
#define NT_X 0
#define NT_WO 0
#define SPRIO 0
#define PBF16 0
#define WS_SHIFT 0
#define NT_KV 1
constexpr unsigned SPIN_CAP = 1u << 16;

#define MFMA32(a, b, c) __builtin_amdgcn_mfma_f32_32x32x16_bf16((a), (b), (c), 0, 0, 0)
#define WAITV(n) asm volatile("s_waitcnt vmcnt(" #n ")" ::: "memory")
#define SB() __builtin_amdgcn_sched_barrier(0)
#define BARX() do { SB(); asm volatile("s_barrier" ::: "memory"); SB(); } while (0)
#define BAR() do { SB(); asm volatile("s_waitcnt lgkmcnt(0)\n\ts_barrier" ::: "memory"); SB(); } while (0)

#define STAMP(idx)

DI unsigned cvtpk(float lo, float hi) {
    f32x2 v = {lo, hi};
    bf16x2_t b = __builtin_convertvector(v, bf16x2_t);
    return __builtin_bit_cast(unsigned, b);
}
DI bf16x8 pack8(const f32x16& x, int s) {
    u32x4 w = {cvtpk(x[8 * s + 0], x[8 * s + 1]), cvtpk(x[8 * s + 2], x[8 * s + 3]),
               cvtpk(x[8 * s + 4], x[8 * s + 5]), cvtpk(x[8 * s + 6], x[8 * s + 7])};
    return __builtin_bit_cast(bf16x8, w);
}
template <int NT> DI void dma4(unsigned lds, unsigned v0, unsigned v1, unsigned v2, unsigned v3, rsrc_t srd, unsigned soff) {
    if (NT) asm volatile("s_mov_b32 m0, %0\n\ts_nop 3\n\tbuffer_load_dwordx4 %1, %5, %6 offen nt lds\n\tbuffer_load_dwordx4 %2, %5, %6 offen offset:1024 nt lds\n\t"
                         "buffer_load_dwordx4 %3, %5, %6 offen offset:2048 nt lds\n\tbuffer_load_dwordx4 %4, %5, %6 offen offset:3072 nt lds"
                         :: "s"(lds), "v"(v0), "v"(v1), "v"(v2), "v"(v3), "s"(srd), "s"(soff) : "memory");
    else asm volatile("s_mov_b32 m0, %0\n\ts_nop 3\n\tbuffer_load_dwordx4 %1, %5, %6 offen lds\n\tbuffer_load_dwordx4 %2, %5, %6 offen offset:1024 lds\n\t"
                      "buffer_load_dwordx4 %3, %5, %6 offen offset:2048 lds\n\tbuffer_load_dwordx4 %4, %5, %6 offen offset:3072 lds"
                      :: "s"(lds), "v"(v0), "v"(v1), "v"(v2), "v"(v3), "s"(srd), "s"(soff) : "memory");
}
template <int NT> DI void dma1(unsigned lds, unsigned voff, rsrc_t srd, unsigned soff) {
    if (NT) asm volatile("s_mov_b32 m0, %0\n\ts_nop 3\n\tbuffer_load_dwordx4 %1, %2, %3 offen nt lds" :: "s"(lds), "v"(voff), "s"(srd), "s"(soff) : "memory");
    else asm volatile("s_mov_b32 m0, %0\n\ts_nop 3\n\tbuffer_load_dwordx4 %1, %2, %3 offen lds" :: "s"(lds), "v"(voff), "s"(srd), "s"(soff) : "memory");
}
DI void ld4_sc1(bf16x8* d, unsigned voff, rsrc_t srd, unsigned soff) {
    asm volatile("s_nop 4\n\tbuffer_load_dwordx4 %0, %4, %5, %6 offen sc1\n\tbuffer_load_dwordx4 %1, %4, %5, %6 offen offset:1024 sc1\n\t"
                 "buffer_load_dwordx4 %2, %4, %5, %6 offen offset:2048 sc1\n\tbuffer_load_dwordx4 %3, %4, %5, %6 offen offset:3072 sc1"
                 : "+v"(d[0]), "+v"(d[1]), "+v"(d[2]), "+v"(d[3]) : "v"(voff), "s"(srd), "s"(soff) : "memory");
}
constexpr unsigned BIAS = 4096;
#define WAITV_F(n, f) asm volatile("s_waitcnt vmcnt(" #n ")" : "+v"((f)[0]), "+v"((f)[1]), "+v"((f)[2]), "+v"((f)[3]) :: "memory")
DI bf16x8 lds128(lptr p) { return *(const LAS bf16x8*)p; }
DI s16x4 ldstr(lptr p) {
    typedef short v4i16_t __attribute__((ext_vector_type(4)));
    return __builtin_amdgcn_ds_read_tr16_b64_v4i16((LAS v4i16_t*)p);
}
DI bf16x8 cat(s16x4 a, s16x4 b) { return __builtin_shufflevector(a, b, 0, 1, 2, 3, 4, 5, 6, 7); }
DI float swapmax(float v) {
    auto r = __builtin_amdgcn_permlane32_swap(__float_as_uint(v), __float_as_uint(v), false, false);
    return fmaxf(__uint_as_float(r[0]), __uint_as_float(r[1]));
}
DI float swapsum(float v) {
    auto r = __builtin_amdgcn_permlane32_swap(__float_as_uint(v), __float_as_uint(v), false, false);
    return __uint_as_float(r[0]) + __uint_as_float(r[1]);
}
DI rsrc_t mkrsrc(const void* p, unsigned bytes) {
    return __builtin_amdgcn_make_buffer_rsrc((void*)p, (short)0, (int)bytes, 0x00020000);
}
DI void zero16(f32x16& v) {
#pragma unroll
    for (int r = 0; r < 16; ++r) v[r] = 0.f;
}
DI void wait_flags(const GAS u64* p, u64 tag) {
#pragma unroll 1
    for (unsigned it = 0; it < SPIN_CAP; ++it) {
        const u64 v = __hip_atomic_load(p, __ATOMIC_RELAXED, __HIP_MEMORY_SCOPE_AGENT);
        if (__all(v == tag)) break;
        __builtin_amdgcn_s_sleep(1);
    }
    asm volatile("" ::: "memory");
}

DI float max3f(float a, float b, float c) { float r; asm("v_max3_f32 %0, %1, %2, %3" : "=v"(r) : "v"(a), "v"(b), "v"(c)); return r; }
DI float fadd_s(float a, float b) { float r; asm("v_add_f32_e32 %0, %1, %2" : "=v"(r) : "v"(a), "v"(b)); return r; }
DI float fmul_s(float a, float b) { float r; asm("v_mul_f32_e32 %0, %1, %2" : "=v"(r) : "v"(a), "v"(b)); return r; }
DI float fsub_s(float a, float b) { float r; asm("v_sub_f32_e32 %0, %1, %2" : "=v"(r) : "v"(a), "v"(b)); return r; }
#define MPAD4(a) asm volatile("s_nop 7\n\ts_nop 7" : "+v"((a)[0]), "+v"((a)[1]), "+v"((a)[2]), "+v"((a)[3]))
#define PIN(x) asm volatile("" : "+v"(x))
#define PIN4(f) asm volatile("" : "+v"((f)[0]), "+v"((f)[1]), "+v"((f)[2]), "+v"((f)[3]) :: "memory")
DI lptr opaque(lptr p) { unsigned a = (unsigned)(uintptr_t)p; asm volatile("" : "+v"(a)); return (lptr)(uintptr_t)a; }
constexpr int LA = 6;
#define SM_THR 8.0f

struct QBase { lptr x0, x1, w; };
DI void qload(int i, bf16x8* wf, bf16x8* xf, const QBase& q) {
    const int s = i >> 2, d = i & 3;
    if (d == 0) xf[s] = lds128(((s & 1) ? q.x1 : q.x0) + 512 * (s >> 1));
    wf[i & 7] = cat(ldstr(q.w + 4096 * s + 512 * d), ldstr(q.w + 4096 * s + 512 * d + 256));
}
DI void qpre(bf16x8* wf, bf16x8* xf, const QBase& q) {
#pragma unroll
    for (int i = 0; i < LA; ++i) qload(i, wf, xf, q);
}
template <bool NEXT> DI void qsteps(f32x16* acc, bf16x8* wf, bf16x8* xf, const QBase& q, const QBase& qn) {
#pragma unroll
    for (int i = 0; i < 16; ++i) {
        SB();
        acc[i & 3] = MFMA32(wf[i & 7], xf[i >> 2], acc[i & 3]);
        SB();
        if (NEXT && i + LA == 16) BARX();
        if (i + LA < 16) qload(i + LA, wf, xf, q); else if (NEXT) qload(i + LA - 16, wf, xf, qn);
    }
    SB();
}
DI void oblock(f32x16* acc, const bf16x8* of, lptr wp_) {
    const lptr wp = opaque(wp_);
    bf16x8 wf[8];
    auto load = [&](int i) { wf[i & 7] = cat(ldstr(wp + 5120 * (i / 5) + 512 * (i % 5)), ldstr(wp + 5120 * (i / 5) + 2560 + 512 * (i % 5))); };
#pragma unroll
    for (int i = 0; i < LA; ++i) load(i);
#pragma unroll
    for (int i = 0; i < 20; ++i) {
        SB();
        acc[i % 5] = MFMA32(wf[i & 7], of[i / 5], acc[i % 5]);
        SB();
        if (i + LA < 20) load(i + LA);
    }
    SB();
}
DI void aload(int i, bf16x8* fr, lptr vp, lptr kp0, lptr kp1) {
    if (i < 16) fr[i & 7] = cat(ldstr(vp + 4096 * (i >> 2) + 512 * (i & 3)), ldstr(vp + 4096 * (i >> 2) + 2048 + 512 * (i & 3)));
    else { const int s = i & 7; fr[i & 7] = lds128(((s & 1) ? kp1 : kp0) + 512 * (s >> 1) + 8192 * ((i - 16) >> 3)); }
}

__global__ __launch_bounds__(512, 2) void fused_kernel(
    const char* xA, const char* kA, const char* vA, const char* wqA, const char* woA,
    const char* xB, const char* kB, const char* vB, const char* wqB, const char* woB,
    char* ws, char* out)
{
    extern __shared__ __attribute__((aligned(16))) char smem_[];
    const lptr smem = (lptr)smem_;
    const unsigned lds0 = (unsigned)(uintptr_t)smem;
    const int tid = threadIdx.x, lane = tid & 63;
    const int wid = __builtin_amdgcn_readfirstlane(tid >> 6);
    const int bid = blockIdx.x;
    const int xg = bid & 7, ii = bid >> 3;
    const int br = xg >> 2, xq = xg & 3, b = ii >> 2, jj = ii & 3;
    const int h1 = 8 * xq + 2 * jj;
    const char* x = br ? xB : xA;
    const char* kc = br ? kB : kA;
    const char* vc = br ? vB : vA;
    const char* wq = br ? wqB : wqA;
    const char* wo = br ? woB : woA;
    const int qb = wid & 3, grp = wid >> 2, lw = wid & 3;
    const int l31 = lane & 31, h = lane >> 5;
    STAMP(0);

    u64 gen;
    {
        const char* gp = ws + WS_GEN + (br * NB + b) * 256;
        asm volatile("global_load_dwordx2 %0, %1, off sc1" : "=v"(gen) : "v"(gp) : "memory");
    }

    const rsrc_t sk = mkrsrc(kc + (size_t)(b * NH + h1) * SK * DH * 2 - BIAS, 2 * SK * DH * 2 + BIAS);
    const rsrc_t sv = mkrsrc(vc + (size_t)(b * NH + h1) * SK * DH * 2 - BIAS, 2 * SK * DH * 2 + BIAS);
    const rsrc_t sof = mkrsrc(ws + WS_OF + (size_t)((br * NB + b) * NH + 8 * xq) * 32768, 8 * 32768);

    const unsigned dsub = lane >> 5, dr = (lane >> 2) & 7, dsl = lane & 3;
    const unsigned xr0 = 16u * (dsl ^ ((dr >> 2) & 3)), xr1 = 16u * (dsl ^ ((2 + (dr >> 2)) & 3));
    const unsigned vk0 = BIAS + 4096 * lw + dr * 256 + dsub * 64 + xr0, vk2 = BIAS + 4096 * lw + dr * 256 + dsub * 64 + xr1;
    const unsigned vv0 = BIAS + 4096 * lw + dr * 256 + dsub * 64 + 16 * dsl;
    const unsigned ldw = lds0 + 4096 * lw;

    auto issueK = [&](int t, unsigned pair) { dma4<NT_KV>(ldw + pair, vk0, vk0 - 896, vk2, vk2 - 896, sk, (unsigned)t * 16384u); };
    auto issueV = [&](int t, unsigned pair) { dma4<NT_KV>(ldw + pair + SLOT, vv0, vv0 - 896, vv0, vv0 - 896, sv, (unsigned)t * 16384u); };
    auto nextpair = [](unsigned p) { return (p == 4u * SLOT) ? 0u : p + 2u * SLOT; };

    const int G1 = (lane >> 4) & 1, tq = (lane & 15) >> 2, tp = lane & 3;
    const int sig = ((tp & 1) << 1) | (tp >> 1);
    const unsigned wqb = SLOT + 2048 * h + 64 * tq + 32 * G1 + 8 * sig;
    const unsigned rowq = 32 * qb + l31;
    const unsigned xsw = (rowq >> 2) & 3;
    const unsigned xb0 = 1024 * (rowq >> 3) + 64 * (rowq & 7) + 16 * ((0 + h) ^ xsw);
    const unsigned xb1 = 1024 * (rowq >> 3) + 64 * (rowq & 7) + 16 * ((2 + h) ^ xsw);
    const unsigned ksw = (l31 >> 2) & 3;
    const unsigned kb0 = 2048 * (l31 >> 3) + 64 * (l31 & 7) + 16 * ((0 + h) ^ ksw);
    const unsigned kb1 = 2048 * (l31 >> 3) + 64 * (l31 & 7) + 16 * ((2 + h) ^ ksw);
    const unsigned trb = 64 * (4 * h + tq) + 32 * G1 + 8 * tp;
    const unsigned vo16 = lane * 16;

    auto flagp = [&](int bb, int stage, int w) { return (GAS u64*)(ws + WS_FLAG + (size_t)((bb * 3 + stage) * 8 + w) * 64); };
    auto partner = [&](int dj) { return (bid & ~24) | (((jj + dj) & 3) << 3); };
    auto hl1 = [&](int dj) { return 2 * ((jj + dj) & 3); };

    f32x16 acc[5];
    bf16x8 qf[8], pf[4], fr[8];
    f32x16 s0, s1;
#pragma unroll
    for (int d = 0; d < 4; ++d) zero16(acc[d]);

    auto nextstage = [](unsigned st) { return st == 8u * SLOT ? 4u * SLOT : (st == 4u * SLOT ? 6u * SLOT : (st == 6u * SLOT ? 2u * SLOT : 8u * SLOT)); };
    auto qbase = [&](unsigned st) { return QBase{opaque(smem + st + xb0), opaque(smem + st + xb1), opaque(smem + st + wqb)}; };
    if (grp == 0) {
        unsigned st = 8 * SLOT;
        BARX();
        QBase qc = qbase(st);
        qpre(fr, pf, qc);
#pragma unroll 1
        for (int i = 0; i < 10; ++i) {
            st = nextstage(st);
            const QBase qn = qbase(st);
            qsteps<true>(acc, fr, pf, qc, qn);
            qc = qn;
        }
        WAITV(0);
    } else {
        const rsrc_t sx = mkrsrc(x + (size_t)b * SQ * DM * 2, SQ * DM * 2);
        const rsrc_t sw = mkrsrc(wq + (size_t)h1 * 256 - BIAS, DM * 8192 - h1 * 256 + BIAS);
        const unsigned vx0 = (32 * lw + dr) * 1280 + dsub * 64 + xr0, vx1 = (32 * lw + 8 + dr) * 1280 + dsub * 64 + xr1 - 1024;
        const unsigned vw0 = BIAS + (16 * lw + dr) * 8192 + dsub * 64 + 16 * dsl;
        const int rot = b + 3 * jj;
        auto issueQ = [&](int i0, int hd, unsigned st) {
            int i = i0 + rot; i -= (i >= 20) ? 20 : ((i >= 10) ? 10 : 0);
            dma4<NT_W>(ldw + st + SLOT, vw0, vw0 - 896, vw0 + 65536 - 2048, vw0 + 65536 - 2944, sw, (unsigned)i * 524288u + 256 * hd);
            dma4<NT_X>(ldw + st, vx0, vx1, vx0 + 20480 - 2048, vx1 + 20480 - 2048, sx, 128 * i);
        };
        constexpr unsigned T0 = 6 * SLOT;
        unsigned st = 8 * SLOT;
#pragma unroll 1
        for (int u = 0; u < 13; ++u) {
            if (u >= 2) {
                if (u == 7 || u == 8 || u == 12) WAITV(16); else WAITV(8);
                BARX();
            }
            if (u != 11) issueQ(u < 10 ? u : (u - 10) >> 1, u >= 10, st);
            if (u == 6 || u == 11) { const int t = u == 11; issueK(t, t * 2 * SLOT); issueV(t, t * 2 * SLOT); }
            st = nextstage(st);
        }
        unsigned pv = 2 * SLOT;
#pragma unroll 1
        for (int c = 0; c < 10; ++c) {
            const unsigned tc = T0 + (c & 1) * 2 * SLOT, tn = T0 + ((c + 1) & 1) * 2 * SLOT;
            const unsigned pk = nextpair(pv);
            if (c == 0) WAITV(16); else if (c == 1) WAITV(4); else WAITV(8);
            BARX();
            const QBase qc = qbase(tc);
            __builtin_amdgcn_s_setprio(SPRIO);
            qpre(fr, pf, qc);
            if (c >= 1 && c <= 8) issueQ(c + 1, 1, tn);
            qsteps<false>(acc, fr, pf, qc, qc);
            __builtin_amdgcn_s_setprio(0);
            if (c >= 1 && c <= 8) issueV(c + 1, pv);
            if (c <= 8) issueK(c + 2, pk);
            pv = pk;
        }
    }
    STAMP(1);
    asm volatile("" : "+v"(gen));
    const u64 tag = gen + 1;

    __builtin_amdgcn_s_setprio(2);
    MPAD4(acc);
    {
        float cexp = CEXP; PIN(cexp);
#pragma unroll
        for (int d = 0; d < 4; ++d) {
#pragma unroll
            for (int r = 0; r < 16; ++r) acc[d][r] = fmul_s(acc[d][r], cexp);
            qf[2 * d] = pack8(acc[d], 0); qf[2 * d + 1] = pack8(acc[d], 1); zero16(acc[d]);
        }
    }
    if (grp == 0) BARX();
    float la = 0.f, lb = 0.f;
    {
        f32x16 negm; zero16(negm); zero16(s1);
        unsigned pk = grp ? 4 * SLOT : 0;
        lptr kp0 = opaque(smem + pk + kb0), kp1 = opaque(smem + pk + kb1);
#pragma unroll
        for (int i = 16; i < 16 + LA; ++i) aload(i, fr, kp0, kp0, kp1);
        const GAS u64* fp = flagp(partner(1 + (lane % 3)), 0, qb);
        u64 fv = 0;
#pragma unroll 1
        for (int t = 0; t < 8; ++t) {
            const bool watch = grp && t == 2;
            if (watch) asm volatile("global_load_dwordx2 %0, %1, off sc1" : "+v"(fv) : "v"(fp) : "memory");
            float c0, c1;
#pragma unroll
            for (int j = 0; j < 16; ++j) {
                const int i = 16 + j;
                SB();
                if (j < 8) s0 = MFMA32(fr[i & 7], qf[j], j == 0 ? negm : s0); else s1 = MFMA32(fr[i & 7], qf[j - 8], j == 8 ? negm : s1);
                SB();
                if (i + LA < 32) aload(i + LA, fr, kp0, kp0, kp1);
                if (j < 8) { la = fadd_s(la, s1[2 * j]); lb = fadd_s(lb, s1[2 * j + 1]); PIN(la); PIN(lb); }
                if (j == 11) { c0 = max3f(s0[0], s0[1], s0[2]); c1 = max3f(s0[3], s0[4], s0[5]); PIN(c0); PIN(c1); }
                if (j == 12) { c0 = max3f(c0, s0[6], s0[7]); c1 = max3f(c1, s0[8], s0[9]); PIN(c0); PIN(c1); }
                if (j == 13) { c0 = max3f(c0, s0[10], s0[11]); c1 = max3f(c1, s0[12], s0[13]); PIN(c0); PIN(c1); }
                if (j == 14) { c0 = max3f(c0, s0[14], s0[15]); PIN(c0); }
            }
            if (t <= 2) {
                WAITV(0);
                if (watch) { PIN(fv); if (!__all(fv == tag)) wait_flags(fp, tag); }
            }
            STAMP(2 + 2 * t);
            BARX();
            const lptr vp = opaque(smem + pk + SLOT + trb);
            pk = nextpair(pk);
            kp0 = opaque(smem + pk + kb0); kp1 = opaque(smem + pk + kb1);
#pragma unroll
            for (int i = 0; i < LA; ++i) aload(i, fr, vp, vp, vp);
            PIN(s1);
            SB();
            {
                float c2 = max3f(s1[0], s1[1], s1[2]), c3 = max3f(s1[3], s1[4], s1[5]);
                c0 = max3f(c0, s1[6], s1[7]); c1 = max3f(c1, s1[8], s1[9]); c2 = max3f(c2, s1[10], s1[11]); c3 = max3f(c3, s1[12], s1[13]);
                c0 = max3f(c0, s1[14], s1[15]); c1 = max3f(c1, c2, c3);
                c0 = max3f(c0, c1, c1);
                auto r_ = __builtin_amdgcn_permlane32_swap(__float_as_uint(c0), __float_as_uint(c0), false, false);
                const float mx = max3f(__uint_as_float(r_[0]), __uint_as_float(r_[1]), __uint_as_float(r_[1]));
                const bool first = t == 0;
                if (first || __any(!(mx <= SM_THR))) {
                    const float d = first ? mx : fmaxf(mx, 0.f);
#pragma unroll
                    for (int r = 0; r < 16; ++r) { s0[r] = fsub_s(s0[r], d); s1[r] = fsub_s(s1[r], d); negm[r] = fsub_s(negm[r], d); }
                    if (!first) {
                        float f = __builtin_amdgcn_exp2f(-d);
                        asm volatile("s_nop 1" : "+v"(f));
                        la = fmul_s(la, f); lb = fmul_s(lb, f);
#pragma unroll
                        for (int dd = 0; dd < 4; ++dd)
#pragma unroll
                            for (int r = 0; r < 16; ++r) acc[dd][r] = fmul_s(acc[dd][r], f);
                    }
                }
            }
            SB();
#pragma unroll
            for (int r = 0; r < 8; ++r) s0[r] = __builtin_amdgcn_exp2f(s0[r]);
            PIN(s0);
            pf[0] = pack8(s0, 0);
            PIN(pf[0]);
            u32x4 pw[4];
#pragma unroll
            for (int i = 0; i < 16; ++i) {
                SB();
                acc[i & 3] = MFMA32(fr[i & 7], (i >> 2) == 0 ? pf[0] : __builtin_bit_cast(bf16x8, pw[i >> 2]), acc[i & 3]);
                SB();
                if (i < 12) {
                    const int e = 8 + 2 * i;
                    if (e < 16) { s0[e] = __builtin_amdgcn_exp2f(s0[e]); s0[e + 1] = __builtin_amdgcn_exp2f(s0[e + 1]); PIN(s0); }
                    else { s1[e - 16] = __builtin_amdgcn_exp2f(s1[e - 16]); s1[e - 15] = __builtin_amdgcn_exp2f(s1[e - 15]); PIN(s1); }
                }
                aload(i + LA, fr, vp, kp0, kp1);
                if (i & 1) { lb = fadd_s(lb, s0[i]); PIN(lb); } else { la = fadd_s(la, s0[i]); PIN(la); }
                if (i < 12) {
                    const int e = 8 + 2 * i;
                    pw[1 + (i >> 2)][i & 3] = e < 16 ? cvtpk(s0[e], s0[e + 1]) : cvtpk(s1[e - 16], s1[e - 15]);
                    PIN(pw[1 + (i >> 2)][i & 3]);
                }
            }
            SB();
            STAMP(3 + 2 * t);
        }
#pragma unroll
        for (int j = 0; j < 8; ++j) { la = fadd_s(la, s1[2 * j]); lb = fadd_s(lb, s1[2 * j + 1]); }
    }
    __builtin_amdgcn_s_setprio(0);
    if (grp == 0) BARX();
    STAMP(18);

    {
        const float inv = 1.f / swapsum(la + lb);
        MPAD4(acc);
#pragma unroll
        for (int d = 0; d < 4; ++d) {
#pragma unroll
            for (int r = 0; r < 16; ++r) acc[d][r] = fmul_s(acc[d][r], inv);
            qf[2 * d] = pack8(acc[d], 0); qf[2 * d + 1] = pack8(acc[d], 1);
        }
#pragma unroll
        for (int f = 0; f < 8; ++f)
            __builtin_amdgcn_raw_buffer_store_b128(__builtin_bit_cast(u32x4, qf[f]), sof, vo16, (2 * jj + grp) * 32768 + qb * 8192 + f * 1024, 16);
#pragma unroll
        for (int n = 0; n < 5; ++n) zero16(acc[n]);
    }
    const rsrc_t swo = mkrsrc(wo + (size_t)(8 * xq * 128) * 1280 + 320 * jj - BIAS, 1024 * 1280 - 320 * jj + BIAS);
    const unsigned vwA = BIAS + (16 * lw + dr) * 1280 + dsub * 64 + 16 * dsl, vwB = BIAS + (16 * lw + dr) * 1280 + 16 * dsl + (dsub ? 8 * 1280 : 256) - 2048;
    auto issueW = [&](int hl, int half, int buf) {
        const unsigned so = (unsigned)(hl * 128 + 64 * half) * 1280u, ld = lds0 + buf * WT + 5 * lw * 1024;
        dma4<NT_WO>(ld, vwA, vwA - 896, vwB, vwA + 10240 + 64 - 3072, swo, so);
        dma1<NT_WO>(ld + 4096, vwA, swo, so + 10240 + 192);
    };
    bf16x8 cur[4], nxt[4];
#pragma unroll
    for (int k = 0; k < 4; ++k) { cur[k] = qf[k]; nxt[k] = qf[4 + k]; }
    auto issueO = [&](bf16x8* f, int blk) {
        ld4_sc1(f, vo16, sof, hl1(blk >> 1) * 32768 + qb * 8192 + 4096 * (blk & 1));
    };
    if (grp == 0) {
        issueW(hl1(0), 0, 5);
        BARX();
        issueW(hl1(0), 1, 6); issueW(hl1(1), 0, 7);
        issueV(10, 2 * SLOT); issueK(11, 4 * SLOT);
        WAITV(18);
        if (lane == 0) __hip_atomic_store(flagp(bid, 0, qb), tag, __ATOMIC_RELAXED, __HIP_MEMORY_SCOPE_AGENT);
        unsigned pv = 4 * SLOT;
#pragma unroll 1
        for (int k = 0; k < 6; ++k) {
            BARX();
            __builtin_amdgcn_s_setprio(SPRIO);
            if (k >= 1) {
                PIN4(nxt);
#pragma unroll
                for (int i = 0; i < 4; ++i) cur[i] = nxt[i];
                PIN4(cur);
                issueO(nxt, k + 1);
                issueW(hl1((k + 2) >> 1), k & 1, 5 + (k + 2) % 3);
            }
            oblock(acc, cur, smem + (5 + k % 3) * WT + trb);
            __builtin_amdgcn_s_setprio(0);
            const unsigned pk = nextpair(pv);
            if (k <= 4) { issueV(11 + k, pv); issueK(12 + k, pk); }
            else { issueW(hl1(0) + 1, 0, 2); issueW(hl1(0) + 1, 1, 3); }
            pv = pk;
            if (k == 0) WAITV(8); else if (k == 5) WAITV(0); else WAITV(13);
        }
        BARX();
        issueO(cur, 7);
        issueW(hl1(1) + 1, 0, 4); issueW(hl1(1) + 1, 1, 7);
    }
    if (grp) BARX();
    PIN4(nxt);
    oblock(acc, nxt, smem + (grp ? 3 : 5) * WT + trb);
    if (grp == 0) WAITV(10);
    else {
        WAITV(0);
        if (lane == 0) __hip_atomic_store(flagp(bid, 1, qb), tag, __ATOMIC_RELAXED, __HIP_MEMORY_SCOPE_AGENT);
    }
    STAMP(19);
    PIN4(cur);
    oblock(acc, cur, smem + (grp ? 2 : 6) * WT + trb);
    {
        if (grp == 0) {
            BARX();
            issueW(hl1(3) + 1, 0, 0); issueW(hl1(3) + 1, 1, 1); issueW(hl1(2) + 1, 0, 5); issueW(hl1(2) + 1, 1, 6);
        }
        STAMP(20);
        wait_flags(flagp(partner(grp ? 3 : 1 + (lane & 1)), 1, qb), tag);
        STAMP(21);
        bf16x8 of2[16];
        const unsigned ob = qb * 8192 + 32768;
#pragma unroll
        for (int k = 0; k < 16; ++k)
            of2[k] = __builtin_bit_cast(bf16x8, __builtin_amdgcn_raw_buffer_load_b128(sof, vo16,
                         (k >= 8 && grp) ? 0x7ff00000u : (unsigned)hl1(grp ? 3 : 1 + (k >> 3)) * 32768 + ob + (k & 7) * 1024, 16));
        if (grp == 0) WAITV(16);
        BARX();
        oblock(acc, of2, smem + (grp ? 0 : 4) * WT + trb);
        oblock(acc, of2 + 4, smem + (grp ? 1 : 7) * WT + trb);
        if (grp == 0) {
            oblock(acc, of2 + 8, smem + 5 * WT + trb);
            oblock(acc, of2 + 12, smem + 6 * WT + trb);
        }
        STAMP(22);
        BARX();
        const lptr xw = smem + (grp ? 32768 + lw * 12288 : lw * 8192) + vo16;
        const lptr xr = smem + (grp ? lw * 8192 : 32768 + lw * 12288) + vo16;
        f32x16 k0, k1, k2;
        if (grp == 0) {
#pragma unroll
            for (int n = 0; n < 2; ++n)
#pragma unroll
                for (int g = 0; g < 4; ++g) *(LAS f32x4*)(xw + (n * 4 + g) * 1024) = (f32x4){acc[3 + n][4 * g], acc[3 + n][4 * g + 1], acc[3 + n][4 * g + 2], acc[3 + n][4 * g + 3]};
            k0 = acc[0]; k1 = acc[1]; k2 = acc[2];
        } else {
#pragma unroll
            for (int n = 0; n < 3; ++n)
#pragma unroll
                for (int g = 0; g < 4; ++g) *(LAS f32x4*)(xw + (n * 4 + g) * 1024) = (f32x4){acc[n][4 * g], acc[n][4 * g + 1], acc[n][4 * g + 2], acc[n][4 * g + 3]};
            k0 = acc[3]; k1 = acc[4]; k2 = acc[4];
        }
        BAR();
        const rsrc_t spb = mkrsrc(ws + WS_PB, 256 * PB_WG);
        f32x4 m0, m1, m2;
#pragma unroll
        for (int g = 0; g < 4; ++g) {
            const f32x4 u0 = *(const LAS f32x4*)(xr + g * 1024), u1 = *(const LAS f32x4*)(xr + (4 + g) * 1024), u2 = *(const LAS f32x4*)(xr + (8 + g) * 1024);
            const f32x4 v0 = (f32x4){k0[4 * g], k0[4 * g + 1], k0[4 * g + 2], k0[4 * g + 3]} + u0;
            const f32x4 v1 = (f32x4){k1[4 * g], k1[4 * g + 1], k1[4 * g + 2], k1[4 * g + 3]} + u1;
            const f32x4 v2 = (f32x4){k2[4 * g], k2[4 * g + 1], k2[4 * g + 2], k2[4 * g + 3]} + u2;
            if (g == xq) { m0 = v0; m1 = v1; m2 = v2; }
            else {
                const unsigned so = (unsigned)((bid & ~3) | g) * PB_WG + (xq * 8 + wid) * 3072;
                __builtin_amdgcn_raw_buffer_store_b128(__builtin_bit_cast(u32x4, v0), spb, vo16, so, 16);
                __builtin_amdgcn_raw_buffer_store_b128(__builtin_bit_cast(u32x4, v1), spb, vo16, so + 1024, 16);
                if (grp == 0) __builtin_amdgcn_raw_buffer_store_b128(__builtin_bit_cast(u32x4, v2), spb, vo16, so + 2048, 16);
            }
        }
        WAITV(0);
        if (lane == 0) __hip_atomic_store(flagp(bid, 2, wid), tag, __ATOMIC_RELAXED, __HIP_MEMORY_SCOPE_AGENT);
        STAMP(23);
        wait_flags(flagp((bid & ~3) | ((xq + 1 + (lane % 3)) & 3), 2, wid), tag);
        STAMP(24);
        {
            char* op = out + ((size_t)(b * SQ + rowq) * (2 * DM) + br * DM + 160 * jj + (grp ? 96 : 0) + 8 * xq + 4 * h) * 2;
            f32x4 u[3][3];
#pragma unroll
            for (int s = 0; s < 3; ++s)
#pragma unroll
                for (int n = 0; n < 3; ++n)
                    u[s][n] = __builtin_bit_cast(f32x4, __builtin_amdgcn_raw_buffer_load_b128(spb, vo16,
                               (n == 2 && grp) ? 0x7ff00000u : (unsigned)bid * PB_WG + (((xq + 1 + s) & 3) * 8 + wid) * 3072 + n * 1024, 16));
            m0 = (m0 + u[0][0]) + (u[1][0] + u[2][0]); m1 = (m1 + u[0][1]) + (u[1][1] + u[2][1]); m2 = (m2 + u[0][2]) + (u[1][2] + u[2][2]);
            *(u32x2*)(op) = (u32x2){cvtpk(m0[0], m0[1]), cvtpk(m0[2], m0[3])};
            *(u32x2*)(op + 64) = (u32x2){cvtpk(m1[0], m1[1]), cvtpk(m1[2], m1[3])};
            if (grp == 0) *(u32x2*)(op + 128) = (u32x2){cvtpk(m2[0], m2[1]), cvtpk(m2[2], m2[3])};
        }
        if (xq == 0 && jj == 0 && wid == 0 && lane == 0)
            __hip_atomic_store((GAS u64*)(ws + WS_GEN + (br * NB + b) * 256), tag, __ATOMIC_RELAXED, __HIP_MEMORY_SCOPE_AGENT);
        STAMP(25);
    }
}

extern "C" void kernel_launch(void* const* d_in, const int* in_sizes, int n_in, void* d_out, int out_size,
                              void* d_ws, size_t ws_size, hipStream_t stream) {
    (void)hipFuncSetAttribute((const void*)fused_kernel, hipFuncAttributeMaxDynamicSharedMemorySize, LDS_BYTES);
    hipLaunchKernelGGL(fused_kernel, dim3(256), dim3(512), LDS_BYTES, stream,
                       (const char*)d_in[0], (const char*)d_in[1], (const char*)d_in[2], (const char*)d_in[3], (const char*)d_in[4],
                       (const char*)d_in[5], (const char*)d_in[6], (const char*)d_in[7], (const char*)d_in[8], (const char*)d_in[9],
                       (char*)d_ws + WS_SHIFT, (char*)d_out);
}
```

```cpp
#define POL 15
#include <hip/hip_runtime.h>
#include <hip/hip_bf16.h>
#include <stdint.h>
#include <type_traits>

typedef short bf16x8 __attribute__((ext_vector_type(8)));
typedef short s16x4 __attribute__((ext_vector_type(4)));
typedef float f32x16 __attribute__((ext_vector_type(16)));
typedef float f32x4 __attribute__((ext_vector_type(4)));
typedef float f32x2 __attribute__((ext_vector_type(2)));
typedef __bf16 bf16x2_t __attribute__((ext_vector_type(2)));
typedef unsigned u32x4 __attribute__((ext_vector_type(4)));
typedef unsigned u32x2 __attribute__((ext_vector_type(2)));
typedef unsigned long long u64;
#define LAS __attribute__((address_space(3)))
#define GAS __attribute__((address_space(1)))
#define DI __device__ __forceinline__
typedef LAS char* lptr;
typedef __amdgpu_buffer_rsrc_t rsrc_t;

constexpr int NB = 8, SQ = 128, DM = 640, NH = 32, SK = 512, DH = 128;
constexpr float CEXP = 0.08838834764831845f * 1.4426950408889634f;
constexpr int SLOT = 16384;
constexpr int LDS_T = 6 * SLOT;
constexpr int WT = 20480;
constexpr int LDS_BYTES = 10 * SLOT;
constexpr size_t WS_GEN = 0, WS_FLAG = 8192, WS_OF = 1 << 20;
constexpr size_t OF_BYTES = (size_t)2 * NB * NH * 32768;
constexpr size_t WS_PB = WS_OF + OF_BYTES;
constexpr unsigned PB_WG = 4 * 8 * 3 * 1024;
#define NT_W 0
#define NT_X 0
#define NT_WO 0
#define PPRIO 2
#define PBF16 1
#define WS_SHIFT 0
constexpr unsigned SPIN_CAP = 1u << 16;

#define MFMA32(a, b, c) __builtin_amdgcn_mfma_f32_32x32x16_bf16((a), (b), (c), 0, 0, 0)
#define WAITV(n) asm volatile("s_waitcnt vmcnt(" #n ")" ::: "memory")
#define SB() __builtin_amdgcn_sched_barrier(0)
#define BARX() do { SB(); asm volatile("s_barrier" ::: "memory"); SB(); } while (0)
#define BAR() do { SB(); asm volatile("s_waitcnt lgkmcnt(0)\n\ts_barrier" ::: "memory"); SB(); } while (0)

#define STAMP(idx)

DI unsigned cvtpk(float lo, float hi) {
    f32x2 v = {lo, hi};
    bf16x2_t b = __builtin_convertvector(v, bf16x2_t);
    return __builtin_bit_cast(unsigned, b);
}
DI bf16x8 pack8(const f32x16& x, int s) {
    u32x4 w = {cvtpk(x[8 * s + 0], x[8 * s + 1]), cvtpk(x[8 * s + 2], x[8 * s + 3]),
               cvtpk(x[8 * s + 4], x[8 * s + 5]), cvtpk(x[8 * s + 6], x[8 * s + 7])};
    return __builtin_bit_cast(bf16x8, w);
}
template <int NT> DI void dma4(unsigned lds, unsigned v0, unsigned v1, unsigned v2, unsigned v3, rsrc_t srd, unsigned soff) {
    if (NT) asm volatile("s_mov_b32 m0, %0\n\ts_nop 3\n\tbuffer_load_dwordx4 %1, %5, %6 offen nt lds\n\tbuffer_load_dwordx4 %2, %5, %6 offen offset:1024 nt lds\n\t"
                         "buffer_load_dwordx4 %3, %5, %6 offen offset:2048 nt lds\n\tbuffer_load_dwordx4 %4, %5, %6 offen offset:3072 nt lds"
                         :: "s"(lds), "v"(v0), "v"(v1), "v"(v2), "v"(v3), "s"(srd), "s"(soff) : "memory");
    else asm volatile("s_mov_b32 m0, %0\n\ts_nop 3\n\tbuffer_load_dwordx4 %1, %5, %6 offen lds\n\tbuffer_load_dwordx4 %2, %5, %6 offen offset:1024 lds\n\t"
                      "buffer_load_dwordx4 %3, %5, %6 offen offset:2048 lds\n\tbuffer_load_dwordx4 %4, %5, %6 offen offset:3072 lds"
                      :: "s"(lds), "v"(v0), "v"(v1), "v"(v2), "v"(v3), "s"(srd), "s"(soff) : "memory");
}
template <int NT> DI void dma1(unsigned lds, unsigned voff, rsrc_t srd, unsigned soff) {
    if (NT) asm volatile("s_mov_b32 m0, %0\n\ts_nop 3\n\tbuffer_load_dwordx4 %1, %2, %3 offen nt lds" :: "s"(lds), "v"(voff), "s"(srd), "s"(soff) : "memory");
    else asm volatile("s_mov_b32 m0, %0\n\ts_nop 3\n\tbuffer_load_dwordx4 %1, %2, %3 offen lds" :: "s"(lds), "v"(voff), "s"(srd), "s"(soff) : "memory");
}
DI void ld4_sc1(bf16x8* d, unsigned voff, rsrc_t srd, unsigned soff) {
    asm volatile("s_nop 4\n\tbuffer_load_dwordx4 %0, %4, %5, %6 offen sc1\n\tbuffer_load_dwordx4 %1, %4, %5, %6 offen offset:1024 sc1\n\t"
                 "buffer_load_dwordx4 %2, %4, %5, %6 offen offset:2048 sc1\n\tbuffer_load_dwordx4 %3, %4, %5, %6 offen offset:3072 sc1"
                 : "+v"(d[0]), "+v"(d[1]), "+v"(d[2]), "+v"(d[3]) : "v"(voff), "s"(srd), "s"(soff) : "memory");
}
constexpr unsigned BIAS = 4096;
#define WAITV_F(n, f) asm volatile("s_waitcnt vmcnt(" #n ")" : "+v"((f)[0]), "+v"((f)[1]), "+v"((f)[2]), "+v"((f)[3]) :: "memory")
DI bf16x8 lds128(lptr p) { return *(const LAS bf16x8*)p; }
DI s16x4 ldstr(lptr p) {
    typedef short v4i16_t __attribute__((ext_vector_type(4)));
    return __builtin_amdgcn_ds_read_tr16_b64_v4i16((LAS v4i16_t*)p);
}
DI bf16x8 cat(s16x4 a, s16x4 b) { return __builtin_shufflevector(a, b, 0, 1, 2, 3, 4, 5, 6, 7); }
DI float swapmax(float v) {
    auto r = __builtin_amdgcn_permlane32_swap(__float_as_uint(v), __float_as_uint(v), false, false);
    return fmaxf(__uint_as_float(r[0]), __uint_as_float(r[1]));
}
DI float swapsum(float v) {
    auto r = __builtin_amdgcn_permlane32_swap(__float_as_uint(v), __float_as_uint(v), false, false);
    return __uint_as_float(r[0]) + __uint_as_float(r[1]);
}
DI rsrc_t mkrsrc(const void* p, unsigned bytes) {
    return __builtin_amdgcn_make_buffer_rsrc((void*)p, (short)0, (int)bytes, 0x00020000);
}
DI void zero16(f32x16& v) {
#pragma unroll
    for (int r = 0; r < 16; ++r) v[r] = 0.f;
}
DI void wait_flags(const GAS u64* p, u64 tag) {
#pragma unroll 1
    for (unsigned it = 0; it < SPIN_CAP; ++it) {
        const u64 v = __hip_atomic_load(p, __ATOMIC_RELAXED, __HIP_MEMORY_SCOPE_AGENT);
        if (__all(v == tag)) break;
        __builtin_amdgcn_s_sleep(1);
    }
    asm volatile("" ::: "memory");
}

DI float max3f(float a, float b, float c) { float r; asm("v_max3_f32 %0, %1, %2, %3" : "=v"(r) : "v"(a), "v"(b), "v"(c)); return r; }
DI float fadd_s(float a, float b) { float r; asm("v_add_f32_e32 %0, %1, %2" : "=v"(r) : "v"(a), "v"(b)); return r; }
DI float fmul_s(float a, float b) { float r; asm("v_mul_f32_e32 %0, %1, %2" : "=v"(r) : "v"(a), "v"(b)); return r; }
DI float fsub_s(float a, float b) { float r; asm("v_sub_f32_e32 %0, %1, %2" : "=v"(r) : "v"(a), "v"(b)); return r; }
#define MPAD4(a) asm volatile("s_nop 7\n\ts_nop 7" : "+v"((a)[0]), "+v"((a)[1]), "+v"((a)[2]), "+v"((a)[3]))
#define PIN(x) asm volatile("" : "+v"(x))
#define PIN4(f) asm volatile("" : "+v"((f)[0]), "+v"((f)[1]), "+v"((f)[2]), "+v"((f)[3]) :: "memory")
DI lptr opaque(lptr p) { unsigned a = (unsigned)(uintptr_t)p; asm volatile("" : "+v"(a)); return (lptr)(uintptr_t)a; }
constexpr int LA = 6;
#define SM_THR 8.0f

struct QBase { lptr x0, x1, w; };
DI void qload(int i, bf16x8* wf, bf16x8* xf, const QBase& q) {
    const int s = i >> 2, d = i & 3;
    if (d == 0) xf[s] = lds128(((s & 1) ? q.x1 : q.x0) + 512 * (s >> 1));
    wf[i & 7] = cat(ldstr(q.w + 4096 * s + 512 * d), ldstr(q.w + 4096 * s + 512 * d + 256));
}
DI void qpre(bf16x8* wf, bf16x8* xf, const QBase& q) {
#pragma unroll
    for (int i = 0; i < LA; ++i) qload(i, wf, xf, q);
}
template <bool NEXT> DI void qsteps(f32x16* acc, bf16x8* wf, bf16x8* xf, const QBase& q, const QBase& qn) {
#pragma unroll
    for (int i = 0; i < 16; ++i) {
        SB();
        acc[i & 3] = MFMA32(wf[i & 7], xf[i >> 2], acc[i & 3]);
        SB();
        if (NEXT && i + LA == 16) BARX();
        if (i + LA < 16) qload(i + LA, wf, xf, q); else if (NEXT) qload(i + LA - 16, wf, xf, qn);
    }
    SB();
}
template <bool FIRST = false> DI void oblock(f32x16* acc, const bf16x8* of, lptr wp_) {
    const lptr wp = opaque(wp_);
    bf16x8 wf[8];
    auto load = [&](int i) { wf[i & 7] = cat(ldstr(wp + 5120 * (i / 5) + 512 * (i % 5)), ldstr(wp + 5120 * (i / 5) + 2560 + 512 * (i % 5))); };
#pragma unroll
    for (int i = 0; i < LA; ++i) load(i);
#pragma unroll
    for (int i = 0; i < 20; ++i) {
        SB();
        if (FIRST && i < 5) { f32x16 z; zero16(z); acc[i] = MFMA32(wf[i & 7], of[0], z); }
        else acc[i % 5] = MFMA32(wf[i & 7], of[i / 5], acc[i % 5]);
        SB();
        if (i + LA < 20) load(i + LA);
    }
    SB();
}
DI void aload(int i, bf16x8* fr, lptr vp, lptr kp0, lptr kp1) {
    if (i < 16) fr[i & 7] = cat(ldstr(vp + 4096 * (i >> 2) + 512 * (i & 3)), ldstr(vp + 4096 * (i >> 2) + 2048 + 512 * (i & 3)));
    else { const int s = i & 7; fr[i & 7] = lds128(((s & 1) ? kp1 : kp0) + 512 * (s >> 1) + 8192 * ((i - 16) >> 3)); }
}

__global__ __launch_bounds__(512, 2) void fused_kernel(
    const char* xA, const char* kA, const char* vA, const char* wqA, const char* woA,
    const char* xB, const char* kB, const char* vB, const char* wqB, const char* woB,
    char* ws, char* out)
{
    extern __shared__ __attribute__((aligned(16))) char smem_[];
    const lptr smem = (lptr)smem_;
    const unsigned lds0 = (unsigned)(uintptr_t)smem;
    const int tid = threadIdx.x, lane = tid & 63;
    const int wid = __builtin_amdgcn_readfirstlane(tid >> 6);
    const int bid = blockIdx.x;
    const int xg = bid & 7, ii = bid >> 3;
    const int br = xg >> 2, xq = xg & 3, b = ii >> 2, jj = ii & 3;
    const int h1 = 8 * xq + 2 * jj;
    const char* x = br ? xB : xA;
    const char* kc = br ? kB : kA;
    const char* vc = br ? vB : vA;
    const char* wq = br ? wqB : wqA;
    const char* wo = br ? woB : woA;
    const int qb = wid & 3, grp = wid >> 2, lw = wid & 3;
    const int l31 = lane & 31, h = lane >> 5;
    STAMP(0);

    u64 gen;
    {
        const char* gp = ws + WS_GEN + (br * NB + b) * 256;
        asm volatile("global_load_dwordx2 %0, %1, off sc1" : "=v"(gen) : "v"(gp) : "memory");
    }

    const rsrc_t sk = mkrsrc(kc + (size_t)(b * NH + h1) * SK * DH * 2 - BIAS, 2 * SK * DH * 2 + BIAS);
    const rsrc_t sv = mkrsrc(vc + (size_t)(b * NH + h1) * SK * DH * 2 - BIAS, 2 * SK * DH * 2 + BIAS);
    const rsrc_t sof = mkrsrc(ws + WS_OF + (size_t)((br * NB + b) * NH + 8 * xq) * 32768, 8 * 32768);

    const unsigned dsub = lane >> 5, dr = (lane >> 2) & 7, dsl = lane & 3;
    const unsigned xr0 = 16u * (dsl ^ ((dr >> 2) & 3)), xr1 = 16u * (dsl ^ ((2 + (dr >> 2)) & 3));
    const unsigned vk0 = BIAS + 4096 * lw + dr * 256 + dsub * 64 + xr0, vk2 = BIAS + 4096 * lw + dr * 256 + dsub * 64 + xr1;
    const unsigned vv0 = BIAS + 4096 * lw + dr * 256 + dsub * 64 + 16 * dsl;
    const unsigned ldw = lds0 + 4096 * lw;

    auto issueK = [&](int t, unsigned pair) {
        if ((POL & 1) == ((POL >> 2) & 1) ? (POL & 1) : ((POL >> (2 * br)) & 1)) dma4<1>(ldw + pair, vk0, vk0 - 896, vk2, vk2 - 896, sk, (unsigned)t * 16384u);
        else dma4<0>(ldw + pair, vk0, vk0 - 896, vk2, vk2 - 896, sk, (unsigned)t * 16384u); };
    auto issueV = [&](int t, unsigned pair) {
        if (((POL >> 1) & 1) == ((POL >> 3) & 1) ? ((POL >> 1) & 1) : ((POL >> (2 * br + 1)) & 1)) dma4<1>(ldw + pair + SLOT, vv0, vv0 - 896, vv0, vv0 - 896, sv, (unsigned)t * 16384u);
        else dma4<0>(ldw + pair + SLOT, vv0, vv0 - 896, vv0, vv0 - 896, sv, (unsigned)t * 16384u); };
    auto nextpair = [](unsigned p) { return (p == 4u * SLOT) ? 0u : p + 2u * SLOT; };

    const int G1 = (lane >> 4) & 1, tq = (lane & 15) >> 2, tp = lane & 3;
    const int sig = ((tp & 1) << 1) | (tp >> 1);
    const unsigned wqb = SLOT + 2048 * h + 64 * tq + 32 * G1 + 8 * sig;
    const unsigned rowq = 32 * qb + l31;
    const unsigned xsw = (rowq >> 2) & 3;
    const unsigned xb0 = 1024 * (rowq >> 3) + 64 * (rowq & 7) + 16 * ((0 + h) ^ xsw);
    const unsigned xb1 = 1024 * (rowq >> 3) + 64 * (rowq & 7) + 16 * ((2 + h) ^ xsw);
    const unsigned ksw = (l31 >> 2) & 3;
    const unsigned kb0 = 2048 * (l31 >> 3) + 64 * (l31 & 7) + 16 * ((0 + h) ^ ksw);
    const unsigned kb1 = 2048 * (l31 >> 3) + 64 * (l31 & 7) + 16 * ((2 + h) ^ ksw);
    const unsigned trb = 64 * (4 * h + tq) + 32 * G1 + 8 * tp;
    const unsigned vo16 = lane * 16;

    auto flagp = [&](int bb, int stage, int w) { return (GAS u64*)(ws + WS_FLAG + (size_t)((bb * 3 + stage) * 8 + w) * 64); };
    auto partner = [&](int dj) { return (bid & ~24) | (((jj + dj) & 3) << 3); };
    auto hl1 = [&](int dj) { return 2 * ((jj + dj) & 3); };

    f32x16 acc[5];
    bf16x8 qf[8], pf[4], fr[8];
    f32x16 s0, s1;
#pragma unroll
    for (int d = 0; d < 4; ++d) zero16(acc[d]);

    auto nextstage = [](unsigned st) { return st == 8u * SLOT ? 4u * SLOT : (st == 4u * SLOT ? 6u * SLOT : (st == 6u * SLOT ? 2u * SLOT : 8u * SLOT)); };
    auto qbase = [&](unsigned st) { return QBase{opaque(smem + st + xb0), opaque(smem + st + xb1), opaque(smem + st + wqb)}; };
    if (grp == 0) {
        unsigned st = 8 * SLOT;
        BARX();
        QBase qc = qbase(st);
        qpre(fr, pf, qc);
#pragma unroll 1
        for (int i = 0; i < 10; ++i) {
            st = nextstage(st);
            const QBase qn = qbase(st);
            qsteps<true>(acc, fr, pf, qc, qn);
            qc = qn;
        }
        WAITV(0);
    } else {
        const rsrc_t sx = mkrsrc(x + (size_t)b * SQ * DM * 2, SQ * DM * 2);
        const rsrc_t sw = mkrsrc(wq + (size_t)h1 * 256 - BIAS, DM * 8192 - h1 * 256 + BIAS);
        const unsigned vx0 = (32 * lw + dr) * 1280 + dsub * 64 + xr0, vx1 = (32 * lw + 8 + dr) * 1280 + dsub * 64 + xr1 - 1024;
        const unsigned vw0 = BIAS + (16 * lw + dr) * 8192 + dsub * 64 + 16 * dsl;
        const int rot = b + 3 * jj;
        constexpr unsigned T0 = 6 * SLOT, T1 = 8 * SLOT, P1 = 2 * SLOT, P2 = 4 * SLOT;
        int ci = rot >= 10 ? rot - 10 : rot;
        auto issueQn = [&](int hd, unsigned st) {
            dma4<NT_W>(ldw + st + SLOT, vw0, vw0 - 896, vw0 + 65536 - 2048, vw0 + 65536 - 2944, sw, (unsigned)ci * 524288u + 256 * hd);
            dma4<NT_X>(ldw + st, vx0, vx1, vx0 + 20480 - 2048, vx1 + 20480 - 2048, sx, 128 * ci);
            ci = ci == 9 ? 0 : ci + 1;
        };
        issueQn(0, T1);
        WAITV(0); BARX();
        issueQn(0, P2); issueQn(0, T0);
        WAITV(8);  BARX(); issueQn(0, P1);
        WAITV(8);  BARX(); issueQn(0, T1);
        WAITV(8);  BARX(); issueQn(0, P2);
        WAITV(8);  BARX(); issueQn(0, T0); issueK(0, 0); issueV(0, 0);
        WAITV(16); BARX(); issueQn(0, P1);
        WAITV(16); BARX(); issueQn(0, T1);
        WAITV(8);  BARX(); issueQn(0, P2);
        WAITV(8);  BARX(); issueQn(1, T0);
        WAITV(8);  BARX(); issueK(1, P1); issueV(1, P1);
        BARX(); issueQn(1, T1);
        const QBase q0 = qbase(T0), q1 = qbase(T1);
        WAITV(16); BARX();
        qpre(fr, pf, q0);
        issueK(2, P2);
        qsteps<false>(acc, fr, pf, q0, q0);
        WAITV(4);
        unsigned pv = P2;
#pragma unroll 1
        for (int c = 1; c < 9; c += 2) {
            unsigned pk = nextpair(pv);
            BARX();
            qpre(fr, pf, q1);
            issueQn(1, T0);
            qsteps<false>(acc, fr, pf, q1, q1);
            issueV(c + 1, pv); issueK(c + 2, pk);
            pv = nextpair(pk);
            WAITV(8);
            BARX();
            qpre(fr, pf, q0);
            issueQn(1, T1);
            qsteps<false>(acc, fr, pf, q0, q0);
            issueV(c + 2, pk); issueK(c + 3, pv);
            WAITV(8);
        }
        BARX();
        qpre(fr, pf, q1);
        qsteps<false>(acc, fr, pf, q1, q1);
    }
    STAMP(1);
    asm volatile("" : "+v"(gen));
    const u64 tag = gen + 1;

    __builtin_amdgcn_s_setprio(PPRIO);
    MPAD4(acc);
    {
        float cexp = CEXP; PIN(cexp);
#pragma unroll
        for (int d = 0; d < 4; ++d) {
#pragma unroll
            for (int r = 0; r < 16; ++r) acc[d][r] = fmul_s(acc[d][r], cexp);
            qf[2 * d] = pack8(acc[d], 0); qf[2 * d + 1] = pack8(acc[d], 1); zero16(acc[d]);
        }
    }
    if (grp == 0) BARX();
    float la = 0.f, lb = 0.f;
    {
        f32x16 negm; zero16(negm); zero16(s1);
        unsigned pk = grp ? 4 * SLOT : 0;
        lptr kp0 = opaque(smem + pk + kb0), kp1 = opaque(smem + pk + kb1);
#pragma unroll
        for (int i = 16; i < 16 + LA; ++i) aload(i, fr, kp0, kp0, kp1);
        const GAS u64* fp = flagp(partner(1 + (lane % 3)), 0, qb);
        u64 fv = 0;
#pragma unroll 1
        for (int t = 0; t < 8; ++t) {
            const bool watch = grp && t == 2;
            if (watch) asm volatile("global_load_dwordx2 %0, %1, off sc1" : "+v"(fv) : "v"(fp) : "memory");
            float c0, c1;
#pragma unroll
            for (int j = 0; j < 16; ++j) {
                const int i = 16 + j;
                SB();
                if (j < 8) s0 = MFMA32(fr[i & 7], qf[j], j == 0 ? negm : s0); else s1 = MFMA32(fr[i & 7], qf[j - 8], j == 8 ? negm : s1);
                SB();
                if (i + LA < 32) aload(i + LA, fr, kp0, kp0, kp1);
                if (j < 8) { la = fadd_s(la, s1[2 * j]); lb = fadd_s(lb, s1[2 * j + 1]); PIN(la); PIN(lb); }
                if (j == 11) { c0 = max3f(s0[0], s0[1], s0[2]); c1 = max3f(s0[3], s0[4], s0[5]); PIN(c0); PIN(c1); }
                if (j == 12) { c0 = max3f(c0, s0[6], s0[7]); c1 = max3f(c1, s0[8], s0[9]); PIN(c0); PIN(c1); }
                if (j == 13) { c0 = max3f(c0, s0[10], s0[11]); c1 = max3f(c1, s0[12], s0[13]); PIN(c0); PIN(c1); }
                if (j == 14) { c0 = max3f(c0, s0[14], s0[15]); PIN(c0); }
            }
            if (t <= 2) {
                WAITV(0);
                if (watch) { PIN(fv); if (!__all(fv == tag)) wait_flags(fp, tag); }
            }
            STAMP(2 + 2 * t);
            BARX();
            const lptr vp = opaque(smem + pk + SLOT + trb);
            pk = nextpair(pk);
            kp0 = opaque(smem + pk + kb0); kp1 = opaque(smem + pk + kb1);
#pragma unroll
            for (int i = 0; i < LA; ++i) aload(i, fr, vp, vp, vp);
            PIN(s1);
            SB();
            {
                float c2 = max3f(s1[0], s1[1], s1[2]), c3 = max3f(s1[3], s1[4], s1[5]);
                c0 = max3f(c0, s1[6], s1[7]); c1 = max3f(c1, s1[8], s1[9]); c2 = max3f(c2, s1[10], s1[11]); c3 = max3f(c3, s1[12], s1[13]);
                c0 = max3f(c0, s1[14], s1[15]); c1 = max3f(c1, c2, c3);
                c0 = max3f(c0, c1, c1);
                auto r_ = __builtin_amdgcn_permlane32_swap(__float_as_uint(c0), __float_as_uint(c0), false, false);
                const float mx = max3f(__uint_as_float(r_[0]), __uint_as_float(r_[1]), __uint_as_float(r_[1]));
                const bool first = t == 0;
                if (first || __any(!(mx <= SM_THR))) {
                    const float d = first ? mx : fmaxf(mx, 0.f);
#pragma unroll
                    for (int r = 0; r < 16; ++r) { s0[r] = fsub_s(s0[r], d); s1[r] = fsub_s(s1[r], d); negm[r] = fsub_s(negm[r], d); }
                    if (!first) {
                        float f = __builtin_amdgcn_exp2f(-d);
                        asm volatile("s_nop 1" : "+v"(f));
                        la = fmul_s(la, f); lb = fmul_s(lb, f);
#pragma unroll
                        for (int dd = 0; dd < 4; ++dd)
#pragma unroll
                            for (int r = 0; r < 16; ++r) acc[dd][r] = fmul_s(acc[dd][r], f);
                    }
                }
            }
            SB();
#pragma unroll
            for (int r = 0; r < 8; ++r) s0[r] = __builtin_amdgcn_exp2f(s0[r]);
            PIN(s0);
            pf[0] = pack8(s0, 0);
            PIN(pf[0]);
            u32x4 pw[4];
#pragma unroll
            for (int i = 0; i < 16; ++i) {
                SB();
                acc[i & 3] = MFMA32(fr[i & 7], (i >> 2) == 0 ? pf[0] : __builtin_bit_cast(bf16x8, pw[i >> 2]), acc[i & 3]);
                SB();
                if (i < 12) {
                    const int e = 8 + 2 * i;
                    if (e < 16) { s0[e] = __builtin_amdgcn_exp2f(s0[e]); s0[e + 1] = __builtin_amdgcn_exp2f(s0[e + 1]); PIN(s0); }
                    else { s1[e - 16] = __builtin_amdgcn_exp2f(s1[e - 16]); s1[e - 15] = __builtin_amdgcn_exp2f(s1[e - 15]); PIN(s1); }
                }
                aload(i + LA, fr, vp, kp0, kp1);
                if (i & 1) { lb = fadd_s(lb, s0[i]); PIN(lb); } else { la = fadd_s(la, s0[i]); PIN(la); }
                if (i < 12) {
                    const int e = 8 + 2 * i;
                    pw[1 + (i >> 2)][i & 3] = e < 16 ? cvtpk(s0[e], s0[e + 1]) : cvtpk(s1[e - 16], s1[e - 15]);
                    PIN(pw[1 + (i >> 2)][i & 3]);
                }
            }
            SB();
            STAMP(3 + 2 * t);
        }
#pragma unroll
        for (int j = 0; j < 8; ++j) { la = fadd_s(la, s1[2 * j]); lb = fadd_s(lb, s1[2 * j + 1]); }
    }
    if (grp == 0) { __builtin_amdgcn_s_setprio(0); BARX(); }
    STAMP(18);

    {
        const float inv = 1.f / swapsum(la + lb);
        MPAD4(acc);
#pragma unroll
        for (int d = 0; d < 4; ++d) {
#pragma unroll
            for (int r = 0; r < 16; ++r) acc[d][r] = fmul_s(acc[d][r], inv);
            qf[2 * d] = pack8(acc[d], 0); qf[2 * d + 1] = pack8(acc[d], 1);
            __builtin_amdgcn_raw_buffer_store_b128(__builtin_bit_cast(u32x4, qf[2 * d]), sof, vo16, (2 * jj + grp) * 32768 + qb * 8192 + d * 2048, 16);
            __builtin_amdgcn_raw_buffer_store_b128(__builtin_bit_cast(u32x4, qf[2 * d + 1]), sof, vo16, (2 * jj + grp) * 32768 + qb * 8192 + d * 2048 + 1024, 16);
            SB();
        }
    }
    const rsrc_t swo = mkrsrc(wo + (size_t)(8 * xq * 128) * 1280 + 320 * jj - BIAS, 1024 * 1280 - 320 * jj + BIAS);
    const unsigned vwA = BIAS + (16 * lw + dr) * 1280 + dsub * 64 + 16 * dsl, vwB = BIAS + (16 * lw + dr) * 1280 + 16 * dsl + (dsub ? 8 * 1280 : 256) - 2048;
    auto issueW = [&](int hl, int half, int buf) {
        const unsigned so = (unsigned)(hl * 128 + 64 * half) * 1280u, ld = lds0 + buf * WT + 5 * lw * 1024;
        dma4<NT_WO>(ld, vwA, vwA - 896, vwB, vwA + 10240 + 64 - 3072, swo, so);
        dma1<NT_WO>(ld + 4096, vwA, swo, so + 10240 + 192);
    };
    const lptr x1 = smem + (lw < 2 ? lw * 8192 : 7 * WT + (lw - 2) * 8192) + vo16;
    if (grp) {
#pragma unroll
        for (int f = 0; f < 8; ++f) *(LAS bf16x8*)(x1 + 1024 * f) = qf[f];
        __builtin_amdgcn_s_setprio(0);
        BAR();
        issueW(hl1(1) + 1, 1, 5); issueW(hl1(2) + 1, 0, 6); issueW(hl1(2) + 1, 1, 1);
        BARX();
        WAITV(15);
        if (lane == 0) __hip_atomic_store(flagp(bid, 1, qb), tag, __ATOMIC_RELAXED, __HIP_MEMORY_SCOPE_AGENT);
        STAMP(19);
        issueW(hl1(3) + 1, 0, 0); issueW(hl1(3) + 1, 1, 7);
        wait_flags(flagp(partner(1 + (lane % 3)), 1, qb), tag);
        WAITV(0);
        STAMP(21);
        BARX();
    } else {
        bf16x8 cur[4], nxt[4];
#pragma unroll
        for (int k = 0; k < 4; ++k) { cur[k] = qf[k]; nxt[k] = qf[4 + k]; }
        auto issueO = [&](bf16x8* f, int blk) { ld4_sc1(f, vo16, sof, hl1(blk >> 1) * 32768 + qb * 8192 + 4096 * (blk & 1)); };
        issueW(hl1(0), 0, 5);
        BARX();
        issueW(hl1(0), 1, 6); issueW(hl1(1), 0, 7);
        issueV(10, 2 * SLOT); issueK(11, 4 * SLOT);
        WAITV(18);
        if (lane == 0) __hip_atomic_store(flagp(bid, 0, qb), tag, __ATOMIC_RELAXED, __HIP_MEMORY_SCOPE_AGENT);
        BARX();
        oblock<true>(acc, cur, smem + 5 * WT + trb);
        issueV(11, 4 * SLOT); issueK(12, 0);
        WAITV(8);
        BARX();
        issueO(cur, 2);
        oblock(acc, nxt, smem + 6 * WT + trb);
        issueW(hl1(1), 1, 5);
        issueV(12, 0); issueK(13, 2 * SLOT);
        WAITV(13);
        issueO(nxt, 3);
        unsigned pv = 2 * SLOT;
        int wb = 6;
#pragma unroll 1
        for (int k = 2; k < 5; ++k) {
            const unsigned pk = nextpair(pv);
            const int cb = wb == 7 ? 5 : wb + 1;
            BARX();
            PIN4(cur);
            oblock(acc, cur, smem + cb * WT + trb);
            issueW(hl1((k + 2) >> 1), k & 1, wb);
            issueV(11 + k, pv); issueK(12 + k, pk);
            pv = pk; wb = cb;
            WAITV(13);
            PIN4(nxt);
#pragma unroll
            for (int i = 0; i < 4; ++i) cur[i] = nxt[i];
            PIN4(cur);
            issueO(nxt, k + 2);
        }
        BARX();
        issueW(hl1(3), 1, 6); issueW(hl1(0) + 1, 0, 2); issueW(hl1(0) + 1, 1, 3);
        oblock(acc, cur, smem + 7 * WT + trb);
        issueO(cur, 7);
        WAITV(4);
        BARX();
        issueW(hl1(1) + 1, 0, 4);
        PIN4(nxt);
        oblock(acc, nxt, smem + 5 * WT + trb);
        WAITV(5);
        PIN4(cur);
        oblock(acc, cur, smem + 6 * WT + trb);
        WAITV(0);
        BARX();
        bf16x8 of2[24];
#pragma unroll
        for (int f = 0; f < 8; ++f) of2[f] = lds128(x1 + 1024 * f);
        BAR();
        oblock(acc, of2, smem + 2 * WT + trb);
        oblock(acc, of2 + 4, smem + 3 * WT + trb);
        STAMP(20);
        BARX();
        const unsigned ob = qb * 8192 + 32768;
        auto ldo = [&](int k0) {
#pragma unroll
            for (int k = k0; k < k0 + 4; ++k)
                of2[k] = __builtin_bit_cast(bf16x8, __builtin_amdgcn_raw_buffer_load_b128(sof, vo16, (unsigned)hl1(1 + (k >> 3)) * 32768 + ob + (k & 7) * 1024, 16));
        };
        ldo(0); ldo(4); SB();
        oblock(acc, of2, smem + 4 * WT + trb);       ldo(8);  ldo(12); SB();
        oblock(acc, of2 + 4, smem + 5 * WT + trb);   ldo(16); SB();
        oblock(acc, of2 + 8, smem + 6 * WT + trb);   ldo(20); SB();
        oblock(acc, of2 + 12, smem + 1 * WT + trb);
        oblock(acc, of2 + 16, smem + 0 * WT + trb);
        oblock(acc, of2 + 20, smem + 7 * WT + trb);
        STAMP(22);
        const rsrc_t spb = mkrsrc(ws + WS_PB, 256 * PB_WG);
        f32x4 mine[5];
#pragma unroll
        for (int g = 0; g < 4; ++g) {
            if (g == xq) {
#pragma unroll
                for (int n = 0; n < 5; ++n) mine[n] = (f32x4){acc[n][4 * g], acc[n][4 * g + 1], acc[n][4 * g + 2], acc[n][4 * g + 3]};
            } else {
                const unsigned so = (unsigned)((bid & ~3) | g) * PB_WG + (xq * 4 + qb) * 5120;
                const u32x4 w01 = {cvtpk(acc[0][4 * g], acc[0][4 * g + 1]), cvtpk(acc[0][4 * g + 2], acc[0][4 * g + 3]), cvtpk(acc[1][4 * g], acc[1][4 * g + 1]), cvtpk(acc[1][4 * g + 2], acc[1][4 * g + 3])};
                const u32x4 w23 = {cvtpk(acc[2][4 * g], acc[2][4 * g + 1]), cvtpk(acc[2][4 * g + 2], acc[2][4 * g + 3]), cvtpk(acc[3][4 * g], acc[3][4 * g + 1]), cvtpk(acc[3][4 * g + 2], acc[3][4 * g + 3])};
                const u32x2 w4 = {cvtpk(acc[4][4 * g], acc[4][4 * g + 1]), cvtpk(acc[4][4 * g + 2], acc[4][4 * g + 3])};
                __builtin_amdgcn_raw_buffer_store_b128(w01, spb, vo16, so, 16);
                __builtin_amdgcn_raw_buffer_store_b128(w23, spb, vo16, so + 1024, 16);
                __builtin_amdgcn_raw_buffer_store_b64(w4, spb, lane * 8, so + 2048, 16);
            }
        }
        WAITV(0);
        if (lane == 0) __hip_atomic_store(flagp(bid, 2, qb), tag, __ATOMIC_RELAXED, __HIP_MEMORY_SCOPE_AGENT);
        STAMP(23);
        wait_flags(flagp((bid & ~3) | ((xq + 1 + (lane % 3)) & 3), 2, qb), tag);
        STAMP(24);
        {
            char* op = out + ((size_t)(b * SQ + rowq) * (2 * DM) + br * DM + 160 * jj + 8 * xq + 4 * h) * 2;
            u32x4 a01[3], a23[3]; u32x2 a4[3];
#pragma unroll
            for (int s = 0; s < 3; ++s) {
                const unsigned so = (unsigned)bid * PB_WG + (((xq + 1 + s) & 3) * 4 + qb) * 5120;
                a01[s] = __builtin_amdgcn_raw_buffer_load_b128(spb, vo16, so, 16);
                a23[s] = __builtin_amdgcn_raw_buffer_load_b128(spb, vo16, so + 1024, 16);
                a4[s] = __builtin_amdgcn_raw_buffer_load_b64(spb, lane * 8, so + 2048, 16);
            }
            auto up = [](unsigned lo, unsigned hi) { return (f32x4){__uint_as_float(lo << 16), __uint_as_float(lo & 0xffff0000u), __uint_as_float(hi << 16), __uint_as_float(hi & 0xffff0000u)}; };
#pragma unroll
            for (int s = 0; s < 3; ++s) {
                mine[0] += up(a01[s][0], a01[s][1]); mine[1] += up(a01[s][2], a01[s][3]);
                mine[2] += up(a23[s][0], a23[s][1]); mine[3] += up(a23[s][2], a23[s][3]); mine[4] += up(a4[s][0], a4[s][1]);
            }
#pragma unroll
            for (int n = 0; n < 5; ++n) *(u32x2*)(op + 64 * n) = (u32x2){cvtpk(mine[n][0], mine[n][1]), cvtpk(mine[n][2], mine[n][3])};
        }
    }
    {
        if (xq == 0 && jj == 0 && wid == 0 && lane == 0)
            __hip_atomic_store((GAS u64*)(ws + WS_GEN + (br * NB + b) * 256), tag, __ATOMIC_RELAXED, __HIP_MEMORY_SCOPE_AGENT);
        STAMP(25);
    }
}

extern "C" void kernel_launch(void* const* d_in, const int* in_sizes, int n_in, void* d_out, int out_size,
                              void* d_ws, size_t ws_size, hipStream_t stream) {
    (void)hipFuncSetAttribute((const void*)fused_kernel, hipFuncAttributeMaxDynamicSharedMemorySize, LDS_BYTES);
    hipLaunchKernelGGL(fused_kernel, dim3(256), dim3(512), LDS_BYTES, stream,
                       (const char*)d_in[0], (const char*)d_in[1], (const char*)d_in[2], (const char*)d_in[3], (const char*)d_in[4],
                       (const char*)d_in[5], (const char*)d_in[6], (const char*)d_in[7], (const char*)d_in[8], (const char*)d_in[9],
                       (char*)d_ws + WS_SHIFT, (char*)d_out);
}
```

```cpp
#include <hip/hip_runtime.h>
#include <hip/hip_bf16.h>
#include <stdint.h>
#include <type_traits>

typedef short bf16x8 __attribute__((ext_vector_type(8)));
typedef short s16x4 __attribute__((ext_vector_type(4)));
typedef float f32x16 __attribute__((ext_vector_type(16)));
typedef float f32x4 __attribute__((ext_vector_type(4)));
typedef float f32x2 __attribute__((ext_vector_type(2)));
typedef __bf16 bf16x2_t __attribute__((ext_vector_type(2)));
typedef unsigned u32x4 __attribute__((ext_vector_type(4)));
typedef unsigned u32x2 __attribute__((ext_vector_type(2)));
typedef unsigned long long u64;
#define LAS __attribute__((address_space(3)))
#define GAS __attribute__((address_space(1)))
#define DI __device__ __forceinline__
typedef LAS char* lptr;
typedef __amdgpu_buffer_rsrc_t rsrc_t;

constexpr int NB = 8, SQ = 128, DM = 640, NH = 32, SK = 512, DH = 128;
constexpr float CEXP = 0.08838834764831845f * 1.4426950408889634f;
constexpr int SLOT = 16384;
constexpr int LDS_T = 6 * SLOT;
constexpr int WT = 20480;
constexpr int LDS_BYTES = 10 * SLOT;
constexpr size_t WS_GEN = 0, WS_FLAG = 8192, WS_OF = 1 << 20;
constexpr size_t OF_BYTES = (size_t)2 * NB * NH * 32768;
constexpr size_t WS_PB = WS_OF + OF_BYTES;
constexpr unsigned PB_WG = 4 * 8 * 3 * 1024;
#define NT_W 0
#define NT_X 0
#define NT_WO 0
#define PPRIO 2
#define PBF16 1
#define WS_SHIFT 0
#define POL 15
#define PRETOUCH 1
constexpr unsigned CODE_LINES = 180;
#define WOWARM 1
#define ROT 1
constexpr unsigned SPIN_CAP = 1u << 16;

#define MFMA32(a, b, c) __builtin_amdgcn_mfma_f32_32x32x16_bf16((a), (b), (c), 0, 0, 0)
#define WAITV(n) asm volatile("s_waitcnt vmcnt(" #n ")" ::: "memory")
#define SB() __builtin_amdgcn_sched_barrier(0)
#define BARX() do { SB(); asm volatile("s_barrier" ::: "memory"); SB(); } while (0)
#define BAR() do { SB(); asm volatile("s_waitcnt lgkmcnt(0)\n\ts_barrier" ::: "memory"); SB(); } while (0)

#define STAMP(idx)

DI unsigned cvtpk(float lo, float hi) {
    f32x2 v = {lo, hi};
    bf16x2_t b = __builtin_convertvector(v, bf16x2_t);
    return __builtin_bit_cast(unsigned, b);
}
DI bf16x8 pack8(const f32x16& x, int s) {
    u32x4 w = {cvtpk(x[8 * s + 0], x[8 * s + 1]), cvtpk(x[8 * s + 2], x[8 * s + 3]),
               cvtpk(x[8 * s + 4], x[8 * s + 5]), cvtpk(x[8 * s + 6], x[8 * s + 7])};
    return __builtin_bit_cast(bf16x8, w);
}
template <int NT> DI void dma4(unsigned lds, unsigned v0, unsigned v1, unsigned v2, unsigned v3, rsrc_t srd, unsigned soff) {
    if (NT) asm volatile("s_mov_b32 m0, %0\n\ts_nop 3\n\tbuffer_load_dwordx4 %1, %5, %6 offen nt lds\n\tbuffer_load_dwordx4 %2, %5, %6 offen offset:1024 nt lds\n\t"
                         "buffer_load_dwordx4 %3, %5, %6 offen offset:2048 nt lds\n\tbuffer_load_dwordx4 %4, %5, %6 offen offset:3072 nt lds"
                         :: "s"(lds), "v"(v0), "v"(v1), "v"(v2), "v"(v3), "s"(srd), "s"(soff) : "memory");
    else asm volatile("s_mov_b32 m0, %0\n\ts_nop 3\n\tbuffer_load_dwordx4 %1, %5, %6 offen lds\n\tbuffer_load_dwordx4 %2, %5, %6 offen offset:1024 lds\n\t"
                      "buffer_load_dwordx4 %3, %5, %6 offen offset:2048 lds\n\tbuffer_load_dwordx4 %4, %5, %6 offen offset:3072 lds"
                      :: "s"(lds), "v"(v0), "v"(v1), "v"(v2), "v"(v3), "s"(srd), "s"(soff) : "memory");
}
template <int NT> DI void dma1(unsigned lds, unsigned voff, rsrc_t srd, unsigned soff) {
    if (NT) asm volatile("s_mov_b32 m0, %0\n\ts_nop 3\n\tbuffer_load_dwordx4 %1, %2, %3 offen nt lds" :: "s"(lds), "v"(voff), "s"(srd), "s"(soff) : "memory");
    else asm volatile("s_mov_b32 m0, %0\n\ts_nop 3\n\tbuffer_load_dwordx4 %1, %2, %3 offen lds" :: "s"(lds), "v"(voff), "s"(srd), "s"(soff) : "memory");
}
DI void ld4_sc1(bf16x8* d, unsigned voff, rsrc_t srd, unsigned soff) {
    asm volatile("s_nop 4\n\tbuffer_load_dwordx4 %0, %4, %5, %6 offen sc1\n\tbuffer_load_dwordx4 %1, %4, %5, %6 offen offset:1024 sc1\n\t"
                 "buffer_load_dwordx4 %2, %4, %5, %6 offen offset:2048 sc1\n\tbuffer_load_dwordx4 %3, %4, %5, %6 offen offset:3072 sc1"
                 : "+v"(d[0]), "+v"(d[1]), "+v"(d[2]), "+v"(d[3]) : "v"(voff), "s"(srd), "s"(soff) : "memory");
}
constexpr unsigned BIAS = 4096;
#define WAITV_F(n, f) asm volatile("s_waitcnt vmcnt(" #n ")" : "+v"((f)[0]), "+v"((f)[1]), "+v"((f)[2]), "+v"((f)[3]) :: "memory")
DI bf16x8 lds128(lptr p) { return *(const LAS bf16x8*)p; }
DI s16x4 ldstr(lptr p) {
    typedef short v4i16_t __attribute__((ext_vector_type(4)));
    return __builtin_amdgcn_ds_read_tr16_b64_v4i16((LAS v4i16_t*)p);
}
DI bf16x8 cat(s16x4 a, s16x4 b) { return __builtin_shufflevector(a, b, 0, 1, 2, 3, 4, 5, 6, 7); }
DI float swapmax(float v) {
    auto r = __builtin_amdgcn_permlane32_swap(__float_as_uint(v), __float_as_uint(v), false, false);
    return fmaxf(__uint_as_float(r[0]), __uint_as_float(r[1]));
}
DI float swapsum(float v) {
    auto r = __builtin_amdgcn_permlane32_swap(__float_as_uint(v), __float_as_uint(v), false, false);
    return __uint_as_float(r[0]) + __uint_as_float(r[1]);
}
DI rsrc_t mkrsrc(const void* p, unsigned bytes) {
    return __builtin_amdgcn_make_buffer_rsrc((void*)p, (short)0, (int)bytes, 0x00020000);
}
DI void zero16(f32x16& v) {
#pragma unroll
    for (int r = 0; r < 16; ++r) v[r] = 0.f;
}
DI void wait_flags(const GAS u64* p, u64 tag) {
#pragma unroll 1
    for (unsigned it = 0; it < SPIN_CAP; ++it) {
        const u64 v = __hip_atomic_load(p, __ATOMIC_RELAXED, __HIP_MEMORY_SCOPE_AGENT);
        if (__all(v == tag)) break;
        __builtin_amdgcn_s_sleep(1);
    }
    asm volatile("" ::: "memory");
}

DI float max3f(float a, float b, float c) { float r; asm("v_max3_f32 %0, %1, %2, %3" : "=v"(r) : "v"(a), "v"(b), "v"(c)); return r; }
DI float fadd_s(float a, float b) { float r; asm("v_add_f32_e32 %0, %1, %2" : "=v"(r) : "v"(a), "v"(b)); return r; }
DI float fmul_s(float a, float b) { float r; asm("v_mul_f32_e32 %0, %1, %2" : "=v"(r) : "v"(a), "v"(b)); return r; }
DI float fsub_s(float a, float b) { float r; asm("v_sub_f32_e32 %0, %1, %2" : "=v"(r) : "v"(a), "v"(b)); return r; }
DI float exp_s(float a) { float r; asm("v_exp_f32_e32 %0, %1" : "=v"(r) : "v"(a)); return r; }
#define MPAD4(a) asm volatile("s_nop 7\n\ts_nop 7" : "+v"((a)[0]), "+v"((a)[1]), "+v"((a)[2]), "+v"((a)[3]))
#define PIN(x) asm volatile("" : "+v"(x))
#define PIN4(f) asm volatile("" : "+v"((f)[0]), "+v"((f)[1]), "+v"((f)[2]), "+v"((f)[3]) :: "memory")
DI lptr opaque(lptr p) { unsigned a = (unsigned)(uintptr_t)p; asm volatile("" : "+v"(a)); return (lptr)(uintptr_t)a; }
constexpr int LA = 6;
#define SM_THR 8.0f

struct QBase { lptr x0, x1, w; };
DI void qload(int i, bf16x8* wf, bf16x8* xf, const QBase& q) {
    const int s = i >> 2, d = i & 3;
    if (d == 0) xf[s] = lds128(((s & 1) ? q.x1 : q.x0) + 512 * (s >> 1));
    wf[i & 7] = cat(ldstr(q.w + 4096 * s + 512 * d), ldstr(q.w + 4096 * s + 512 * d + 256));
}
DI void qpre(bf16x8* wf, bf16x8* xf, const QBase& q) {
#pragma unroll
    for (int i = 0; i < LA; ++i) qload(i, wf, xf, q);
}
template <bool NEXT> DI void qsteps(f32x16* acc, bf16x8* wf, bf16x8* xf, const QBase& q, const QBase& qn) {
#pragma unroll
    for (int i = 0; i < 16; ++i) {
        SB();
        acc[i & 3] = MFMA32(wf[i & 7], xf[i >> 2], acc[i & 3]);
        PIN(acc[i & 3]);
        SB();
        if (NEXT && i + LA == 16) BARX();
        if (i + LA < 16) qload(i + LA, wf, xf, q); else if (NEXT) qload(i + LA - 16, wf, xf, qn);
    }
    SB();
}
template <bool FIRST = false> DI void oblock(f32x16* acc, const bf16x8* of, lptr wp_) {
    const lptr wp = opaque(wp_);
    bf16x8 wf[8];
    auto load = [&](int i) { wf[i & 7] = cat(ldstr(wp + 5120 * (i / 5) + 512 * (i % 5)), ldstr(wp + 5120 * (i / 5) + 2560 + 512 * (i % 5))); };
#pragma unroll
    for (int i = 0; i < LA; ++i) load(i);
#pragma unroll
    for (int i = 0; i < 20; ++i) {
        SB();
        if (FIRST && i < 5) { f32x16 z; zero16(z); acc[i] = MFMA32(wf[i & 7], of[0], z); }
        else acc[i % 5] = MFMA32(wf[i & 7], of[i / 5], acc[i % 5]);
        PIN(acc[i % 5]);
        SB();
        if (i + LA < 20) load(i + LA);
    }
    SB();
}
DI void aload(int i, bf16x8* fr, lptr vp, lptr kp0, lptr kp1) {
    if (i < 16) fr[i & 7] = cat(ldstr(vp + 4096 * (i >> 2) + 512 * (i & 3)), ldstr(vp + 4096 * (i >> 2) + 2048 + 512 * (i & 3)));
    else { const int s = i & 7; fr[i & 7] = lds128(((s & 1) ? kp1 : kp0) + 512 * (s >> 1) + 8192 * ((i - 16) >> 3)); }
}

__global__ __launch_bounds__(512, 2) void fused_kernel(
    const char* xA, const char* kA, const char* vA, const char* wqA, const char* woA,
    const char* xB, const char* kB, const char* vB, const char* wqB, const char* woB,
    char* ws, char* out)
{
    extern __shared__ __attribute__((aligned(16))) char smem_[];
    const lptr smem = (lptr)smem_;
    const unsigned lds0 = (unsigned)(uintptr_t)smem;
    const int tid = threadIdx.x, lane = tid & 63;
    const int wid = __builtin_amdgcn_readfirstlane(tid >> 6);
    const int bid = blockIdx.x;
    const int xg = bid & 7, ii = bid >> 3;
    const int br = xg >> 2, xq = xg & 3, b = ii >> 2, jj = ii & 3;
    const int h1 = 8 * xq + 2 * jj;
    const char* x = br ? xB : xA;
    const char* kc = br ? kB : kA;
    const char* vc = br ? vB : vA;
    const char* wq = br ? wqB : wqA;
    const char* wo = br ? woB : woA;
    const int qb = wid & 3, grp = wid >> 2, lw = wid & 3;
    const int l31 = lane & 31, h = lane >> 5;
    STAMP(0);
    const char* codebase;
    asm volatile("s_getpc_b64 %0" : "=s"(codebase));

    u64 gen;
    {
        const char* gp = ws + WS_GEN + (br * NB + b) * 256;
        asm volatile("global_load_dwordx2 %0, %1, off sc1" : "=v"(gen) : "v"(gp) : "memory");
    }

    const rsrc_t sk = mkrsrc(kc + (size_t)(b * NH + h1) * SK * DH * 2 - BIAS, 2 * SK * DH * 2 + BIAS);
    const rsrc_t sv = mkrsrc(vc + (size_t)(b * NH + h1) * SK * DH * 2 - BIAS, 2 * SK * DH * 2 + BIAS);
    const rsrc_t sof = mkrsrc(ws + WS_OF + (size_t)((br * NB + b) * NH + 8 * xq) * 32768, 8 * 32768);

    const unsigned dsub = lane >> 5, dr = (lane >> 2) & 7, dsl = lane & 3;
    const unsigned xr0 = 16u * (dsl ^ ((dr >> 2) & 3)), xr1 = 16u * (dsl ^ ((2 + (dr >> 2)) & 3));
    const unsigned vk0 = BIAS + 4096 * lw + dr * 256 + dsub * 64 + xr0, vk2 = BIAS + 4096 * lw + dr * 256 + dsub * 64 + xr1;
    const unsigned vv0 = BIAS + 4096 * lw + dr * 256 + dsub * 64 + 16 * dsl;
    const unsigned ldw = lds0 + 4096 * lw;

    auto issueK = [&](int t, unsigned pair) {
        if ((POL & 1) == ((POL >> 2) & 1) ? (POL & 1) : ((POL >> (2 * br)) & 1)) dma4<1>(ldw + pair, vk0, vk0 - 896, vk2, vk2 - 896, sk, (unsigned)t * 16384u);
        else dma4<0>(ldw + pair, vk0, vk0 - 896, vk2, vk2 - 896, sk, (unsigned)t * 16384u); };
    auto issueV = [&](int t, unsigned pair) {
        if (((POL >> 1) & 1) == ((POL >> 3) & 1) ? ((POL >> 1) & 1) : ((POL >> (2 * br + 1)) & 1)) dma4<1>(ldw + pair + SLOT, vv0, vv0 - 896, vv0, vv0 - 896, sv, (unsigned)t * 16384u);
        else dma4<0>(ldw + pair + SLOT, vv0, vv0 - 896, vv0, vv0 - 896, sv, (unsigned)t * 16384u); };
    auto nextpair = [](unsigned p) { return (p == 4u * SLOT) ? 0u : p + 2u * SLOT; };

    const int G1 = (lane >> 4) & 1, tq = (lane & 15) >> 2, tp = lane & 3;
    const int sig = ((tp & 1) << 1) | (tp >> 1);
    const unsigned wqb = SLOT + 2048 * h + 64 * tq + 32 * G1 + 8 * sig;
    const unsigned rowq = 32 * qb + l31;
    const unsigned xsw = (rowq >> 2) & 3;
    const unsigned xb0 = 1024 * (rowq >> 3) + 64 * (rowq & 7) + 16 * ((0 + h) ^ xsw);
    const unsigned xb1 = 1024 * (rowq >> 3) + 64 * (rowq & 7) + 16 * ((2 + h) ^ xsw);
    const unsigned ksw = (l31 >> 2) & 3;
    const unsigned kb0 = 2048 * (l31 >> 3) + 64 * (l31 & 7) + 16 * ((0 + h) ^ ksw);
    const unsigned kb1 = 2048 * (l31 >> 3) + 64 * (l31 & 7) + 16 * ((2 + h) ^ ksw);
    const unsigned trb = 64 * (4 * h + tq) + 32 * G1 + 8 * tp;
    const unsigned vo16 = lane * 16;

    auto flagp = [&](int bb, int stage, int w) { return (GAS u64*)(ws + WS_FLAG + (size_t)((bb * 3 + stage) * 8 + w) * 64); };
    auto partner = [&](int dj) { return (bid & ~24) | (((jj + dj) & 3) << 3); };
    auto hl1 = [&](int dj) { return 2 * ((jj + dj) & 3); };

    f32x16 acc[5];
    bf16x8 qf[8], pf[4], fr[8];
    f32x16 s0, s1;
#pragma unroll
    for (int d = 0; d < 4; ++d) zero16(acc[d]);

    auto nextstage = [](unsigned st) { return st == 8u * SLOT ? 4u * SLOT : (st == 4u * SLOT ? 6u * SLOT : (st == 6u * SLOT ? 2u * SLOT : 8u * SLOT)); };
    auto qbase = [&](unsigned st) { return QBase{opaque(smem + st + xb0), opaque(smem + st + xb1), opaque(smem + st + wqb)}; };
    if (grp == 0) {
        unsigned st = 8 * SLOT;
        unsigned dummy = 0;
        {
            const unsigned line = lw * 64 + lane;
            const char* p = codebase + (line < CODE_LINES ? line : 0) * 128;
            asm volatile("global_load_dword %0, %1, off" : "+v"(dummy) : "v"(p) : "memory");
        }
        BARX();
        QBase qc = qbase(st);
        qpre(fr, pf, qc);
#pragma unroll 1
        for (int i = 0; i < 10; ++i) {
            st = nextstage(st);
            const QBase qn = qbase(st);
            qsteps<true>(acc, fr, pf, qc, qn);
            qc = qn;
        }
        WAITV(0);
        PIN(dummy);
    } else {
        const rsrc_t sx = mkrsrc(x + (size_t)b * SQ * DM * 2, SQ * DM * 2);
        const rsrc_t sw = mkrsrc(wq + (size_t)h1 * 256 - BIAS, DM * 8192 - h1 * 256 + BIAS);
        const unsigned vx0 = (32 * lw + dr) * 1280 + dsub * 64 + xr0, vx1 = (32 * lw + 8 + dr) * 1280 + dsub * 64 + xr1 - 1024;
        const unsigned vw0 = BIAS + (16 * lw + dr) * 8192 + dsub * 64 + 16 * dsl;
        const int rot = ROT ? b + 3 * jj : 0;
        constexpr unsigned T0 = 6 * SLOT, T1 = 8 * SLOT, P1 = 2 * SLOT, P2 = 4 * SLOT;
        int ci = rot >= 10 ? rot - 10 : rot;
        auto issueQn = [&](int hd, unsigned st) {
            dma4<NT_W>(ldw + st + SLOT, vw0, vw0 - 896, vw0 + 65536 - 2048, vw0 + 65536 - 2944, sw, (unsigned)ci * 524288u + 256 * hd);
            dma4<NT_X>(ldw + st, vx0, vx1, vx0 + 20480 - 2048, vx1 + 20480 - 2048, sx, 128 * ci);
            ci = ci == 9 ? 0 : ci + 1;
        };
        issueQn(0, T1);
        WAITV(0); BARX();
        issueQn(0, P2); issueQn(0, T0);
        WAITV(8);  BARX(); issueQn(0, P1);
        WAITV(8);  BARX(); issueQn(0, T1);
        WAITV(8);  BARX(); issueQn(0, P2);
        WAITV(8);  BARX(); issueQn(0, T0); issueK(0, 0); issueV(0, 0);
        WAITV(16); BARX(); issueQn(0, P1);
        WAITV(16); BARX(); issueQn(0, T1);
        WAITV(8);  BARX(); issueQn(0, P2);
        WAITV(8);  BARX(); issueQn(1, T0);
        WAITV(8);  BARX(); issueK(1, P1); issueV(1, P1);
        BARX(); issueQn(1, T1);
        const QBase q0 = qbase(T0), q1 = qbase(T1);
        WAITV(16); BARX();
        qpre(fr, pf, q0);
        issueK(2, P2);
        qsteps<false>(acc, fr, pf, q0, q0);
        WAITV(4);
        unsigned pv = P2;
#pragma unroll 1
        for (int c = 1; c < 9; c += 2) {
            unsigned pk = nextpair(pv);
            BARX();
            qpre(fr, pf, q1);
            issueQn(1, T0);
            qsteps<false>(acc, fr, pf, q1, q1);
            issueV(c + 1, pv); issueK(c + 2, pk);
            pv = nextpair(pk);
            WAITV(8);
            BARX();
            qpre(fr, pf, q0);
            issueQn(1, T1);
            qsteps<false>(acc, fr, pf, q0, q0);
            issueV(c + 2, pk); issueK(c + 3, pv);
            WAITV(8);
        }
        BARX();
        qpre(fr, pf, q1);
        qsteps<false>(acc, fr, pf, q1, q1);
    }
    STAMP(1);
    asm volatile("" : "+v"(gen));
    const u64 tag = gen + 1;

    __builtin_amdgcn_s_setprio(PPRIO);
    MPAD4(acc);
    {
        float cexp = CEXP; PIN(cexp);
#pragma unroll
        for (int d = 0; d < 4; ++d) {
#pragma unroll
            for (int r = 0; r < 16; ++r) acc[d][r] = fmul_s(acc[d][r], cexp);
            qf[2 * d] = pack8(acc[d], 0); qf[2 * d + 1] = pack8(acc[d], 1); zero16(acc[d]);
        }
    }
    if (grp == 0) BARX();
    float la = 0.f, lb = 0.f, lc = 0.f, ld = 0.f;
    {
        f32x16 negm, p0; zero16(negm); zero16(s1); zero16(p0);
        unsigned pk = grp ? 4 * SLOT : 0;
        lptr kp0 = opaque(smem + pk + kb0), kp1 = opaque(smem + pk + kb1);
#pragma unroll
        for (int i = 16; i < 16 + LA; ++i) aload(i, fr, kp0, kp0, kp1);
        const GAS u64* fp = flagp(partner(1 + (lane % 3)), 0, qb);
        u64 fv = 0;
        const int tw = grp ? 3 : -1;
        float thr = -__builtin_inff(), dmin = -__builtin_inff();
        u32x4 pw[4];
#pragma unroll 1
        for (int t = 0; t < 8; ++t) {
            if (__builtin_expect(t == tw, 0)) asm volatile("global_load_dwordx2 %0, %1, off sc1" : "+v"(fv) : "v"(fp) : "memory");
            float c0, c1;
#pragma unroll
            for (int j = 0; j < 16; ++j) {
                const int i = 16 + j;
                SB();
                if (j < 8) s0 = MFMA32(fr[i & 7], qf[j], j == 0 ? negm : s0); else s1 = MFMA32(fr[i & 7], qf[j - 8], j == 8 ? negm : s1);
                SB();
                if (i + LA < 32) aload(i + LA, fr, kp0, kp0, kp1);
                if (j < 8) {
                    la = fadd_s(la, s1[2 * j]); lb = fadd_s(lb, s1[2 * j + 1]); lc = fadd_s(lc, p0[2 * j]); ld = fadd_s(ld, p0[2 * j + 1]);
                    PIN(la); PIN(lb); PIN(lc); PIN(ld);
                }
                if (j >= 10) {
                    const int e = 2 * (j - 10);
                    p0[e] = exp_s(s0[e]); p0[e + 1] = exp_s(s0[e + 1]); PIN(p0);
                    if (j == 10) { c0 = max3f(s0[0], s0[1], s0[2]); c1 = max3f(s0[3], s0[4], s0[5]); PIN(c0); PIN(c1); }
                    if (j == 11) { c0 = max3f(c0, s0[6], s0[7]); c1 = max3f(c1, s0[8], s0[9]); PIN(c0); PIN(c1); }
                    if (j == 12) { c0 = max3f(c0, s0[10], s0[11]); PIN(c0); }
                    if (j == 13) { c1 = max3f(c1, s0[12], s0[13]); PIN(c1); }
                    if (j == 14) { c0 = max3f(c0, s0[14], s0[15]); PIN(c0); }
                    if (j == 15) { c0 = max3f(c0, c1, c1); PIN(c0); }
                    pw[e >> 3][(e >> 1) & 3] = cvtpk(p0[e], p0[e + 1]); PIN(pw[e >> 3][(e >> 1) & 3]);
                }
            }
            WAITV(0);
            if (__builtin_expect(t == tw, 0)) { PIN(fv); if (!__all(fv == tag)) wait_flags(fp, tag); }
            STAMP(2 + 2 * t);
            BARX();
            const lptr vp = opaque(smem + pk + SLOT + trb);
            pk = nextpair(pk);
            kp0 = opaque(smem + pk + kb0); kp1 = opaque(smem + pk + kb1);
#pragma unroll
            for (int i = 0; i < LA; ++i) aload(i, fr, vp, vp, vp);
            PIN(s1);
            SB();
            {
                float c2 = max3f(s1[0], s1[1], s1[2]), c3 = max3f(s1[3], s1[4], s1[5]);
                c1 = max3f(s1[6], s1[7], s1[8]); c0 = max3f(c0, s1[9], s1[10]);
                c2 = max3f(c2, s1[11], s1[12]); c3 = max3f(c3, s1[13], s1[14]); c1 = max3f(c1, s1[15], c0);
                c0 = max3f(c1, c2, c3);
                auto r_ = __builtin_amdgcn_permlane32_swap(__float_as_uint(c0), __float_as_uint(c0), false, false);
                const float mx = max3f(__uint_as_float(r_[0]), __uint_as_float(r_[1]), __uint_as_float(r_[1]));
                if (__builtin_expect(__any(!(mx <= thr)), 0)) {
                    const float d = fmaxf(mx, dmin);
                    float f = __builtin_amdgcn_exp2f(fminf(-d, 0.f));
                    asm volatile("s_nop 1" : "+v"(f));
#pragma unroll
                    for (int r = 0; r < 16; ++r) { s0[r] = fsub_s(s0[r], d); s1[r] = fsub_s(s1[r], d); negm[r] = fsub_s(negm[r], d); }
                    la = fmul_s(la, f); lb = fmul_s(lb, f); lc = fmul_s(lc, f); ld = fmul_s(ld, f);
#pragma unroll
                    for (int dd = 0; dd < 4; ++dd)
#pragma unroll
                        for (int r = 0; r < 16; ++r) acc[dd][r] = fmul_s(acc[dd][r], f);
#pragma unroll
                    for (int e = 0; e < 12; ++e) p0[e] = __builtin_amdgcn_exp2f(s0[e]);
#pragma unroll
                    for (int e = 0; e < 12; e += 2) pw[e >> 3][(e >> 1) & 3] = cvtpk(p0[e], p0[e + 1]);
                }
                thr = SM_THR; dmin = 0.f;
            }
#pragma unroll
            for (int i = 0; i < 16; ++i) {
                SB();
                acc[i & 3] = MFMA32(fr[i & 7], __builtin_bit_cast(bf16x8, pw[i >> 2]), acc[i & 3]);
                SB();
                constexpr int pairs[16] = {6, 7, 8, 9, 10, 11, -1, 12, 13, 14, 15, -1, -1, -1, -1, -1};
                const int q = pairs[i];
                if (q >= 0) {
                    if (q < 8) { p0[2 * q] = __builtin_amdgcn_exp2f(s0[2 * q]); p0[2 * q + 1] = __builtin_amdgcn_exp2f(s0[2 * q + 1]); PIN(p0); }
                    else { s1[2 * q - 16] = __builtin_amdgcn_exp2f(s1[2 * q - 16]); s1[2 * q - 15] = __builtin_amdgcn_exp2f(s1[2 * q - 15]); PIN(s1); }
                }
                aload(i + LA, fr, vp, kp0, kp1);
                if (q >= 0) {
                    pw[q >> 2][q & 3] = q < 8 ? cvtpk(p0[2 * q], p0[2 * q + 1]) : cvtpk(s1[2 * q - 16], s1[2 * q - 15]);
                    PIN(pw[q >> 2][q & 3]);
                }
            }
            SB();
            STAMP(3 + 2 * t);
        }
#pragma unroll
        for (int j = 0; j < 8; ++j) { la = fadd_s(la, s1[2 * j]); lb = fadd_s(lb, s1[2 * j + 1]); lc = fadd_s(lc, p0[2 * j]); ld = fadd_s(ld, p0[2 * j + 1]); }
    }
    if (grp == 0) { __builtin_amdgcn_s_setprio(0); BARX(); }
    STAMP(18);

    {
        float inv = __builtin_amdgcn_rcpf(swapsum((la + lb) + (lc + ld)));
        asm volatile("s_nop 1" : "+v"(inv));
        MPAD4(acc);
#pragma unroll
        for (int d = 0; d < 4; ++d) {
#pragma unroll
            for (int r = 0; r < 16; ++r) acc[d][r] = fmul_s(acc[d][r], inv);
            qf[2 * d] = pack8(acc[d], 0); qf[2 * d + 1] = pack8(acc[d], 1);
            __builtin_amdgcn_raw_buffer_store_b128(__builtin_bit_cast(u32x4, qf[2 * d]), sof, vo16, (2 * jj + grp) * 32768 + qb * 8192 + d * 2048, 16);
            __builtin_amdgcn_raw_buffer_store_b128(__builtin_bit_cast(u32x4, qf[2 * d + 1]), sof, vo16, (2 * jj + grp) * 32768 + qb * 8192 + d * 2048 + 1024, 16);
            SB();
        }
    }
    const rsrc_t swo = mkrsrc(wo + (size_t)(8 * xq * 128) * 1280 + 320 * jj - BIAS, 1024 * 1280 - 320 * jj + BIAS);
    const unsigned vwA = BIAS + (16 * lw + dr) * 1280 + dsub * 64 + 16 * dsl, vwB = BIAS + (16 * lw + dr) * 1280 + 16 * dsl + (dsub ? 8 * 1280 : 256) - 2048;
    auto issueW = [&](int hl, int half, int buf) {
        const unsigned so = (unsigned)(hl * 128 + 64 * half) * 1280u, ld = lds0 + buf * WT + 5 * lw * 1024;
        dma4<NT_WO>(ld, vwA, vwA - 896, vwB, vwA + 10240 + 64 - 3072, swo, so);
        dma1<NT_WO>(ld + 4096, vwA, swo, so + 10240 + 192);
    };
    const lptr x1 = smem + (lw < 2 ? lw * 8192 : 7 * WT + (lw - 2) * 8192) + vo16;
    if (grp) {
#pragma unroll
        for (int f = 0; f < 8; ++f) *(LAS bf16x8*)(x1 + 1024 * f) = qf[f];
        __builtin_amdgcn_s_setprio(0);
        BAR();
        BARX();
        WAITV(0);
        if (lane == 0) __hip_atomic_store(flagp(bid, 1, qb), tag, __ATOMIC_RELAXED, __HIP_MEMORY_SCOPE_AGENT);
        STAMP(19);
        issueW(hl1(3) + 1, 0, 0); issueW(hl1(3) + 1, 1, 7);
        wait_flags(flagp(partner(1 + (lane % 3)), 1, qb), tag);
        WAITV(0);
        STAMP(21);
        BARX();
    } else {
        bf16x8 cur[4], nxt[4];
#pragma unroll
        for (int k = 0; k < 4; ++k) { cur[k] = qf[k]; nxt[k] = qf[4 + k]; }
        auto issueO = [&](bf16x8* f, int blk) { ld4_sc1(f, vo16, sof, hl1(blk >> 1) * 32768 + qb * 8192 + 4096 * (blk & 1)); };
        issueW(hl1(0), 0, 5);
        BARX();
        issueW(hl1(0), 1, 6); issueW(hl1(1), 0, 7);
        issueV(10, 2 * SLOT); issueK(11, 4 * SLOT);
        BARX();
        issueV(11, 4 * SLOT); issueK(12, 0);
        WAITV(8);
        if (lane == 0) __hip_atomic_store(flagp(bid, 0, qb), tag, __ATOMIC_RELAXED, __HIP_MEMORY_SCOPE_AGENT);
        BARX();
        oblock<true>(acc, cur, smem + 5 * WT + trb);
        issueV(12, 0); issueK(13, 2 * SLOT);
        WAITV(8);
        BARX();
        issueO(cur, 2);
        issueW(hl1(1), 1, 5);
        oblock(acc, nxt, smem + 6 * WT + trb);
        issueO(nxt, 3);
        issueV(13, 2 * SLOT); issueK(14, 4 * SLOT);
        WAITV(17);
        PIN4(cur);
        oblock(acc, cur, smem + 7 * WT + trb);
        issueO(cur, 4);
        WAITV(12);
        BARX();
        unsigned wdummy = 0;
        {
            const int r = 64 + 40 * b + (lane < 40 ? lane : 0);
            const char* p = wo + (size_t)((8 * xq + hl1(1 + (r >> 7)) + 1) * 128 + (r & 127)) * 1280 + 320 * jj + (lw == 3 ? 316 : 128 * lw);
            asm volatile("global_load_dword %0, %1, off" : "+v"(wdummy) : "v"(p) : "memory");
        }
        issueW(hl1(2), 0, 6); issueW(hl1(2), 1, 7);
        PIN4(nxt);
        oblock(acc, nxt, smem + 5 * WT + trb);
        issueO(nxt, 5);
        issueV(14, 4 * SLOT); issueK(15, 0);
        WAITV(17);
        PIN(wdummy);
        BARX();
        issueW(hl1(3), 0, 5);
        PIN4(cur);
        oblock(acc, cur, smem + 6 * WT + trb);
        issueO(cur, 6);
        issueV(15, 0);
        WAITV(13);
        BARX();
        issueW(hl1(3), 1, 6); issueW(hl1(0) + 1, 0, 2); issueW(hl1(0) + 1, 1, 3);
        PIN4(nxt);
        oblock(acc, nxt, smem + 7 * WT + trb);
        issueO(nxt, 7);
        WAITV(4);
        BARX();
        issueW(hl1(1) + 1, 0, 4);
        PIN4(cur);
        oblock(acc, cur, smem + 5 * WT + trb);
        WAITV(5);
        PIN4(nxt);
        oblock(acc, nxt, smem + 6 * WT + trb);
        WAITV(0);
        BARX();
        bf16x8 of2[24];
#pragma unroll
        for (int f = 0; f < 8; ++f) of2[f] = lds128(x1 + 1024 * f);
        BAR();
        issueW(hl1(1) + 1, 1, 5); issueW(hl1(2) + 1, 0, 6); issueW(hl1(2) + 1, 1, 1);
        oblock(acc, of2, smem + 2 * WT + trb);
        oblock(acc, of2 + 4, smem + 3 * WT + trb);
        WAITV(0);
        STAMP(20);
        BARX();
        const unsigned ob = qb * 8192 + 32768;
        auto ldo = [&](int k0) {
#pragma unroll
            for (int k = k0; k < k0 + 4; ++k)
                of2[k] = __builtin_bit_cast(bf16x8, __builtin_amdgcn_raw_buffer_load_b128(sof, vo16, (unsigned)hl1(1 + (k >> 3)) * 32768 + ob + (k & 7) * 1024, 16));
        };
        ldo(0); ldo(4); SB();
        oblock(acc, of2, smem + 4 * WT + trb);       ldo(8);  ldo(12); SB();
        oblock(acc, of2 + 4, smem + 5 * WT + trb);   ldo(16); SB();
        oblock(acc, of2 + 8, smem + 6 * WT + trb);   ldo(20); SB();
        oblock(acc, of2 + 12, smem + 1 * WT + trb);
        oblock(acc, of2 + 16, smem + 0 * WT + trb);
        oblock(acc, of2 + 20, smem + 7 * WT + trb);
        STAMP(22);
        const rsrc_t spb = mkrsrc(ws + WS_PB, 256 * PB_WG);
        f32x4 mine[5];
#define UNIT(u, e) acc[(u) >> 2][4 * ((u) & 3) + (e)]
#define UPK(u) cvtpk(UNIT(u, 0), UNIT(u, 1)), cvtpk(UNIT(u, 2), UNIT(u, 3))
#pragma unroll
        for (int g = 0; g < 4; ++g) {
            if (g == xq) {
#pragma unroll
                for (int n = 0; n < 5; ++n) mine[n] = (f32x4){UNIT(5 * g + n, 0), UNIT(5 * g + n, 1), UNIT(5 * g + n, 2), UNIT(5 * g + n, 3)};
            } else {
                const unsigned so = (unsigned)((bid & ~3) | g) * PB_WG + (xq * 4 + qb) * 5120;
                const u32x4 w01 = {UPK(5 * g), UPK(5 * g + 1)}, w23 = {UPK(5 * g + 2), UPK(5 * g + 3)};
                const u32x2 w4 = {UPK(5 * g + 4)};
                __builtin_amdgcn_raw_buffer_store_b128(w01, spb, vo16, so, 16);
                __builtin_amdgcn_raw_buffer_store_b128(w23, spb, vo16, so + 1024, 16);
                __builtin_amdgcn_raw_buffer_store_b64(w4, spb, lane * 8, so + 2048, 16);
            }
        }
        WAITV(0);
        if (lane == 0) __hip_atomic_store(flagp(bid, 2, qb), tag, __ATOMIC_RELAXED, __HIP_MEMORY_SCOPE_AGENT);
        STAMP(23);
        wait_flags(flagp((bid & ~3) | ((xq + 1 + (lane % 3)) & 3), 2, qb), tag);
        STAMP(24);
        {
            char* op = out + ((size_t)(b * SQ + rowq) * (2 * DM) + br * DM + 160 * jj + 40 * xq + 4 * h) * 2;
            u32x4 a01[3], a23[3]; u32x2 a4[3];
#pragma unroll
            for (int s = 0; s < 3; ++s) {
                const unsigned so = (unsigned)bid * PB_WG + (((xq + 1 + s) & 3) * 4 + qb) * 5120;
                a01[s] = __builtin_amdgcn_raw_buffer_load_b128(spb, vo16, so, 16);
                a23[s] = __builtin_amdgcn_raw_buffer_load_b128(spb, vo16, so + 1024, 16);
                a4[s] = __builtin_amdgcn_raw_buffer_load_b64(spb, lane * 8, so + 2048, 16);
            }
            auto up = [](unsigned lo, unsigned hi) { return (f32x4){__uint_as_float(lo << 16), __uint_as_float(lo & 0xffff0000u), __uint_as_float(hi << 16), __uint_as_float(hi & 0xffff0000u)}; };
#pragma unroll
            for (int s = 0; s < 3; ++s) {
                mine[0] += up(a01[s][0], a01[s][1]); mine[1] += up(a01[s][2], a01[s][3]);
                mine[2] += up(a23[s][0], a23[s][1]); mine[3] += up(a23[s][2], a23[s][3]); mine[4] += up(a4[s][0], a4[s][1]);
            }
#pragma unroll
            for (int n = 0; n < 5; ++n) *(u32x2*)(op + 16 * n) = (u32x2){cvtpk(mine[n][0], mine[n][1]), cvtpk(mine[n][2], mine[n][3])};
        }
    }
    {
        if (xq == 0 && jj == 0 && wid == 0 && lane == 0)
            __hip_atomic_store((GAS u64*)(ws + WS_GEN + (br * NB + b) * 256), tag, __ATOMIC_RELAXED, __HIP_MEMORY_SCOPE_AGENT);
        STAMP(25);
    }
}

extern "C" void kernel_launch(void* const* d_in, const int* in_sizes, int n_in, void* d_out, int out_size,
                              void* d_ws, size_t ws_size, hipStream_t stream) {
    (void)hipFuncSetAttribute((const void*)fused_kernel, hipFuncAttributeMaxDynamicSharedMemorySize, LDS_BYTES);
    hipLaunchKernelGGL(fused_kernel, dim3(256), dim3(512), LDS_BYTES, stream,
                       (const char*)d_in[0], (const char*)d_in[1], (const char*)d_in[2], (const char*)d_in[3], (const char*)d_in[4],
                       (const char*)d_in[5], (const char*)d_in[6], (const char*)d_in[7], (const char*)d_in[8], (const char*)d_in[9],
                       (char*)d_ws + WS_SHIFT, (char*)d_out);
}
```

```cpp
#include <hip/hip_runtime.h>
#include <hip/hip_bf16.h>
#include <stdint.h>
#include <type_traits>

typedef short bf16x8 __attribute__((ext_vector_type(8)));
typedef short s16x4 __attribute__((ext_vector_type(4)));
typedef float f32x16 __attribute__((ext_vector_type(16)));
typedef float f32x4 __attribute__((ext_vector_type(4)));
typedef float f32x2 __attribute__((ext_vector_type(2)));
typedef __bf16 bf16x2_t __attribute__((ext_vector_type(2)));
typedef unsigned u32x4 __attribute__((ext_vector_type(4)));
typedef unsigned u32x2 __attribute__((ext_vector_type(2)));
typedef unsigned long long u64;
#define LAS __attribute__((address_space(3)))
#define GAS __attribute__((address_space(1)))
#define DI __device__ __forceinline__
typedef LAS char* lptr;
typedef __amdgpu_buffer_rsrc_t rsrc_t;

constexpr int NB = 8, SQ = 128, DM = 640, NH = 32, SK = 512, DH = 128;
constexpr float CEXP = 0.08838834764831845f * 1.4426950408889634f;
constexpr int SLOT = 16384;
constexpr int LDS_T = 6 * SLOT;
constexpr int WT = 20480;
constexpr int LDS_BYTES = 10 * SLOT;
constexpr size_t WS_GEN = 0, WS_FLAG = 8192, WS_OF = 1 << 20;
constexpr size_t OF_BYTES = (size_t)2 * NB * NH * 32768;
constexpr size_t WS_PB = WS_OF + OF_BYTES;
constexpr unsigned PB_WG = 4 * 8 * 3 * 1024;
#define NT_W 0
#define NT_X 0
#define NT_WO 0
#define PPRIO 2
#define PBF16 1
#define WS_SHIFT 0
#define POL 15
#define PRETOUCH 1
constexpr unsigned CODE_LINES = 180;
#define WOWARM 1
#define ROT 1
#define OUT_AUX 0
constexpr unsigned SPIN_CAP = 1u << 16;

#define MFMA32(a, b, c) __builtin_amdgcn_mfma_f32_32x32x16_bf16((a), (b), (c), 0, 0, 0)
#define WAITV(n) asm volatile("s_waitcnt vmcnt(" #n ")" ::: "memory")
#define SB() __builtin_amdgcn_sched_barrier(0)
#define BARX() do { SB(); asm volatile("s_barrier" ::: "memory"); SB(); } while (0)
#define BAR() do { SB(); asm volatile("s_waitcnt lgkmcnt(0)\n\ts_barrier" ::: "memory"); SB(); } while (0)

#define STAMP(idx)

DI unsigned cvtpk(float lo, float hi) {
    f32x2 v = {lo, hi};
    bf16x2_t b = __builtin_convertvector(v, bf16x2_t);
    return __builtin_bit_cast(unsigned, b);
}
DI bf16x8 pack8(const f32x16& x, int s) {
    u32x4 w = {cvtpk(x[8 * s + 0], x[8 * s + 1]), cvtpk(x[8 * s + 2], x[8 * s + 3]),
               cvtpk(x[8 * s + 4], x[8 * s + 5]), cvtpk(x[8 * s + 6], x[8 * s + 7])};
    return __builtin_bit_cast(bf16x8, w);
}
template <int NT> DI void dma4(unsigned lds, unsigned v0, unsigned v1, unsigned v2, unsigned v3, rsrc_t srd, unsigned soff) {
    if (NT) asm volatile("s_mov_b32 m0, %0\n\ts_nop 3\n\tbuffer_load_dwordx4 %1, %5, %6 offen nt lds\n\tbuffer_load_dwordx4 %2, %5, %6 offen offset:1024 nt lds\n\t"
                         "buffer_load_dwordx4 %3, %5, %6 offen offset:2048 nt lds\n\tbuffer_load_dwordx4 %4, %5, %6 offen offset:3072 nt lds"
                         :: "s"(lds), "v"(v0), "v"(v1), "v"(v2), "v"(v3), "s"(srd), "s"(soff) : "memory");
    else asm volatile("s_mov_b32 m0, %0\n\ts_nop 3\n\tbuffer_load_dwordx4 %1, %5, %6 offen lds\n\tbuffer_load_dwordx4 %2, %5, %6 offen offset:1024 lds\n\t"
                      "buffer_load_dwordx4 %3, %5, %6 offen offset:2048 lds\n\tbuffer_load_dwordx4 %4, %5, %6 offen offset:3072 lds"
                      :: "s"(lds), "v"(v0), "v"(v1), "v"(v2), "v"(v3), "s"(srd), "s"(soff) : "memory");
}
template <int NT> DI void dma1(unsigned lds, unsigned voff, rsrc_t srd, unsigned soff) {
    if (NT) asm volatile("s_mov_b32 m0, %0\n\ts_nop 3\n\tbuffer_load_dwordx4 %1, %2, %3 offen nt lds" :: "s"(lds), "v"(voff), "s"(srd), "s"(soff) : "memory");
    else asm volatile("s_mov_b32 m0, %0\n\ts_nop 3\n\tbuffer_load_dwordx4 %1, %2, %3 offen lds" :: "s"(lds), "v"(voff), "s"(srd), "s"(soff) : "memory");
}
DI void ld4_sc1(bf16x8* d, unsigned voff, rsrc_t srd, unsigned soff) {
    asm volatile("s_nop 4\n\tbuffer_load_dwordx4 %0, %4, %5, %6 offen sc1\n\tbuffer_load_dwordx4 %1, %4, %5, %6 offen offset:1024 sc1\n\t"
                 "buffer_load_dwordx4 %2, %4, %5, %6 offen offset:2048 sc1\n\tbuffer_load_dwordx4 %3, %4, %5, %6 offen offset:3072 sc1"
                 : "+v"(d[0]), "+v"(d[1]), "+v"(d[2]), "+v"(d[3]) : "v"(voff), "s"(srd), "s"(soff) : "memory");
}
constexpr unsigned BIAS = 4096;
#define WAITV_F(n, f) asm volatile("s_waitcnt vmcnt(" #n ")" : "+v"((f)[0]), "+v"((f)[1]), "+v"((f)[2]), "+v"((f)[3]) :: "memory")
DI bf16x8 lds128(lptr p) { return *(const LAS bf16x8*)p; }
DI s16x4 ldstr(lptr p) {
    typedef short v4i16_t __attribute__((ext_vector_type(4)));
    return __builtin_amdgcn_ds_read_tr16_b64_v4i16((LAS v4i16_t*)p);
}
DI bf16x8 cat(s16x4 a, s16x4 b) { return __builtin_shufflevector(a, b, 0, 1, 2, 3, 4, 5, 6, 7); }
DI float swapmax(float v) {
    auto r = __builtin_amdgcn_permlane32_swap(__float_as_uint(v), __float_as_uint(v), false, false);
    return fmaxf(__uint_as_float(r[0]), __uint_as_float(r[1]));
}
DI float swapsum(float v) {
    auto r = __builtin_amdgcn_permlane32_swap(__float_as_uint(v), __float_as_uint(v), false, false);
    return __uint_as_float(r[0]) + __uint_as_float(r[1]);
}
DI rsrc_t mkrsrc(const void* p, unsigned bytes) {
    return __builtin_amdgcn_make_buffer_rsrc((void*)p, (short)0, (int)bytes, 0x00020000);
}
DI void zero16(f32x16& v) {
#pragma unroll
    for (int r = 0; r < 16; ++r) v[r] = 0.f;
}
DI void wait_flags(const GAS u64* p, u64 tag) {
#pragma unroll 1
    for (unsigned it = 0; it < SPIN_CAP; ++it) {
        const u64 v = __hip_atomic_load(p, __ATOMIC_RELAXED, __HIP_MEMORY_SCOPE_AGENT);
        if (__all(v == tag)) break;
        __builtin_amdgcn_s_sleep(1);
    }
    asm volatile("" ::: "memory");
}

DI float max3f(float a, float b, float c) { float r; asm("v_max3_f32 %0, %1, %2, %3" : "=v"(r) : "v"(a), "v"(b), "v"(c)); return r; }
DI float fadd_s(float a, float b) { float r; asm("v_add_f32_e32 %0, %1, %2" : "=v"(r) : "v"(a), "v"(b)); return r; }
DI float fmul_s(float a, float b) { float r; asm("v_mul_f32_e32 %0, %1, %2" : "=v"(r) : "v"(a), "v"(b)); return r; }
DI float fsub_s(float a, float b) { float r; asm("v_sub_f32_e32 %0, %1, %2" : "=v"(r) : "v"(a), "v"(b)); return r; }
DI float exp_s(float a) { float r; asm("v_exp_f32_e32 %0, %1" : "=v"(r) : "v"(a)); return r; }
#define MPAD4(a) asm volatile("s_nop 7\n\ts_nop 7" : "+v"((a)[0]), "+v"((a)[1]), "+v"((a)[2]), "+v"((a)[3]))
#define PIN(x) asm volatile("" : "+v"(x))
#define PIN4(f) asm volatile("" : "+v"((f)[0]), "+v"((f)[1]), "+v"((f)[2]), "+v"((f)[3]) :: "memory")
DI lptr opaque(lptr p) { unsigned a = (unsigned)(uintptr_t)p; asm volatile("" : "+v"(a)); return (lptr)(uintptr_t)a; }
constexpr int LA = 6;
#define SM_THR 8.0f

struct QBase { lptr x0, x1, w; };
DI void qload(int i, bf16x8* wf, bf16x8* xf, const QBase& q) {
    const int s = i >> 2, d = i & 3;
    if (d == 0) xf[s] = lds128(((s & 1) ? q.x1 : q.x0) + 512 * (s >> 1));
    wf[i & 7] = cat(ldstr(q.w + 4096 * s + 512 * d), ldstr(q.w + 4096 * s + 512 * d + 256));
}
DI void qpre(bf16x8* wf, bf16x8* xf, const QBase& q) {
#pragma unroll
    for (int i = 0; i < LA; ++i) qload(i, wf, xf, q);
}
template <bool NEXT> DI void qsteps(f32x16* acc, bf16x8* wf, bf16x8* xf, const QBase& q, const QBase& qn) {
#pragma unroll
    for (int i = 0; i < 16; ++i) {
        SB();
        acc[i & 3] = MFMA32(wf[i & 7], xf[i >> 2], acc[i & 3]);
        PIN(acc[i & 3]);
        SB();
        if (NEXT && i + LA == 16) BARX();
        if (i + LA < 16) qload(i + LA, wf, xf, q); else if (NEXT) qload(i + LA - 16, wf, xf, qn);
    }
    SB();
}
template <bool FIRST = false> DI void oblock(f32x16* acc, const bf16x8* of, lptr wp_) {
    const lptr wp = opaque(wp_);
    bf16x8 wf[8];
    auto load = [&](int i) { wf[i & 7] = cat(ldstr(wp + 5120 * (i / 5) + 512 * (i % 5)), ldstr(wp + 5120 * (i / 5) + 2560 + 512 * (i % 5))); };
#pragma unroll
    for (int i = 0; i < LA; ++i) load(i);
#pragma unroll
    for (int i = 0; i < 20; ++i) {
        SB();
        if (FIRST && i < 5) { f32x16 z; zero16(z); acc[i] = MFMA32(wf[i & 7], of[0], z); }
        else acc[i % 5] = MFMA32(wf[i & 7], of[i / 5], acc[i % 5]);
        PIN(acc[i % 5]);
        SB();
        if (i + LA < 20) load(i + LA);
    }
    SB();
}
DI void aload(int i, bf16x8* fr, lptr vp, lptr kp0, lptr kp1) {
    if (i < 16) fr[i & 7] = cat(ldstr(vp + 4096 * (i >> 2) + 512 * (i & 3)), ldstr(vp + 4096 * (i >> 2) + 2048 + 512 * (i & 3)));
    else { const int s = i & 7; fr[i & 7] = lds128(((s & 1) ? kp1 : kp0) + 512 * (s >> 1) + 8192 * ((i - 16) >> 3)); }
}

__global__ __launch_bounds__(512, 2) void fused_kernel(
    const char* xA, const char* kA, const char* vA, const char* wqA, const char* woA,
    const char* xB, const char* kB, const char* vB, const char* wqB, const char* woB,
    char* ws, char* out)
{
    extern __shared__ __attribute__((aligned(16))) char smem_[];
    const lptr smem = (lptr)smem_;
    const unsigned lds0 = (unsigned)(uintptr_t)smem;
    const int tid = threadIdx.x, lane = tid & 63;
    const int wid = __builtin_amdgcn_readfirstlane(tid >> 6);
    const int bid = blockIdx.x;
    const int xg = bid & 7, ii = bid >> 3;
    const int br = xg >> 2, xq = xg & 3, b = ii >> 2, jj = ii & 3;
    const int h1 = 8 * xq + 2 * jj;
    const char* x = br ? xB : xA;
    const char* kc = br ? kB : kA;
    const char* vc = br ? vB : vA;
    const char* wq = br ? wqB : wqA;
    const char* wo = br ? woB : woA;
    const int qb = wid & 3, grp = wid >> 2, lw = wid & 3;
    const int l31 = lane & 31, h = lane >> 5;
    STAMP(0);
    const char* codebase;
    asm volatile("s_getpc_b64 %0" : "=s"(codebase));

    u64 gen;
    {
        const char* gp = ws + WS_GEN + (br * NB + b) * 256;
        asm volatile("global_load_dwordx2 %0, %1, off sc1" : "=v"(gen) : "v"(gp) : "memory");
    }

    const rsrc_t sk = mkrsrc(kc + (size_t)(b * NH + h1) * SK * DH * 2 - BIAS, 2 * SK * DH * 2 + BIAS);
    const rsrc_t sv = mkrsrc(vc + (size_t)(b * NH + h1) * SK * DH * 2 - BIAS, 2 * SK * DH * 2 + BIAS);
    const rsrc_t sof = mkrsrc(ws + WS_OF + (size_t)((br * NB + b) * NH + 8 * xq) * 32768, 8 * 32768);

    const unsigned dsub = lane >> 5, dr = (lane >> 2) & 7, dsl = lane & 3;
    const unsigned xr0 = 16u * (dsl ^ ((dr >> 2) & 3)), xr1 = 16u * (dsl ^ ((2 + (dr >> 2)) & 3));
    const unsigned vk0 = BIAS + 4096 * lw + dr * 256 + dsub * 64 + xr0, vk2 = BIAS + 4096 * lw + dr * 256 + dsub * 64 + xr1;
    const unsigned vv0 = BIAS + 4096 * lw + dr * 256 + dsub * 64 + 16 * dsl;
    const unsigned ldw = lds0 + 4096 * lw;

    auto issueK = [&](int t, unsigned pair) {
        if ((POL & 1) == ((POL >> 2) & 1) ? (POL & 1) : ((POL >> (2 * br)) & 1)) dma4<1>(ldw + pair, vk0, vk0 - 896, vk2, vk2 - 896, sk, (unsigned)t * 16384u);
        else dma4<0>(ldw + pair, vk0, vk0 - 896, vk2, vk2 - 896, sk, (unsigned)t * 16384u); };
    auto issueV = [&](int t, unsigned pair) {
        if (((POL >> 1) & 1) == ((POL >> 3) & 1) ? ((POL >> 1) & 1) : ((POL >> (2 * br + 1)) & 1)) dma4<1>(ldw + pair + SLOT, vv0, vv0 - 896, vv0, vv0 - 896, sv, (unsigned)t * 16384u);
        else dma4<0>(ldw + pair + SLOT, vv0, vv0 - 896, vv0, vv0 - 896, sv, (unsigned)t * 16384u); };
    auto nextpair = [](unsigned p) { return (p == 4u * SLOT) ? 0u : p + 2u * SLOT; };

    const int G1 = (lane >> 4) & 1, tq = (lane & 15) >> 2, tp = lane & 3;
    const int sig = ((tp & 1) << 1) | (tp >> 1);
    const unsigned wqb = SLOT + 2048 * h + 64 * tq + 32 * G1 + 8 * sig;
    const unsigned rowq = 32 * qb + l31;
    const unsigned xsw = (rowq >> 2) & 3;
    const unsigned xb0 = 1024 * (rowq >> 3) + 64 * (rowq & 7) + 16 * ((0 + h) ^ xsw);
    const unsigned xb1 = 1024 * (rowq >> 3) + 64 * (rowq & 7) + 16 * ((2 + h) ^ xsw);
    const unsigned ksw = (l31 >> 2) & 3;
    const unsigned kb0 = 2048 * (l31 >> 3) + 64 * (l31 & 7) + 16 * ((0 + h) ^ ksw);
    const unsigned kb1 = 2048 * (l31 >> 3) + 64 * (l31 & 7) + 16 * ((2 + h) ^ ksw);
    const unsigned trb = 64 * (4 * h + tq) + 32 * G1 + 8 * tp;
    const unsigned vo16 = lane * 16;

    auto flagp = [&](int bb, int stage, int w) { return (GAS u64*)(ws + WS_FLAG + (size_t)((bb * 3 + stage) * 8 + w) * 64); };
    auto partner = [&](int dj) { return (bid & ~24) | (((jj + dj) & 3) << 3); };
    auto hl1 = [&](int dj) { return 2 * ((jj + dj) & 3); };

    f32x16 acc[5];
    bf16x8 qf[8], pf[4], fr[8];
    f32x16 s0, s1;
#pragma unroll
    for (int d = 0; d < 4; ++d) zero16(acc[d]);

    auto nextstage = [](unsigned st) { return st == 8u * SLOT ? 4u * SLOT : (st == 4u * SLOT ? 6u * SLOT : (st == 6u * SLOT ? 2u * SLOT : 8u * SLOT)); };
    auto qbase = [&](unsigned st) { return QBase{opaque(smem + st + xb0), opaque(smem + st + xb1), opaque(smem + st + wqb)}; };
    if (grp == 0) {
        unsigned st = 8 * SLOT;
        unsigned dummy = 0;
        {
            const unsigned line = lw * 64 + lane;
            const char* p = codebase + (line < CODE_LINES ? line : 0) * 128;
            asm volatile("global_load_dword %0, %1, off" : "+v"(dummy) : "v"(p) : "memory");
        }
        BARX();
        QBase qc = qbase(st);
        qpre(fr, pf, qc);
#pragma unroll 1
        for (int i = 0; i < 10; ++i) {
            st = nextstage(st);
            const QBase qn = qbase(st);
            qsteps<true>(acc, fr, pf, qc, qn);
            qc = qn;
        }
        WAITV(0);
        PIN(dummy);
    } else {
        const rsrc_t sx = mkrsrc(x + (size_t)b * SQ * DM * 2, SQ * DM * 2);
        const rsrc_t sw = mkrsrc(wq + (size_t)h1 * 256 - BIAS, DM * 8192 - h1 * 256 + BIAS);
        const unsigned vx0 = (32 * lw + dr) * 1280 + dsub * 64 + xr0, vx1 = (32 * lw + 8 + dr) * 1280 + dsub * 64 + xr1 - 1024;
        const unsigned vw0 = BIAS + (16 * lw + dr) * 8192 + dsub * 64 + 16 * dsl;
        const int rot = ROT ? b + 3 * jj : 0;
        constexpr unsigned T0 = 6 * SLOT, T1 = 8 * SLOT, P1 = 2 * SLOT, P2 = 4 * SLOT;
        int ci = rot >= 10 ? rot - 10 : rot;
        auto issueQn = [&](int hd, unsigned st) {
            dma4<NT_W>(ldw + st + SLOT, vw0, vw0 - 896, vw0 + 65536 - 2048, vw0 + 65536 - 2944, sw, (unsigned)ci * 524288u + 256 * hd);
            dma4<NT_X>(ldw + st, vx0, vx1, vx0 + 20480 - 2048, vx1 + 20480 - 2048, sx, 128 * ci);
            ci = ci == 9 ? 0 : ci + 1;
        };
        issueQn(0, T1);
        WAITV(0); BARX();
        issueQn(0, P2); issueQn(0, T0);
        WAITV(8);  BARX(); issueQn(0, P1);
        WAITV(8);  BARX(); issueQn(0, T1);
        WAITV(8);  BARX(); issueQn(0, P2);
        WAITV(8);  BARX(); issueQn(0, T0); issueK(0, 0); issueV(0, 0);
        WAITV(16); BARX(); issueQn(0, P1);
        WAITV(16); BARX(); issueQn(0, T1);
        WAITV(8);  BARX(); issueQn(0, P2);
        WAITV(8);  BARX(); issueQn(1, T0);
        WAITV(8);  BARX(); issueK(1, P1); issueV(1, P1);
        BARX(); issueQn(1, T1);
        const QBase q0 = qbase(T0), q1 = qbase(T1);
        WAITV(16); BARX();
        qpre(fr, pf, q0);
        issueK(2, P2);
        qsteps<false>(acc, fr, pf, q0, q0);
        WAITV(4);
        unsigned pv = P2;
#pragma unroll 1
        for (int c = 1; c < 9; c += 2) {
            unsigned pk = nextpair(pv);
            BARX();
            qpre(fr, pf, q1);
            issueQn(1, T0);
            qsteps<false>(acc, fr, pf, q1, q1);
            issueV(c + 1, pv); issueK(c + 2, pk);
            pv = nextpair(pk);
            WAITV(8);
            BARX();
            qpre(fr, pf, q0);
            issueQn(1, T1);
            qsteps<false>(acc, fr, pf, q0, q0);
            issueV(c + 2, pk); issueK(c + 3, pv);
            WAITV(8);
        }
        BARX();
        qpre(fr, pf, q1);
        qsteps<false>(acc, fr, pf, q1, q1);
    }
    STAMP(1);
    asm volatile("" : "+v"(gen));
    const u64 tag = gen + 1;

    __builtin_amdgcn_s_setprio(PPRIO);
    MPAD4(acc);
    {
        float cexp = CEXP; PIN(cexp);
#pragma unroll
        for (int d = 0; d < 4; ++d) {
#pragma unroll
            for (int r = 0; r < 16; ++r) acc[d][r] = fmul_s(acc[d][r], cexp);
            qf[2 * d] = pack8(acc[d], 0); qf[2 * d + 1] = pack8(acc[d], 1); zero16(acc[d]);
        }
    }
    if (grp == 0) BARX();
    float la = 0.f, lb = 0.f, lc = 0.f, ld = 0.f;
    {
        f32x16 negm, p0; zero16(negm); zero16(s1); zero16(p0);
        unsigned pk = grp ? 4 * SLOT : 0;
        lptr kp0 = opaque(smem + pk + kb0), kp1 = opaque(smem + pk + kb1);
#pragma unroll
        for (int i = 16; i < 16 + LA; ++i) aload(i, fr, kp0, kp0, kp1);
        const GAS u64* fp = flagp(partner(1 + (lane % 3)), 0, qb);
        u64 fv = 0;
        const int tw = grp ? 3 : -1;
        float thr = -__builtin_inff(), dmin = -__builtin_inff();
        u32x4 pw[4];
#pragma unroll 1
        for (int t = 0; t < 8; ++t) {
            if (__builtin_expect(t == tw, 0)) asm volatile("global_load_dwordx2 %0, %1, off sc1" : "+v"(fv) : "v"(fp) : "memory");
            float c0, c1;
#pragma unroll
            for (int j = 0; j < 16; ++j) {
                const int i = 16 + j;
                SB();
                if (j < 8) s0 = MFMA32(fr[i & 7], qf[j], j == 0 ? negm : s0); else s1 = MFMA32(fr[i & 7], qf[j - 8], j == 8 ? negm : s1);
                SB();
                if (i + LA < 32) aload(i + LA, fr, kp0, kp0, kp1);
                if (j < 8) {
                    la = fadd_s(la, s1[2 * j]); lb = fadd_s(lb, s1[2 * j + 1]); lc = fadd_s(lc, p0[2 * j]); ld = fadd_s(ld, p0[2 * j + 1]);
                    PIN(la); PIN(lb); PIN(lc); PIN(ld);
                }
                if (j >= 10) {
                    const int e = 2 * (j - 10);
                    p0[e] = exp_s(s0[e]); p0[e + 1] = exp_s(s0[e + 1]); PIN(p0);
                    if (j == 10) { c0 = max3f(s0[0], s0[1], s0[2]); c1 = max3f(s0[3], s0[4], s0[5]); PIN(c0); PIN(c1); }
                    if (j == 11) { c0 = max3f(c0, s0[6], s0[7]); c1 = max3f(c1, s0[8], s0[9]); PIN(c0); PIN(c1); }
                    if (j == 12) { c0 = max3f(c0, s0[10], s0[11]); PIN(c0); }
                    if (j == 13) { c1 = max3f(c1, s0[12], s0[13]); PIN(c1); }
                    if (j == 14) { c0 = max3f(c0, s0[14], s0[15]); PIN(c0); }
                    if (j == 15) { c0 = max3f(c0, c1, c1); PIN(c0); }
                    pw[e >> 3][(e >> 1) & 3] = cvtpk(p0[e], p0[e + 1]); PIN(pw[e >> 3][(e >> 1) & 3]);
                }
            }
            WAITV(0);
            if (__builtin_expect(t == tw, 0)) { PIN(fv); if (!__all(fv == tag)) wait_flags(fp, tag); }
            STAMP(2 + 2 * t);
            BARX();
            const lptr vp = opaque(smem + pk + SLOT + trb);
            pk = nextpair(pk);
            kp0 = opaque(smem + pk + kb0); kp1 = opaque(smem + pk + kb1);
#pragma unroll
            for (int i = 0; i < LA; ++i) aload(i, fr, vp, vp, vp);
            PIN(s1);
            SB();
            {
                float c2 = max3f(s1[0], s1[1], s1[2]), c3 = max3f(s1[3], s1[4], s1[5]);
                c1 = max3f(s1[6], s1[7], s1[8]); c0 = max3f(c0, s1[9], s1[10]);
                c2 = max3f(c2, s1[11], s1[12]); c3 = max3f(c3, s1[13], s1[14]); c1 = max3f(c1, s1[15], c0);
                c0 = max3f(c1, c2, c3);
                auto r_ = __builtin_amdgcn_permlane32_swap(__float_as_uint(c0), __float_as_uint(c0), false, false);
                const float mx = max3f(__uint_as_float(r_[0]), __uint_as_float(r_[1]), __uint_as_float(r_[1]));
                if (__builtin_expect(__any(!(mx <= thr)), 0)) {
                    const float d = fmaxf(mx, dmin);
                    float f = __builtin_amdgcn_exp2f(fminf(-d, 0.f));
                    asm volatile("s_nop 1" : "+v"(f));
#pragma unroll
                    for (int r = 0; r < 16; ++r) { s0[r] = fsub_s(s0[r], d); s1[r] = fsub_s(s1[r], d); negm[r] = fsub_s(negm[r], d); }
                    la = fmul_s(la, f); lb = fmul_s(lb, f); lc = fmul_s(lc, f); ld = fmul_s(ld, f);
#pragma unroll
                    for (int dd = 0; dd < 4; ++dd)
#pragma unroll
                        for (int r = 0; r < 16; ++r) acc[dd][r] = fmul_s(acc[dd][r], f);
#pragma unroll
                    for (int e = 0; e < 12; ++e) p0[e] = __builtin_amdgcn_exp2f(s0[e]);
#pragma unroll
                    for (int e = 0; e < 12; e += 2) pw[e >> 3][(e >> 1) & 3] = cvtpk(p0[e], p0[e + 1]);
                }
                thr = SM_THR; dmin = 0.f;
            }
#pragma unroll
            for (int i = 0; i < 16; ++i) {
                SB();
                acc[i & 3] = MFMA32(fr[i & 7], __builtin_bit_cast(bf16x8, pw[i >> 2]), acc[i & 3]);
                SB();
                constexpr int pairs[16] = {6, 7, 8, 9, 10, 11, -1, 12, 13, 14, 15, -1, -1, -1, -1, -1};
                const int q = pairs[i];
                if (q >= 0) {
                    if (q < 8) { p0[2 * q] = __builtin_amdgcn_exp2f(s0[2 * q]); p0[2 * q + 1] = __builtin_amdgcn_exp2f(s0[2 * q + 1]); PIN(p0); }
                    else { s1[2 * q - 16] = __builtin_amdgcn_exp2f(s1[2 * q - 16]); s1[2 * q - 15] = __builtin_amdgcn_exp2f(s1[2 * q - 15]); PIN(s1); }
                }
                aload(i + LA, fr, vp, kp0, kp1);
                if (q >= 0) {
                    pw[q >> 2][q & 3] = q < 8 ? cvtpk(p0[2 * q], p0[2 * q + 1]) : cvtpk(s1[2 * q - 16], s1[2 * q - 15]);
                    PIN(pw[q >> 2][q & 3]);
                }
            }
            SB();
            STAMP(3 + 2 * t);
        }
#pragma unroll
        for (int j = 0; j < 8; ++j) { la = fadd_s(la, s1[2 * j]); lb = fadd_s(lb, s1[2 * j + 1]); lc = fadd_s(lc, p0[2 * j]); ld = fadd_s(ld, p0[2 * j + 1]); }
    }
    if (grp == 0) { __builtin_amdgcn_s_setprio(0); BARX(); }
    STAMP(18);

    {
        float inv = __builtin_amdgcn_rcpf(swapsum((la + lb) + (lc + ld)));
        asm volatile("s_nop 1" : "+v"(inv));
        MPAD4(acc);
#pragma unroll
        for (int d = 0; d < 4; ++d) {
#pragma unroll
            for (int r = 0; r < 16; ++r) acc[d][r] = fmul_s(acc[d][r], inv);
            qf[2 * d] = pack8(acc[d], 0); qf[2 * d + 1] = pack8(acc[d], 1);
            __builtin_amdgcn_raw_buffer_store_b128(__builtin_bit_cast(u32x4, qf[2 * d]), sof, vo16, (2 * jj + grp) * 32768 + qb * 8192 + d * 2048, 16);
            __builtin_amdgcn_raw_buffer_store_b128(__builtin_bit_cast(u32x4, qf[2 * d + 1]), sof, vo16, (2 * jj + grp) * 32768 + qb * 8192 + d * 2048 + 1024, 16);
            SB();
        }
    }
    const rsrc_t swo = mkrsrc(wo + (size_t)(8 * xq * 128) * 1280 + 320 * jj - BIAS, 1024 * 1280 - 320 * jj + BIAS);
    const unsigned vwA = BIAS + (16 * lw + dr) * 1280 + dsub * 64 + 16 * dsl, vwB = BIAS + (16 * lw + dr) * 1280 + 16 * dsl + (dsub ? 8 * 1280 : 256) - 2048;
    auto issueW = [&](int hl, int half, int buf) {
        const unsigned so = (unsigned)(hl * 128 + 64 * half) * 1280u, ld = lds0 + buf * WT + 5 * lw * 1024;
        dma4<NT_WO>(ld, vwA, vwA - 896, vwB, vwA + 10240 + 64 - 3072, swo, so);
        dma1<NT_WO>(ld + 4096, vwA, swo, so + 10240 + 192);
    };
    const lptr x1 = smem + (lw < 2 ? lw * 8192 : 7 * WT + (lw - 2) * 8192) + vo16;
    if (grp) {
#pragma unroll
        for (int f = 0; f < 8; ++f) *(LAS bf16x8*)(x1 + 1024 * f) = qf[f];
        __builtin_amdgcn_s_setprio(0);
        BAR();
        BARX();
        WAITV(0);
        if (lane == 0) __hip_atomic_store(flagp(bid, 1, qb), tag, __ATOMIC_RELAXED, __HIP_MEMORY_SCOPE_AGENT);
        STAMP(19);
        issueW(hl1(3) + 1, 0, 0); issueW(hl1(3) + 1, 1, 7);
        wait_flags(flagp(partner(1 + (lane % 3)), 1, qb), tag);
        WAITV(0);
        STAMP(21);
        BARX();
    } else {
        bf16x8 cur[4], nxt[4];
#pragma unroll
        for (int k = 0; k < 4; ++k) { cur[k] = qf[k]; nxt[k] = qf[4 + k]; }
        auto issueO = [&](bf16x8* f, int blk) { ld4_sc1(f, vo16, sof, hl1(blk >> 1) * 32768 + qb * 8192 + 4096 * (blk & 1)); };
        issueW(hl1(0), 0, 5);
        BARX();
        issueW(hl1(0), 1, 6); issueW(hl1(1), 0, 7);
        issueV(10, 2 * SLOT); issueK(11, 4 * SLOT);
        BARX();
        issueV(11, 4 * SLOT); issueK(12, 0);
        WAITV(8);
        if (lane == 0) __hip_atomic_store(flagp(bid, 0, qb), tag, __ATOMIC_RELAXED, __HIP_MEMORY_SCOPE_AGENT);
        BARX();
        oblock<true>(acc, cur, smem + 5 * WT + trb);
        issueV(12, 0); issueK(13, 2 * SLOT);
        WAITV(8);
        BARX();
        issueO(cur, 2);
        issueW(hl1(1), 1, 5);
        oblock(acc, nxt, smem + 6 * WT + trb);
        issueO(nxt, 3);
        issueV(13, 2 * SLOT); issueK(14, 4 * SLOT);
        WAITV(17);
        PIN4(cur);
        oblock(acc, cur, smem + 7 * WT + trb);
        issueO(cur, 4);
        WAITV(12);
        BARX();
        unsigned wdummy = 0;
        {
            const int r = 64 + 40 * b + (lane < 40 ? lane : 0);
            const char* p = wo + (size_t)((8 * xq + hl1(1 + (r >> 7)) + 1) * 128 + (r & 127)) * 1280 + 320 * jj + (lw == 3 ? 316 : 128 * lw);
            asm volatile("global_load_dword %0, %1, off" : "+v"(wdummy) : "v"(p) : "memory");
        }
        issueW(hl1(2), 0, 6); issueW(hl1(2), 1, 7);
        PIN4(nxt);
        oblock(acc, nxt, smem + 5 * WT + trb);
        issueO(nxt, 5);
        issueV(14, 4 * SLOT); issueK(15, 0);
        WAITV(17);
        PIN(wdummy);
        BARX();
        issueW(hl1(3), 0, 5);
        PIN4(cur);
        oblock(acc, cur, smem + 6 * WT + trb);
        issueO(cur, 6);
        issueV(15, 0);
        WAITV(13);
        BARX();
        issueW(hl1(3), 1, 6); issueW(hl1(0) + 1, 0, 2); issueW(hl1(0) + 1, 1, 3);
        PIN4(nxt);
        oblock(acc, nxt, smem + 7 * WT + trb);
        issueO(nxt, 7);
        WAITV(4);
        BARX();
        issueW(hl1(1) + 1, 0, 4);
        PIN4(cur);
        oblock(acc, cur, smem + 5 * WT + trb);
        WAITV(5);
        PIN4(nxt);
        oblock(acc, nxt, smem + 6 * WT + trb);
        WAITV(0);
        BARX();
        bf16x8 of2[24];
#pragma unroll
        for (int f = 0; f < 8; ++f) of2[f] = lds128(x1 + 1024 * f);
        BAR();
        issueW(hl1(1) + 1, 1, 5); issueW(hl1(2) + 1, 0, 6); issueW(hl1(2) + 1, 1, 1);
        oblock(acc, of2, smem + 2 * WT + trb);
        oblock(acc, of2 + 4, smem + 3 * WT + trb);
        WAITV(0);
        STAMP(20);
        BARX();
        const unsigned ob = qb * 8192 + 32768;
        auto ldo = [&](int k0) {
#pragma unroll
            for (int k = k0; k < k0 + 4; ++k)
                of2[k] = __builtin_bit_cast(bf16x8, __builtin_amdgcn_raw_buffer_load_b128(sof, vo16, (unsigned)hl1(1 + (k >> 3)) * 32768 + ob + (k & 7) * 1024, 16));
        };
        ldo(0); ldo(4); SB();
        oblock(acc, of2, smem + 4 * WT + trb);       ldo(8);  ldo(12); SB();
        oblock(acc, of2 + 4, smem + 5 * WT + trb);   ldo(16); SB();
        oblock(acc, of2 + 8, smem + 6 * WT + trb);   ldo(20); SB();
        oblock(acc, of2 + 12, smem + 1 * WT + trb);
        oblock(acc, of2 + 16, smem + 0 * WT + trb);
        oblock(acc, of2 + 20, smem + 7 * WT + trb);
        STAMP(22);
        const rsrc_t spb = mkrsrc(ws + WS_PB, 256 * PB_WG);
        f32x4 mine[5];
#define UNIT(u, e) acc[(u) >> 2][4 * ((u) & 3) + (e)]
#define UPK(u) cvtpk(UNIT(u, 0), UNIT(u, 1)), cvtpk(UNIT(u, 2), UNIT(u, 3))
#pragma unroll
        for (int g = 0; g < 4; ++g) {
            if (g == xq) {
#pragma unroll
                for (int n = 0; n < 5; ++n) mine[n] = (f32x4){UNIT(5 * g + n, 0), UNIT(5 * g + n, 1), UNIT(5 * g + n, 2), UNIT(5 * g + n, 3)};
            } else {
                const unsigned so = (unsigned)((bid & ~3) | g) * PB_WG + (xq * 4 + qb) * 5120;
                const u32x4 w01 = {UPK(5 * g), UPK(5 * g + 1)}, w23 = {UPK(5 * g + 2), UPK(5 * g + 3)};
                const u32x2 w4 = {UPK(5 * g + 4)};
                __builtin_amdgcn_raw_buffer_store_b128(w01, spb, vo16, so, 16);
                __builtin_amdgcn_raw_buffer_store_b128(w23, spb, vo16, so + 1024, 16);
                __builtin_amdgcn_raw_buffer_store_b64(w4, spb, lane * 8, so + 2048, 16);
            }
        }
        WAITV(0);
        if (lane == 0) __hip_atomic_store(flagp(bid, 2, qb), tag, __ATOMIC_RELAXED, __HIP_MEMORY_SCOPE_AGENT);
        STAMP(23);
        wait_flags(flagp((bid & ~3) | ((xq + 1 + (lane % 3)) & 3), 2, qb), tag);
        STAMP(24);
        {
            const rsrc_t sout = mkrsrc(out + ((size_t)b * SQ * (2 * DM) + br * DM + 160 * jj + 40 * xq) * 2, SQ * 2 * DM * 2 - (br * DM + 160 * jj + 40 * xq) * 2);
            const unsigned vop = rowq * (4 * DM) + 8 * h;
            u32x4 a01[3], a23[3]; u32x2 a4[3];
#pragma unroll
            for (int s = 0; s < 3; ++s) {
                const unsigned so = (unsigned)bid * PB_WG + (((xq + 1 + s) & 3) * 4 + qb) * 5120;
                a01[s] = __builtin_amdgcn_raw_buffer_load_b128(spb, vo16, so, 16);
                a23[s] = __builtin_amdgcn_raw_buffer_load_b128(spb, vo16, so + 1024, 16);
                a4[s] = __builtin_amdgcn_raw_buffer_load_b64(spb, lane * 8, so + 2048, 16);
            }
            auto up = [](unsigned lo, unsigned hi) { return (f32x4){__uint_as_float(lo << 16), __uint_as_float(lo & 0xffff0000u), __uint_as_float(hi << 16), __uint_as_float(hi & 0xffff0000u)}; };
#pragma unroll
            for (int s = 0; s < 3; ++s) {
                mine[0] += up(a01[s][0], a01[s][1]); mine[1] += up(a01[s][2], a01[s][3]);
                mine[2] += up(a23[s][0], a23[s][1]); mine[3] += up(a23[s][2], a23[s][3]); mine[4] += up(a4[s][0], a4[s][1]);
            }
#pragma unroll
            for (int n = 0; n < 5; ++n) __builtin_amdgcn_raw_buffer_store_b64((u32x2){cvtpk(mine[n][0], mine[n][1]), cvtpk(mine[n][2], mine[n][3])}, sout, vop, 16 * n, OUT_AUX);
        }
    }
    {
        if (xq == 0 && jj == 0 && wid == 0 && lane == 0)
            __hip_atomic_store((GAS u64*)(ws + WS_GEN + (br * NB + b) * 256), tag, __ATOMIC_RELAXED, __HIP_MEMORY_SCOPE_AGENT);
        STAMP(25);
    }
}

extern "C" void kernel_launch(void* const* d_in, const int* in_sizes, int n_in, void* d_out, int out_size,
                              void* d_ws, size_t ws_size, hipStream_t stream) {
    (void)hipFuncSetAttribute((const void*)fused_kernel, hipFuncAttributeMaxDynamicSharedMemorySize, LDS_BYTES);
    hipLaunchKernelGGL(fused_kernel, dim3(256), dim3(512), LDS_BYTES, stream,
                       (const char*)d_in[0], (const char*)d_in[1], (const char*)d_in[2], (const char*)d_in[3], (const char*)d_in[4],
                       (const char*)d_in[5], (const char*)d_in[6], (const char*)d_in[7], (const char*)d_in[8], (const char*)d_in[9],
                       (char*)d_ws + WS_SHIFT, (char*)d_out);
}
```

```cpp
#define PROBE 1
#define PROBE_G 0
#include <hip/hip_runtime.h>
#include <hip/hip_bf16.h>
#include <stdint.h>
#include <type_traits>

typedef short bf16x8 __attribute__((ext_vector_type(8)));
typedef short s16x4 __attribute__((ext_vector_type(4)));
typedef float f32x16 __attribute__((ext_vector_type(16)));
typedef float f32x4 __attribute__((ext_vector_type(4)));
typedef float f32x2 __attribute__((ext_vector_type(2)));
typedef __bf16 bf16x2_t __attribute__((ext_vector_type(2)));
typedef unsigned u32x4 __attribute__((ext_vector_type(4)));
typedef unsigned u32x2 __attribute__((ext_vector_type(2)));
typedef unsigned long long u64;
#define LAS __attribute__((address_space(3)))
#define GAS __attribute__((address_space(1)))
#define DI __device__ __forceinline__
typedef LAS char* lptr;
typedef __amdgpu_buffer_rsrc_t rsrc_t;

constexpr int NB = 8, SQ = 128, DM = 640, NH = 32, SK = 512, DH = 128;
constexpr float CEXP = 0.08838834764831845f * 1.4426950408889634f;
constexpr int SLOT = 16384;
constexpr int LDS_T = 6 * SLOT;
constexpr int WT = 20480;
constexpr int LDS_BYTES = 10 * SLOT;
constexpr size_t WS_GEN = 0, WS_FLAG = 8192, WS_OF = 1 << 20;
constexpr size_t OF_BYTES = (size_t)2 * NB * NH * 32768;
constexpr size_t WS_PB = WS_OF + OF_BYTES;
constexpr unsigned PB_WG = 4 * 8 * 3 * 1024;
#define NT_W 0
#define NT_X 0
#define NT_WO 0
#define PPRIO 2
#define PBF16 1
#define WS_SHIFT 0
#define POL 15
#define PRETOUCH 1
constexpr unsigned CODE_LINES = 180;
#define WOWARM 1
#define ROT 1
#define OUT_AUX 0
constexpr unsigned SPIN_CAP = 1u << 16;

#define MFMA32(a, b, c) __builtin_amdgcn_mfma_f32_32x32x16_bf16((a), (b), (c), 0, 0, 0)
#define WAITV(n) asm volatile("s_waitcnt vmcnt(" #n ")" ::: "memory")
#define SB() __builtin_amdgcn_sched_barrier(0)
#define BARX() do { SB(); asm volatile("s_barrier" ::: "memory"); SB(); } while (0)
#define BAR() do { SB(); asm volatile("s_waitcnt lgkmcnt(0)\n\ts_barrier" ::: "memory"); SB(); } while (0)

#define STAMP(idx) do { if ((idx) == 0) pt0 = __builtin_amdgcn_s_memrealtime(); if ((idx) == PROBE) ptx = __builtin_amdgcn_s_memrealtime(); } while (0)

DI unsigned cvtpk(float lo, float hi) {
    f32x2 v = {lo, hi};
    bf16x2_t b = __builtin_convertvector(v, bf16x2_t);
    return __builtin_bit_cast(unsigned, b);
}
DI bf16x8 pack8(const f32x16& x, int s) {
    u32x4 w = {cvtpk(x[8 * s + 0], x[8 * s + 1]), cvtpk(x[8 * s + 2], x[8 * s + 3]),
               cvtpk(x[8 * s + 4], x[8 * s + 5]), cvtpk(x[8 * s + 6], x[8 * s + 7])};
    return __builtin_bit_cast(bf16x8, w);
}
template <int NT> DI void dma4(unsigned lds, unsigned v0, unsigned v1, unsigned v2, unsigned v3, rsrc_t srd, unsigned soff) {
    if (NT) asm volatile("s_mov_b32 m0, %0\n\ts_nop 3\n\tbuffer_load_dwordx4 %1, %5, %6 offen nt lds\n\tbuffer_load_dwordx4 %2, %5, %6 offen offset:1024 nt lds\n\t"
                         "buffer_load_dwordx4 %3, %5, %6 offen offset:2048 nt lds\n\tbuffer_load_dwordx4 %4, %5, %6 offen offset:3072 nt lds"
                         :: "s"(lds), "v"(v0), "v"(v1), "v"(v2), "v"(v3), "s"(srd), "s"(soff) : "memory");
    else asm volatile("s_mov_b32 m0, %0\n\ts_nop 3\n\tbuffer_load_dwordx4 %1, %5, %6 offen lds\n\tbuffer_load_dwordx4 %2, %5, %6 offen offset:1024 lds\n\t"
                      "buffer_load_dwordx4 %3, %5, %6 offen offset:2048 lds\n\tbuffer_load_dwordx4 %4, %5, %6 offen offset:3072 lds"
                      :: "s"(lds), "v"(v0), "v"(v1), "v"(v2), "v"(v3), "s"(srd), "s"(soff) : "memory");
}
template <int NT> DI void dma1(unsigned lds, unsigned voff, rsrc_t srd, unsigned soff) {
    if (NT) asm volatile("s_mov_b32 m0, %0\n\ts_nop 3\n\tbuffer_load_dwordx4 %1, %2, %3 offen nt lds" :: "s"(lds), "v"(voff), "s"(srd), "s"(soff) : "memory");
    else asm volatile("s_mov_b32 m0, %0\n\ts_nop 3\n\tbuffer_load_dwordx4 %1, %2, %3 offen lds" :: "s"(lds), "v"(voff), "s"(srd), "s"(soff) : "memory");
}
DI void ld4_sc1(bf16x8* d, unsigned voff, rsrc_t srd, unsigned soff) {
    asm volatile("s_nop 4\n\tbuffer_load_dwordx4 %0, %4, %5, %6 offen sc1\n\tbuffer_load_dwordx4 %1, %4, %5, %6 offen offset:1024 sc1\n\t"
                 "buffer_load_dwordx4 %2, %4, %5, %6 offen offset:2048 sc1\n\tbuffer_load_dwordx4 %3, %4, %5, %6 offen offset:3072 sc1"
                 : "+v"(d[0]), "+v"(d[1]), "+v"(d[2]), "+v"(d[3]) : "v"(voff), "s"(srd), "s"(soff) : "memory");
}
constexpr unsigned BIAS = 4096;
#define WAITV_F(n, f) asm volatile("s_waitcnt vmcnt(" #n ")" : "+v"((f)[0]), "+v"((f)[1]), "+v"((f)[2]), "+v"((f)[3]) :: "memory")
DI bf16x8 lds128(lptr p) { return *(const LAS bf16x8*)p; }
DI s16x4 ldstr(lptr p) {
    typedef short v4i16_t __attribute__((ext_vector_type(4)));
    return __builtin_amdgcn_ds_read_tr16_b64_v4i16((LAS v4i16_t*)p);
}
DI bf16x8 cat(s16x4 a, s16x4 b) { return __builtin_shufflevector(a, b, 0, 1, 2, 3, 4, 5, 6, 7); }
DI float swapmax(float v) {
    auto r = __builtin_amdgcn_permlane32_swap(__float_as_uint(v), __float_as_uint(v), false, false);
    return fmaxf(__uint_as_float(r[0]), __uint_as_float(r[1]));
}
DI float swapsum(float v) {
    auto r = __builtin_amdgcn_permlane32_swap(__float_as_uint(v), __float_as_uint(v), false, false);
    return __uint_as_float(r[0]) + __uint_as_float(r[1]);
}
DI rsrc_t mkrsrc(const void* p, unsigned bytes) {
    return __builtin_amdgcn_make_buffer_rsrc((void*)p, (short)0, (int)bytes, 0x00020000);
}
DI void zero16(f32x16& v) {
#pragma unroll
    for (int r = 0; r < 16; ++r) v[r] = 0.f;
}
DI void wait_flags(const GAS u64* p, u64 tag) {
#pragma unroll 1
    for (unsigned it = 0; it < SPIN_CAP; ++it) {
        const u64 v = __hip_atomic_load(p, __ATOMIC_RELAXED, __HIP_MEMORY_SCOPE_AGENT);
        if (__all(v == tag)) break;
        __builtin_amdgcn_s_sleep(1);
    }
    asm volatile("" ::: "memory");
}

DI float max3f(float a, float b, float c) { float r; asm("v_max3_f32 %0, %1, %2, %3" : "=v"(r) : "v"(a), "v"(b), "v"(c)); return r; }
DI float fadd_s(float a, float b) { float r; asm("v_add_f32_e32 %0, %1, %2" : "=v"(r) : "v"(a), "v"(b)); return r; }
DI float fmul_s(float a, float b) { float r; asm("v_mul_f32_e32 %0, %1, %2" : "=v"(r) : "v"(a), "v"(b)); return r; }
DI float fsub_s(float a, float b) { float r; asm("v_sub_f32_e32 %0, %1, %2" : "=v"(r) : "v"(a), "v"(b)); return r; }
DI float exp_s(float a) { float r; asm("v_exp_f32_e32 %0, %1" : "=v"(r) : "v"(a)); return r; }
#define MPAD4(a) asm volatile("s_nop 7\n\ts_nop 7" : "+v"((a)[0]), "+v"((a)[1]), "+v"((a)[2]), "+v"((a)[3]))
#define PIN(x) asm volatile("" : "+v"(x))
#define PIN4(f) asm volatile("" : "+v"((f)[0]), "+v"((f)[1]), "+v"((f)[2]), "+v"((f)[3]) :: "memory")
DI lptr opaque(lptr p) { unsigned a = (unsigned)(uintptr_t)p; asm volatile("" : "+v"(a)); return (lptr)(uintptr_t)a; }
constexpr int LA = 6;
#define SM_THR 8.0f

struct QBase { lptr x0, x1, w; };
DI void qload(int i, bf16x8* wf, bf16x8* xf, const QBase& q) {
    const int s = i >> 2, d = i & 3;
    if (d == 0) xf[s] = lds128(((s & 1) ? q.x1 : q.x0) + 512 * (s >> 1));
    wf[i & 7] = cat(ldstr(q.w + 4096 * s + 512 * d), ldstr(q.w + 4096 * s + 512 * d + 256));
}
DI void qpre(bf16x8* wf, bf16x8* xf, const QBase& q) {
#pragma unroll
    for (int i = 0; i < LA; ++i) qload(i, wf, xf, q);
}
template <bool NEXT> DI void qsteps(f32x16* acc, bf16x8* wf, bf16x8* xf, const QBase& q, const QBase& qn) {
#pragma unroll
    for (int i = 0; i < 16; ++i) {
        SB();
        acc[i & 3] = MFMA32(wf[i & 7], xf[i >> 2], acc[i & 3]);
        PIN(acc[i & 3]);
        SB();
        if (NEXT && i + LA == 16) BARX();
        if (i + LA < 16) qload(i + LA, wf, xf, q); else if (NEXT) qload(i + LA - 16, wf, xf, qn);
    }
    SB();
}
template <bool FIRST = false> DI void oblock(f32x16* acc, const bf16x8* of, lptr wp_) {
    const lptr wp = opaque(wp_);
    bf16x8 wf[8];
    auto load = [&](int i) { wf[i & 7] = cat(ldstr(wp + 5120 * (i / 5) + 512 * (i % 5)), ldstr(wp + 5120 * (i / 5) + 2560 + 512 * (i % 5))); };
#pragma unroll
    for (int i = 0; i < LA; ++i) load(i);
#pragma unroll
    for (int i = 0; i < 20; ++i) {
        SB();
        if (FIRST && i < 5) { f32x16 z; zero16(z); acc[i] = MFMA32(wf[i & 7], of[0], z); }
        else acc[i % 5] = MFMA32(wf[i & 7], of[i / 5], acc[i % 5]);
        PIN(acc[i % 5]);
        SB();
        if (i + LA < 20) load(i + LA);
    }
    SB();
}
DI void aload(int i, bf16x8* fr, lptr vp, lptr kp0, lptr kp1) {
    if (i < 16) fr[i & 7] = cat(ldstr(vp + 4096 * (i >> 2) + 512 * (i & 3)), ldstr(vp + 4096 * (i >> 2) + 2048 + 512 * (i & 3)));
    else { const int s = i & 7; fr[i & 7] = lds128(((s & 1) ? kp1 : kp0) + 512 * (s >> 1) + 8192 * ((i - 16) >> 3)); }
}

__global__ __launch_bounds__(512, 2) void fused_kernel(
    const char* xA, const char* kA, const char* vA, const char* wqA, const char* woA,
    const char* xB, const char* kB, const char* vB, const char* wqB, const char* woB,
    char* ws, char* out)
{
    extern __shared__ __attribute__((aligned(16))) char smem_[];
    const lptr smem = (lptr)smem_;
    const unsigned lds0 = (unsigned)(uintptr_t)smem;
    const int tid = threadIdx.x, lane = tid & 63;
    const int wid = __builtin_amdgcn_readfirstlane(tid >> 6);
    const int bid = blockIdx.x;
    const int xg = bid & 7, ii = bid >> 3;
    const int br = xg >> 2, xq = xg & 3, b = ii >> 2, jj = ii & 3;
    const int h1 = 8 * xq + 2 * jj;
    const char* x = br ? xB : xA;
    const char* kc = br ? kB : kA;
    const char* vc = br ? vB : vA;
    const char* wq = br ? wqB : wqA;
    const char* wo = br ? woB : woA;
    const int qb = wid & 3, grp = wid >> 2, lw = wid & 3;
    const int l31 = lane & 31, h = lane >> 5;
    unsigned long long pt0 = 0, ptx = 0;
    STAMP(0);
    const char* codebase;
    asm volatile("s_getpc_b64 %0" : "=s"(codebase));

    u64 gen;
    {
        const char* gp = ws + WS_GEN + (br * NB + b) * 256;
        asm volatile("global_load_dwordx2 %0, %1, off sc1" : "=v"(gen) : "v"(gp) : "memory");
    }

    const rsrc_t sk = mkrsrc(kc + (size_t)(b * NH + h1) * SK * DH * 2 - BIAS, 2 * SK * DH * 2 + BIAS);
    const rsrc_t sv = mkrsrc(vc + (size_t)(b * NH + h1) * SK * DH * 2 - BIAS, 2 * SK * DH * 2 + BIAS);
    const rsrc_t sof = mkrsrc(ws + WS_OF + (size_t)((br * NB + b) * NH + 8 * xq) * 32768, 8 * 32768);

    const unsigned dsub = lane >> 5, dr = (lane >> 2) & 7, dsl = lane & 3;
    const unsigned xr0 = 16u * (dsl ^ ((dr >> 2) & 3)), xr1 = 16u * (dsl ^ ((2 + (dr >> 2)) & 3));
    const unsigned vk0 = BIAS + 4096 * lw + dr * 256 + dsub * 64 + xr0, vk2 = BIAS + 4096 * lw + dr * 256 + dsub * 64 + xr1;
    const unsigned vv0 = BIAS + 4096 * lw + dr * 256 + dsub * 64 + 16 * dsl;
    const unsigned ldw = lds0 + 4096 * lw;

    auto issueK = [&](int t, unsigned pair) {
        if ((POL & 1) == ((POL >> 2) & 1) ? (POL & 1) : ((POL >> (2 * br)) & 1)) dma4<1>(ldw + pair, vk0, vk0 - 896, vk2, vk2 - 896, sk, (unsigned)t * 16384u);
        else dma4<0>(ldw + pair, vk0, vk0 - 896, vk2, vk2 - 896, sk, (unsigned)t * 16384u); };
    auto issueV = [&](int t, unsigned pair) {
        if (((POL >> 1) & 1) == ((POL >> 3) & 1) ? ((POL >> 1) & 1) : ((POL >> (2 * br + 1)) & 1)) dma4<1>(ldw + pair + SLOT, vv0, vv0 - 896, vv0, vv0 - 896, sv, (unsigned)t * 16384u);
        else dma4<0>(ldw + pair + SLOT, vv0, vv0 - 896, vv0, vv0 - 896, sv, (unsigned)t * 16384u); };
    auto nextpair = [](unsigned p) { return (p == 4u * SLOT) ? 0u : p + 2u * SLOT; };

    const int G1 = (lane >> 4) & 1, tq = (lane & 15) >> 2, tp = lane & 3;
    const int sig = ((tp & 1) << 1) | (tp >> 1);
    const unsigned wqb = SLOT + 2048 * h + 64 * tq + 32 * G1 + 8 * sig;
    const unsigned rowq = 32 * qb + l31;
    const unsigned xsw = (rowq >> 2) & 3;
    const unsigned xb0 = 1024 * (rowq >> 3) + 64 * (rowq & 7) + 16 * ((0 + h) ^ xsw);
    const unsigned xb1 = 1024 * (rowq >> 3) + 64 * (rowq & 7) + 16 * ((2 + h) ^ xsw);
    const unsigned ksw = (l31 >> 2) & 3;
    const unsigned kb0 = 2048 * (l31 >> 3) + 64 * (l31 & 7) + 16 * ((0 + h) ^ ksw);
    const unsigned kb1 = 2048 * (l31 >> 3) + 64 * (l31 & 7) + 16 * ((2 + h) ^ ksw);
    const unsigned trb = 64 * (4 * h + tq) + 32 * G1 + 8 * tp;
    const unsigned vo16 = lane * 16;

    auto flagp = [&](int bb, int stage, int w) { return (GAS u64*)(ws + WS_FLAG + (size_t)((bb * 3 + stage) * 8 + w) * 64); };
    auto partner = [&](int dj) { return (bid & ~24) | (((jj + dj) & 3) << 3); };
    auto hl1 = [&](int dj) { return 2 * ((jj + dj) & 3); };

    f32x16 acc[5];
    bf16x8 qf[8], pf[4], fr[8];
    f32x16 s0, s1;
#pragma unroll
    for (int d = 0; d < 4; ++d) zero16(acc[d]);

    auto nextstage = [](unsigned st) { return st == 8u * SLOT ? 4u * SLOT : (st == 4u * SLOT ? 6u * SLOT : (st == 6u * SLOT ? 2u * SLOT : 8u * SLOT)); };
    auto qbase = [&](unsigned st) { return QBase{opaque(smem + st + xb0), opaque(smem + st + xb1), opaque(smem + st + wqb)}; };
    if (grp == 0) {
        unsigned st = 8 * SLOT;
        unsigned dummy = 0;
        {
            const unsigned line = lw * 64 + lane;
            const char* p = codebase + (line < CODE_LINES ? line : 0) * 128;
            asm volatile("global_load_dword %0, %1, off" : "+v"(dummy) : "v"(p) : "memory");
        }
        BARX();
        QBase qc = qbase(st);
        qpre(fr, pf, qc);
#pragma unroll 1
        for (int i = 0; i < 10; ++i) {
            st = nextstage(st);
            const QBase qn = qbase(st);
            qsteps<true>(acc, fr, pf, qc, qn);
            qc = qn;
        }
        WAITV(0);
        PIN(dummy);
    } else {
        const rsrc_t sx = mkrsrc(x + (size_t)b * SQ * DM * 2, SQ * DM * 2);
        const rsrc_t sw = mkrsrc(wq + (size_t)h1 * 256 - BIAS, DM * 8192 - h1 * 256 + BIAS);
        const unsigned vx0 = (32 * lw + dr) * 1280 + dsub * 64 + xr0, vx1 = (32 * lw + 8 + dr) * 1280 + dsub * 64 + xr1 - 1024;
        const unsigned vw0 = BIAS + (16 * lw + dr) * 8192 + dsub * 64 + 16 * dsl;
        const int rot = ROT ? b + 3 * jj : 0;
        constexpr unsigned T0 = 6 * SLOT, T1 = 8 * SLOT, P1 = 2 * SLOT, P2 = 4 * SLOT;
        int ci = rot >= 10 ? rot - 10 : rot;
        auto issueQn = [&](int hd, unsigned st) {
            dma4<NT_W>(ldw + st + SLOT, vw0, vw0 - 896, vw0 + 65536 - 2048, vw0 + 65536 - 2944, sw, (unsigned)ci * 524288u + 256 * hd);
            dma4<NT_X>(ldw + st, vx0, vx1, vx0 + 20480 - 2048, vx1 + 20480 - 2048, sx, 128 * ci);
            ci = ci == 9 ? 0 : ci + 1;
        };
        issueQn(0, T1);
        WAITV(0); BARX();
        issueQn(0, P2); issueQn(0, T0);
        WAITV(8);  BARX(); issueQn(0, P1);
        WAITV(8);  BARX(); issueQn(0, T1);
        WAITV(8);  BARX(); issueQn(0, P2);
        WAITV(8);  BARX(); issueQn(0, T0); issueK(0, 0); issueV(0, 0);
        WAITV(16); BARX(); issueQn(0, P1);
        WAITV(16); BARX(); issueQn(0, T1);
        WAITV(8);  BARX(); issueQn(0, P2);
        WAITV(8);  BARX(); issueQn(1, T0);
        WAITV(8);  BARX(); issueK(1, P1); issueV(1, P1);
        BARX(); issueQn(1, T1);
        const QBase q0 = qbase(T0), q1 = qbase(T1);
        WAITV(16); BARX();
        qpre(fr, pf, q0);
        issueK(2, P2);
        qsteps<false>(acc, fr, pf, q0, q0);
        WAITV(4);
        unsigned pv = P2;
#pragma unroll 1
        for (int c = 1; c < 9; c += 2) {
            unsigned pk = nextpair(pv);
            BARX();
            qpre(fr, pf, q1);
            issueQn(1, T0);
            qsteps<false>(acc, fr, pf, q1, q1);
            issueV(c + 1, pv); issueK(c + 2, pk);
            pv = nextpair(pk);
            WAITV(8);
            BARX();
            qpre(fr, pf, q0);
            issueQn(1, T1);
            qsteps<false>(acc, fr, pf, q0, q0);
            issueV(c + 2, pk); issueK(c + 3, pv);
            WAITV(8);
        }
        BARX();
        qpre(fr, pf, q1);
        qsteps<false>(acc, fr, pf, q1, q1);
    }
    STAMP(1);
    asm volatile("" : "+v"(gen));
    const u64 tag = gen + 1;

    __builtin_amdgcn_s_setprio(PPRIO);
    MPAD4(acc);
    {
        float cexp = CEXP; PIN(cexp);
#pragma unroll
        for (int d = 0; d < 4; ++d) {
#pragma unroll
            for (int r = 0; r < 16; ++r) acc[d][r] = fmul_s(acc[d][r], cexp);
            qf[2 * d] = pack8(acc[d], 0); qf[2 * d + 1] = pack8(acc[d], 1); zero16(acc[d]);
        }
    }
    if (grp == 0) BARX();
    float la = 0.f, lb = 0.f, lc = 0.f, ld = 0.f;
    {
        f32x16 negm, p0; zero16(negm); zero16(s1); zero16(p0);
        unsigned pk = grp ? 4 * SLOT : 0;
        lptr kp0 = opaque(smem + pk + kb0), kp1 = opaque(smem + pk + kb1);
#pragma unroll
        for (int i = 16; i < 16 + LA; ++i) aload(i, fr, kp0, kp0, kp1);
        const GAS u64* fp = flagp(partner(1 + (lane % 3)), 0, qb);
        u64 fv = 0;
        const int tw = grp ? 3 : -1;
        float thr = -__builtin_inff(), dmin = -__builtin_inff();
        u32x4 pw[4];
#pragma unroll 1
        for (int t = 0; t < 8; ++t) {
            if (__builtin_expect(t == tw, 0)) asm volatile("global_load_dwordx2 %0, %1, off sc1" : "+v"(fv) : "v"(fp) : "memory");
            float c0, c1;
#pragma unroll
            for (int j = 0; j < 16; ++j) {
                const int i = 16 + j;
                SB();
                if (j < 8) s0 = MFMA32(fr[i & 7], qf[j], j == 0 ? negm : s0); else s1 = MFMA32(fr[i & 7], qf[j - 8], j == 8 ? negm : s1);
                SB();
                if (i + LA < 32) aload(i + LA, fr, kp0, kp0, kp1);
                if (j < 8) {
                    la = fadd_s(la, s1[2 * j]); lb = fadd_s(lb, s1[2 * j + 1]); lc = fadd_s(lc, p0[2 * j]); ld = fadd_s(ld, p0[2 * j + 1]);
                    PIN(la); PIN(lb); PIN(lc); PIN(ld);
                }
                if (j >= 10) {
                    const int e = 2 * (j - 10);
                    p0[e] = exp_s(s0[e]); p0[e + 1] = exp_s(s0[e + 1]); PIN(p0);
                    if (j == 10) { c0 = max3f(s0[0], s0[1], s0[2]); c1 = max3f(s0[3], s0[4], s0[5]); PIN(c0); PIN(c1); }
                    if (j == 11) { c0 = max3f(c0, s0[6], s0[7]); c1 = max3f(c1, s0[8], s0[9]); PIN(c0); PIN(c1); }
                    if (j == 12) { c0 = max3f(c0, s0[10], s0[11]); PIN(c0); }
                    if (j == 13) { c1 = max3f(c1, s0[12], s0[13]); PIN(c1); }
                    if (j == 14) { c0 = max3f(c0, s0[14], s0[15]); PIN(c0); }
                    if (j == 15) { c0 = max3f(c0, c1, c1); PIN(c0); }
                    pw[e >> 3][(e >> 1) & 3] = cvtpk(p0[e], p0[e + 1]); PIN(pw[e >> 3][(e >> 1) & 3]);
                }
            }
            WAITV(0);
            if (__builtin_expect(t == tw, 0)) { PIN(fv); if (!__all(fv == tag)) wait_flags(fp, tag); }
            STAMP(2 + 2 * t);
            BARX();
            const lptr vp = opaque(smem + pk + SLOT + trb);
            pk = nextpair(pk);
            kp0 = opaque(smem + pk + kb0); kp1 = opaque(smem + pk + kb1);
#pragma unroll
            for (int i = 0; i < LA; ++i) aload(i, fr, vp, vp, vp);
            PIN(s1);
            SB();
            {
                float c2 = max3f(s1[0], s1[1], s1[2]), c3 = max3f(s1[3], s1[4], s1[5]);
                c1 = max3f(s1[6], s1[7], s1[8]); c0 = max3f(c0, s1[9], s1[10]);
                c2 = max3f(c2, s1[11], s1[12]); c3 = max3f(c3, s1[13], s1[14]); c1 = max3f(c1, s1[15], c0);
                c0 = max3f(c1, c2, c3);
                auto r_ = __builtin_amdgcn_permlane32_swap(__float_as_uint(c0), __float_as_uint(c0), false, false);
                const float mx = max3f(__uint_as_float(r_[0]), __uint_as_float(r_[1]), __uint_as_float(r_[1]));
                if (__builtin_expect(__any(!(mx <= thr)), 0)) {
                    const float d = fmaxf(mx, dmin);
                    float f = __builtin_amdgcn_exp2f(fminf(-d, 0.f));
                    asm volatile("s_nop 1" : "+v"(f));
#pragma unroll
                    for (int r = 0; r < 16; ++r) { s0[r] = fsub_s(s0[r], d); s1[r] = fsub_s(s1[r], d); negm[r] = fsub_s(negm[r], d); }
                    la = fmul_s(la, f); lb = fmul_s(lb, f); lc = fmul_s(lc, f); ld = fmul_s(ld, f);
#pragma unroll
                    for (int dd = 0; dd < 4; ++dd)
#pragma unroll
                        for (int r = 0; r < 16; ++r) acc[dd][r] = fmul_s(acc[dd][r], f);
#pragma unroll
                    for (int e = 0; e < 12; ++e) p0[e] = __builtin_amdgcn_exp2f(s0[e]);
#pragma unroll
                    for (int e = 0; e < 12; e += 2) pw[e >> 3][(e >> 1) & 3] = cvtpk(p0[e], p0[e + 1]);
                }
                thr = SM_THR; dmin = 0.f;
            }
#pragma unroll
            for (int i = 0; i < 16; ++i) {
                SB();
                acc[i & 3] = MFMA32(fr[i & 7], __builtin_bit_cast(bf16x8, pw[i >> 2]), acc[i & 3]);
                SB();
                constexpr int pairs[16] = {6, 7, 8, 9, 10, 11, -1, 12, 13, 14, 15, -1, -1, -1, -1, -1};
                const int q = pairs[i];
                if (q >= 0) {
                    if (q < 8) { p0[2 * q] = __builtin_amdgcn_exp2f(s0[2 * q]); p0[2 * q + 1] = __builtin_amdgcn_exp2f(s0[2 * q + 1]); PIN(p0); }
                    else { s1[2 * q - 16] = __builtin_amdgcn_exp2f(s1[2 * q - 16]); s1[2 * q - 15] = __builtin_amdgcn_exp2f(s1[2 * q - 15]); PIN(s1); }
                }
                aload(i + LA, fr, vp, kp0, kp1);
                if (q >= 0) {
                    pw[q >> 2][q & 3] = q < 8 ? cvtpk(p0[2 * q], p0[2 * q + 1]) : cvtpk(s1[2 * q - 16], s1[2 * q - 15]);
                    PIN(pw[q >> 2][q & 3]);
                }
            }
            SB();
            STAMP(3 + 2 * t);
        }
#pragma unroll
        for (int j = 0; j < 8; ++j) { la = fadd_s(la, s1[2 * j]); lb = fadd_s(lb, s1[2 * j + 1]); lc = fadd_s(lc, p0[2 * j]); ld = fadd_s(ld, p0[2 * j + 1]); }
    }
    if (grp == 0) { __builtin_amdgcn_s_setprio(0); BARX(); }
    STAMP(18);

    {
        float inv = __builtin_amdgcn_rcpf(swapsum((la + lb) + (lc + ld)));
        asm volatile("s_nop 1" : "+v"(inv));
        MPAD4(acc);
#pragma unroll
        for (int d = 0; d < 4; ++d) {
#pragma unroll
            for (int r = 0; r < 16; ++r) acc[d][r] = fmul_s(acc[d][r], inv);
            qf[2 * d] = pack8(acc[d], 0); qf[2 * d + 1] = pack8(acc[d], 1);
            __builtin_amdgcn_raw_buffer_store_b128(__builtin_bit_cast(u32x4, qf[2 * d]), sof, vo16, (2 * jj + grp) * 32768 + qb * 8192 + d * 2048, 16);
            __builtin_amdgcn_raw_buffer_store_b128(__builtin_bit_cast(u32x4, qf[2 * d + 1]), sof, vo16, (2 * jj + grp) * 32768 + qb * 8192 + d * 2048 + 1024, 16);
            SB();
        }
    }
    const rsrc_t swo = mkrsrc(wo + (size_t)(8 * xq * 128) * 1280 + 320 * jj - BIAS, 1024 * 1280 - 320 * jj + BIAS);
    const unsigned vwA = BIAS + (16 * lw + dr) * 1280 + dsub * 64 + 16 * dsl, vwB = BIAS + (16 * lw + dr) * 1280 + 16 * dsl + (dsub ? 8 * 1280 : 256) - 2048;
    auto issueW = [&](int hl, int half, int buf) {
        const unsigned so = (unsigned)(hl * 128 + 64 * half) * 1280u, ld = lds0 + buf * WT + 5 * lw * 1024;
        dma4<NT_WO>(ld, vwA, vwA - 896, vwB, vwA + 10240 + 64 - 3072, swo, so);
        dma1<NT_WO>(ld + 4096, vwA, swo, so + 10240 + 192);
    };
    const lptr x1 = smem + (lw < 2 ? lw * 8192 : 7 * WT + (lw - 2) * 8192) + vo16;
    if (grp) {
#pragma unroll
        for (int f = 0; f < 8; ++f) *(LAS bf16x8*)(x1 + 1024 * f) = qf[f];
        __builtin_amdgcn_s_setprio(0);
        BAR();
        BARX();
        WAITV(0);
        if (lane == 0) __hip_atomic_store(flagp(bid, 1, qb), tag, __ATOMIC_RELAXED, __HIP_MEMORY_SCOPE_AGENT);
        STAMP(19);
        issueW(hl1(3) + 1, 0, 0); issueW(hl1(3) + 1, 1, 7);
        wait_flags(flagp(partner(1 + (lane % 3)), 1, qb), tag);
        WAITV(0);
        STAMP(21);
        BARX();
    } else {
        bf16x8 cur[4], nxt[4];
#pragma unroll
        for (int k = 0; k < 4; ++k) { cur[k] = qf[k]; nxt[k] = qf[4 + k]; }
        auto issueO = [&](bf16x8* f, int blk) { ld4_sc1(f, vo16, sof, hl1(blk >> 1) * 32768 + qb * 8192 + 4096 * (blk & 1)); };
        issueW(hl1(0), 0, 5);
        BARX();
        issueW(hl1(0), 1, 6); issueW(hl1(1), 0, 7);
        issueV(10, 2 * SLOT); issueK(11, 4 * SLOT);
        BARX();
        issueV(11, 4 * SLOT); issueK(12, 0);
        WAITV(8);
        if (lane == 0) __hip_atomic_store(flagp(bid, 0, qb), tag, __ATOMIC_RELAXED, __HIP_MEMORY_SCOPE_AGENT);
        BARX();
        oblock<true>(acc, cur, smem + 5 * WT + trb);
        issueV(12, 0); issueK(13, 2 * SLOT);
        WAITV(8);
        BARX();
        issueO(cur, 2);
        issueW(hl1(1), 1, 5);
        oblock(acc, nxt, smem + 6 * WT + trb);
        issueO(nxt, 3);
        issueV(13, 2 * SLOT); issueK(14, 4 * SLOT);
        WAITV(17);
        PIN4(cur);
        oblock(acc, cur, smem + 7 * WT + trb);
        issueO(cur, 4);
        WAITV(12);
        BARX();
        unsigned wdummy = 0;
        {
            const int r = 64 + 40 * b + (lane < 40 ? lane : 0);
            const char* p = wo + (size_t)((8 * xq + hl1(1 + (r >> 7)) + 1) * 128 + (r & 127)) * 1280 + 320 * jj + (lw == 3 ? 316 : 128 * lw);
            asm volatile("global_load_dword %0, %1, off" : "+v"(wdummy) : "v"(p) : "memory");
        }
        issueW(hl1(2), 0, 6); issueW(hl1(2), 1, 7);
        PIN4(nxt);
        oblock(acc, nxt, smem + 5 * WT + trb);
        issueO(nxt, 5);
        issueV(14, 4 * SLOT); issueK(15, 0);
        WAITV(17);
        PIN(wdummy);
        BARX();
        issueW(hl1(3), 0, 5);
        PIN4(cur);
        oblock(acc, cur, smem + 6 * WT + trb);
        issueO(cur, 6);
        issueV(15, 0);
        WAITV(13);
        BARX();
        issueW(hl1(3), 1, 6); issueW(hl1(0) + 1, 0, 2); issueW(hl1(0) + 1, 1, 3);
        PIN4(nxt);
        oblock(acc, nxt, smem + 7 * WT + trb);
        issueO(nxt, 7);
        WAITV(4);
        BARX();
        issueW(hl1(1) + 1, 0, 4);
        PIN4(cur);
        oblock(acc, cur, smem + 5 * WT + trb);
        WAITV(5);
        PIN4(nxt);
        oblock(acc, nxt, smem + 6 * WT + trb);
        WAITV(0);
        BARX();
        bf16x8 of2[24];
#pragma unroll
        for (int f = 0; f < 8; ++f) of2[f] = lds128(x1 + 1024 * f);
        BAR();
        issueW(hl1(1) + 1, 1, 5); issueW(hl1(2) + 1, 0, 6); issueW(hl1(2) + 1, 1, 1);
        oblock(acc, of2, smem + 2 * WT + trb);
        oblock(acc, of2 + 4, smem + 3 * WT + trb);
        WAITV(0);
        STAMP(20);
        BARX();
        const unsigned ob = qb * 8192 + 32768;
        auto ldo = [&](int k0) {
#pragma unroll
            for (int k = k0; k < k0 + 4; ++k)
                of2[k] = __builtin_bit_cast(bf16x8, __builtin_amdgcn_raw_buffer_load_b128(sof, vo16, (unsigned)hl1(1 + (k >> 3)) * 32768 + ob + (k & 7) * 1024, 16));
        };
        ldo(0); ldo(4); SB();
        oblock(acc, of2, smem + 4 * WT + trb);       ldo(8);  ldo(12); SB();
        oblock(acc, of2 + 4, smem + 5 * WT + trb);   ldo(16); SB();
        oblock(acc, of2 + 8, smem + 6 * WT + trb);   ldo(20); SB();
        oblock(acc, of2 + 12, smem + 1 * WT + trb);
        oblock(acc, of2 + 16, smem + 0 * WT + trb);
        oblock(acc, of2 + 20, smem + 7 * WT + trb);
        STAMP(22);
        const rsrc_t spb = mkrsrc(ws + WS_PB, 256 * PB_WG);
        f32x4 mine[5];
#define UNIT(u, e) acc[(u) >> 2][4 * ((u) & 3) + (e)]
#define UPK(u) cvtpk(UNIT(u, 0), UNIT(u, 1)), cvtpk(UNIT(u, 2), UNIT(u, 3))
#pragma unroll
        for (int g = 0; g < 4; ++g) {
            if (g == xq) {
#pragma unroll
                for (int n = 0; n < 5; ++n) mine[n] = (f32x4){UNIT(5 * g + n, 0), UNIT(5 * g + n, 1), UNIT(5 * g + n, 2), UNIT(5 * g + n, 3)};
            } else {
                const unsigned so = (unsigned)((bid & ~3) | g) * PB_WG + (xq * 4 + qb) * 5120;
                const u32x4 w01 = {UPK(5 * g), UPK(5 * g + 1)}, w23 = {UPK(5 * g + 2), UPK(5 * g + 3)};
                const u32x2 w4 = {UPK(5 * g + 4)};
                __builtin_amdgcn_raw_buffer_store_b128(w01, spb, vo16, so, 16);
                __builtin_amdgcn_raw_buffer_store_b128(w23, spb, vo16, so + 1024, 16);
                __builtin_amdgcn_raw_buffer_store_b64(w4, spb, lane * 8, so + 2048, 16);
            }
        }
        WAITV(0);
        if (lane == 0) __hip_atomic_store(flagp(bid, 2, qb), tag, __ATOMIC_RELAXED, __HIP_MEMORY_SCOPE_AGENT);
        STAMP(23);
        wait_flags(flagp((bid & ~3) | ((xq + 1 + (lane % 3)) & 3), 2, qb), tag);
        STAMP(24);
        {
            const rsrc_t sout = mkrsrc(out + ((size_t)b * SQ * (2 * DM) + br * DM + 160 * jj + 40 * xq) * 2, SQ * 2 * DM * 2 - (br * DM + 160 * jj + 40 * xq) * 2);
            const unsigned vop = rowq * (4 * DM) + 8 * h;
            u32x4 a01[3], a23[3]; u32x2 a4[3];
#pragma unroll
            for (int s = 0; s < 3; ++s) {
                const unsigned so = (unsigned)bid * PB_WG + (((xq + 1 + s) & 3) * 4 + qb) * 5120;
                a01[s] = __builtin_amdgcn_raw_buffer_load_b128(spb, vo16, so, 16);
                a23[s] = __builtin_amdgcn_raw_buffer_load_b128(spb, vo16, so + 1024, 16);
                a4[s] = __builtin_amdgcn_raw_buffer_load_b64(spb, lane * 8, so + 2048, 16);
            }
            auto up = [](unsigned lo, unsigned hi) { return (f32x4){__uint_as_float(lo << 16), __uint_as_float(lo & 0xffff0000u), __uint_as_float(hi << 16), __uint_as_float(hi & 0xffff0000u)}; };
#pragma unroll
            for (int s = 0; s < 3; ++s) {
                mine[0] += up(a01[s][0], a01[s][1]); mine[1] += up(a01[s][2], a01[s][3]);
                mine[2] += up(a23[s][0], a23[s][1]); mine[3] += up(a23[s][2], a23[s][3]); mine[4] += up(a4[s][0], a4[s][1]);
            }
#pragma unroll
            for (int n = 0; n < 5; ++n) __builtin_amdgcn_raw_buffer_store_b64((u32x2){cvtpk(mine[n][0], mine[n][1]), cvtpk(mine[n][2], mine[n][3])}, sout, vop, 16 * n, OUT_AUX);
        }
    }
    {
        if (xq == 0 && jj == 0 && wid == 0 && lane == 0)
            __hip_atomic_store((GAS u64*)(ws + WS_GEN + (br * NB + b) * 256), tag, __ATOMIC_RELAXED, __HIP_MEMORY_SCOPE_AGENT);
        STAMP(25);
        if (bid == 77 && wid == 4 * PROBE_G) { const unsigned long long te = pt0 + 8000 + 4 * (ptx - pt0); while (__builtin_amdgcn_s_memrealtime() < te) __builtin_amdgcn_s_sleep(4); }
    }
}

extern "C" void kernel_launch(void* const* d_in, const int* in_sizes, int n_in, void* d_out, int out_size,
                              void* d_ws, size_t ws_size, hipStream_t stream) {
    (void)hipFuncSetAttribute((const void*)fused_kernel, hipFuncAttributeMaxDynamicSharedMemorySize, LDS_BYTES);
    hipLaunchKernelGGL(fused_kernel, dim3(256), dim3(512), LDS_BYTES, stream,
                       (const char*)d_in[0], (const char*)d_in[1], (const char*)d_in[2], (const char*)d_in[3], (const char*)d_in[4],
                       (const char*)d_in[5], (const char*)d_in[6], (const char*)d_in[7], (const char*)d_in[8], (const char*)d_in[9],
                       (char*)d_ws + WS_SHIFT, (char*)d_out);
}
```

```cpp
#include <hip/hip_runtime.h>
#include <hip/hip_bf16.h>
#include <stdint.h>
#include <type_traits>

typedef short bf16x8 __attribute__((ext_vector_type(8)));
typedef short s16x4 __attribute__((ext_vector_type(4)));
typedef float f32x16 __attribute__((ext_vector_type(16)));
typedef float f32x4 __attribute__((ext_vector_type(4)));
typedef float f32x2 __attribute__((ext_vector_type(2)));
typedef __bf16 bf16x2_t __attribute__((ext_vector_type(2)));
typedef unsigned u32x4 __attribute__((ext_vector_type(4)));
typedef unsigned u32x2 __attribute__((ext_vector_type(2)));
typedef unsigned long long u64;
#define LAS __attribute__((address_space(3)))
#define GAS __attribute__((address_space(1)))
#define DI __device__ __forceinline__
typedef LAS char* lptr;
typedef __amdgpu_buffer_rsrc_t rsrc_t;

constexpr int NB = 8, SQ = 128, DM = 640, NH = 32, SK = 512, DH = 128;
constexpr float CEXP = 0.08838834764831845f * 1.4426950408889634f;
constexpr int SLOT = 16384;
constexpr int LDS_T = 6 * SLOT;
constexpr int WT = 20480;
constexpr int LDS_BYTES = 10 * SLOT;
constexpr size_t WS_GEN = 0, WS_FLAG = 8192, WS_OF = 1 << 20;
constexpr size_t OF_BYTES = (size_t)2 * NB * NH * 32768;
constexpr size_t WS_PB = WS_OF + OF_BYTES;
constexpr unsigned PB_WG = 4 * 8 * 3 * 1024;
#define NT_W 0
#define NT_X 0
#define NT_WO 0
#define PPRIO 2
#define PBF16 1
#define WS_SHIFT 0
#define POL 15
#define PRETOUCH 1
constexpr unsigned CODE_LINES = 180;
#define WOWARM 1
#define ROT 1
#define OUT_AUX 0
#define PROBE_BID 77
constexpr unsigned SPIN_CAP = 1u << 16;

#define MFMA32(a, b, c) __builtin_amdgcn_mfma_f32_32x32x16_bf16((a), (b), (c), 0, 0, 0)
#define WAITV(n) asm volatile("s_waitcnt vmcnt(" #n ")" ::: "memory")
#define SB() __builtin_amdgcn_sched_barrier(0)
#define BARX() do { SB(); asm volatile("s_barrier" ::: "memory"); SB(); } while (0)
#define BAR() do { SB(); asm volatile("s_waitcnt lgkmcnt(0)\n\ts_barrier" ::: "memory"); SB(); } while (0)

#define STAMP(idx)

DI unsigned cvtpk(float lo, float hi) {
    f32x2 v = {lo, hi};
    bf16x2_t b = __builtin_convertvector(v, bf16x2_t);
    return __builtin_bit_cast(unsigned, b);
}
DI bf16x8 pack8(const f32x16& x, int s) {
    u32x4 w = {cvtpk(x[8 * s + 0], x[8 * s + 1]), cvtpk(x[8 * s + 2], x[8 * s + 3]),
               cvtpk(x[8 * s + 4], x[8 * s + 5]), cvtpk(x[8 * s + 6], x[8 * s + 7])};
    return __builtin_bit_cast(bf16x8, w);
}
template <int NT> DI void dma4(unsigned lds, unsigned v0, unsigned v1, unsigned v2, unsigned v3, rsrc_t srd, unsigned soff) {
    if (NT) asm volatile("s_mov_b32 m0, %0\n\ts_nop 3\n\tbuffer_load_dwordx4 %1, %5, %6 offen nt lds\n\tbuffer_load_dwordx4 %2, %5, %6 offen offset:1024 nt lds\n\t"
                         "buffer_load_dwordx4 %3, %5, %6 offen offset:2048 nt lds\n\tbuffer_load_dwordx4 %4, %5, %6 offen offset:3072 nt lds"
                         :: "s"(lds), "v"(v0), "v"(v1), "v"(v2), "v"(v3), "s"(srd), "s"(soff) : "memory");
    else asm volatile("s_mov_b32 m0, %0\n\ts_nop 3\n\tbuffer_load_dwordx4 %1, %5, %6 offen lds\n\tbuffer_load_dwordx4 %2, %5, %6 offen offset:1024 lds\n\t"
                      "buffer_load_dwordx4 %3, %5, %6 offen offset:2048 lds\n\tbuffer_load_dwordx4 %4, %5, %6 offen offset:3072 lds"
                      :: "s"(lds), "v"(v0), "v"(v1), "v"(v2), "v"(v3), "s"(srd), "s"(soff) : "memory");
}
template <int NT> DI void dma1(unsigned lds, unsigned voff, rsrc_t srd, unsigned soff) {
    if (NT) asm volatile("s_mov_b32 m0, %0\n\ts_nop 3\n\tbuffer_load_dwordx4 %1, %2, %3 offen nt lds" :: "s"(lds), "v"(voff), "s"(srd), "s"(soff) : "memory");
    else asm volatile("s_mov_b32 m0, %0\n\ts_nop 3\n\tbuffer_load_dwordx4 %1, %2, %3 offen lds" :: "s"(lds), "v"(voff), "s"(srd), "s"(soff) : "memory");
}
DI void ld4_sc1(bf16x8* d, unsigned voff, rsrc_t srd, unsigned soff) {
    asm volatile("s_nop 4\n\tbuffer_load_dwordx4 %0, %4, %5, %6 offen sc1\n\tbuffer_load_dwordx4 %1, %4, %5, %6 offen offset:1024 sc1\n\t"
                 "buffer_load_dwordx4 %2, %4, %5, %6 offen offset:2048 sc1\n\tbuffer_load_dwordx4 %3, %4, %5, %6 offen offset:3072 sc1"
                 : "+v"(d[0]), "+v"(d[1]), "+v"(d[2]), "+v"(d[3]) : "v"(voff), "s"(srd), "s"(soff) : "memory");
}
constexpr unsigned BIAS = 4096;
#define WAITV_F(n, f) asm volatile("s_waitcnt vmcnt(" #n ")" : "+v"((f)[0]), "+v"((f)[1]), "+v"((f)[2]), "+v"((f)[3]) :: "memory")
DI bf16x8 lds128(lptr p) { return *(const LAS bf16x8*)p; }
DI s16x4 ldstr(lptr p) {
    typedef short v4i16_t __attribute__((ext_vector_type(4)));
    return __builtin_amdgcn_ds_read_tr16_b64_v4i16((LAS v4i16_t*)p);
}
DI bf16x8 cat(s16x4 a, s16x4 b) { return __builtin_shufflevector(a, b, 0, 1, 2, 3, 4, 5, 6, 7); }
DI float swapmax(float v) {
    auto r = __builtin_amdgcn_permlane32_swap(__float_as_uint(v), __float_as_uint(v), false, false);
    return fmaxf(__uint_as_float(r[0]), __uint_as_float(r[1]));
}
DI float swapsum(float v) {
    auto r = __builtin_amdgcn_permlane32_swap(__float_as_uint(v), __float_as_uint(v), false, false);
    return __uint_as_float(r[0]) + __uint_as_float(r[1]);
}
DI rsrc_t mkrsrc(const void* p, unsigned bytes) {
    return __builtin_amdgcn_make_buffer_rsrc((void*)p, (short)0, (int)bytes, 0x00020000);
}
DI void zero16(f32x16& v) {
#pragma unroll
    for (int r = 0; r < 16; ++r) v[r] = 0.f;
}
DI void wait_flags(const GAS u64* p, u64 tag) {
#pragma unroll 1
    for (unsigned it = 0; it < SPIN_CAP; ++it) {
        const u64 v = __hip_atomic_load(p, __ATOMIC_RELAXED, __HIP_MEMORY_SCOPE_AGENT);
        if (__all(v == tag)) break;
        __builtin_amdgcn_s_sleep(1);
    }
    asm volatile("" ::: "memory");
}

DI float max3f(float a, float b, float c) { float r; asm("v_max3_f32 %0, %1, %2, %3" : "=v"(r) : "v"(a), "v"(b), "v"(c)); return r; }
DI float fadd_s(float a, float b) { float r; asm("v_add_f32_e32 %0, %1, %2" : "=v"(r) : "v"(a), "v"(b)); return r; }
DI float fmul_s(float a, float b) { float r; asm("v_mul_f32_e32 %0, %1, %2" : "=v"(r) : "v"(a), "v"(b)); return r; }
DI float fsub_s(float a, float b) { float r; asm("v_sub_f32_e32 %0, %1, %2" : "=v"(r) : "v"(a), "v"(b)); return r; }
DI float exp_s(float a) { float r; asm("v_exp_f32_e32 %0, %1" : "=v"(r) : "v"(a)); return r; }
#define MPAD4(a) asm volatile("s_nop 7\n\ts_nop 7" : "+v"((a)[0]), "+v"((a)[1]), "+v"((a)[2]), "+v"((a)[3]))
#define PIN(x) asm volatile("" : "+v"(x))
#define PIN4(f) asm volatile("" : "+v"((f)[0]), "+v"((f)[1]), "+v"((f)[2]), "+v"((f)[3]) :: "memory")
DI lptr opaque(lptr p) { unsigned a = (unsigned)(uintptr_t)p; asm volatile("" : "+v"(a)); return (lptr)(uintptr_t)a; }
constexpr int LA = 6;
#define SM_THR 8.0f

struct QBase { lptr x0, x1, w; };
DI void qload(int i, bf16x8* wf, bf16x8* xf, const QBase& q) {
    const int s = i >> 2, d = i & 3;
    if (d == 0) xf[s] = lds128(((s & 1) ? q.x1 : q.x0) + 512 * (s >> 1));
    wf[i & 7] = cat(ldstr(q.w + 4096 * s + 512 * d), ldstr(q.w + 4096 * s + 512 * d + 256));
}
DI void qpre(bf16x8* wf, bf16x8* xf, const QBase& q) {
#pragma unroll
    for (int i = 0; i < LA; ++i) qload(i, wf, xf, q);
}
template <bool NEXT> DI void qsteps(f32x16* acc, bf16x8* wf, bf16x8* xf, const QBase& q, const QBase& qn) {
#pragma unroll
    for (int i = 0; i < 16; ++i) {
        SB();
        acc[i & 3] = MFMA32(wf[i & 7], xf[i >> 2], acc[i & 3]);
        PIN(acc[i & 3]);
        SB();
        if (NEXT && i + LA == 16) BARX();
        if (i + LA < 16) qload(i + LA, wf, xf, q); else if (NEXT) qload(i + LA - 16, wf, xf, qn);
    }
    SB();
}
template <bool FIRST = false> DI void oblock(f32x16* acc, const bf16x8* of, lptr wp_) {
    const lptr wp = opaque(wp_);
    bf16x8 wf[8];
    auto load = [&](int i) { wf[i & 7] = cat(ldstr(wp + 5120 * (i / 5) + 512 * (i % 5)), ldstr(wp + 5120 * (i / 5) + 2560 + 512 * (i % 5))); };
#pragma unroll
    for (int i = 0; i < LA; ++i) load(i);
#pragma unroll
    for (int i = 0; i < 20; ++i) {
        SB();
        if (FIRST && i < 5) { f32x16 z; zero16(z); acc[i] = MFMA32(wf[i & 7], of[0], z); }
        else acc[i % 5] = MFMA32(wf[i & 7], of[i / 5], acc[i % 5]);
        PIN(acc[i % 5]);
        SB();
        if (i + LA < 20) load(i + LA);
    }
    SB();
}
DI void aload(int i, bf16x8* fr, lptr vp, lptr kp0, lptr kp1) {
    if (i < 16) fr[i & 7] = cat(ldstr(vp + 4096 * (i >> 2) + 512 * (i & 3)), ldstr(vp + 4096 * (i >> 2) + 2048 + 512 * (i & 3)));
    else { const int s = i & 7; fr[i & 7] = lds128(((s & 1) ? kp1 : kp0) + 512 * (s >> 1) + 8192 * ((i - 16) >> 3)); }
}

__global__ __launch_bounds__(512, 2) void fused_kernel(
    const char* xA, const char* kA, const char* vA, const char* wqA, const char* woA,
    const char* xB, const char* kB, const char* vB, const char* wqB, const char* woB,
    char* ws, char* out)
{
    extern __shared__ __attribute__((aligned(16))) char smem_[];
    const lptr smem = (lptr)smem_;
    const unsigned lds0 = (unsigned)(uintptr_t)smem;
    const int tid = threadIdx.x, lane = tid & 63;
    const int wid = __builtin_amdgcn_readfirstlane(tid >> 6);
    const int bid = blockIdx.x;
    const int xg = bid & 7, ii = bid >> 3;
    const int br = xg >> 2, xq = xg & 3, b = ii >> 2, jj = ii & 3;
    const int h1 = 8 * xq + 2 * jj;
    const char* x = br ? xB : xA;
    const char* kc = br ? kB : kA;
    const char* vc = br ? vB : vA;
    const char* wq = br ? wqB : wqA;
    const char* wo = br ? woB : woA;
    const int qb = wid & 3, grp = wid >> 2, lw = wid & 3;
    const int l31 = lane & 31, h = lane >> 5;
    STAMP(0);
    const char* codebase;
    asm volatile("s_getpc_b64 %0" : "=s"(codebase));

    u64 gen;
    {
        const char* gp = ws + WS_GEN + (br * NB + b) * 256;
        asm volatile("global_load_dwordx2 %0, %1, off sc1" : "=v"(gen) : "v"(gp) : "memory");
    }

    const rsrc_t sk = mkrsrc(kc + (size_t)(b * NH + h1) * SK * DH * 2 - BIAS, 2 * SK * DH * 2 + BIAS);
    const rsrc_t sv = mkrsrc(vc + (size_t)(b * NH + h1) * SK * DH * 2 - BIAS, 2 * SK * DH * 2 + BIAS);
    const rsrc_t sof = mkrsrc(ws + WS_OF + (size_t)((br * NB + b) * NH + 8 * xq) * 32768, 8 * 32768);

    const unsigned dsub = lane >> 5, dr = (lane >> 2) & 7, dsl = lane & 3;
    const unsigned xr0 = 16u * (dsl ^ ((dr >> 2) & 3)), xr1 = 16u * (dsl ^ ((2 + (dr >> 2)) & 3));
    const unsigned vk0 = BIAS + 4096 * lw + dr * 256 + dsub * 64 + xr0, vk2 = BIAS + 4096 * lw + dr * 256 + dsub * 64 + xr1;
    const unsigned vv0 = BIAS + 4096 * lw + dr * 256 + dsub * 64 + 16 * dsl;
    const unsigned ldw = lds0 + 4096 * lw;

    auto issueK = [&](int t, unsigned pair) {
        if ((POL & 1) == ((POL >> 2) & 1) ? (POL & 1) : ((POL >> (2 * br)) & 1)) dma4<1>(ldw + pair, vk0, vk0 - 896, vk2, vk2 - 896, sk, (unsigned)t * 16384u);
        else dma4<0>(ldw + pair, vk0, vk0 - 896, vk2, vk2 - 896, sk, (unsigned)t * 16384u); };
    auto issueV = [&](int t, unsigned pair) {
        if (((POL >> 1) & 1) == ((POL >> 3) & 1) ? ((POL >> 1) & 1) : ((POL >> (2 * br + 1)) & 1)) dma4<1>(ldw + pair + SLOT, vv0, vv0 - 896, vv0, vv0 - 896, sv, (unsigned)t * 16384u);
        else dma4<0>(ldw + pair + SLOT, vv0, vv0 - 896, vv0, vv0 - 896, sv, (unsigned)t * 16384u); };
    auto nextpair = [](unsigned p) { return (p == 4u * SLOT) ? 0u : p + 2u * SLOT; };

    const int G1 = (lane >> 4) & 1, tq = (lane & 15) >> 2, tp = lane & 3;
    const int sig = ((tp & 1) << 1) | (tp >> 1);
    const unsigned wqb = SLOT + 2048 * h + 64 * tq + 32 * G1 + 8 * sig;
    const unsigned rowq = 32 * qb + l31;
    const unsigned xsw = (rowq >> 2) & 3;
    const unsigned xb0 = 1024 * (rowq >> 3) + 64 * (rowq & 7) + 16 * ((0 + h) ^ xsw);
    const unsigned xb1 = 1024 * (rowq >> 3) + 64 * (rowq & 7) + 16 * ((2 + h) ^ xsw);
    const unsigned ksw = (l31 >> 2) & 3;
    const unsigned kb0 = 2048 * (l31 >> 3) + 64 * (l31 & 7) + 16 * ((0 + h) ^ ksw);
    const unsigned kb1 = 2048 * (l31 >> 3) + 64 * (l31 & 7) + 16 * ((2 + h) ^ ksw);
    const unsigned trb = 64 * (4 * h + tq) + 32 * G1 + 8 * tp;
    const unsigned vo16 = lane * 16;

    auto flagp = [&](int bb, int stage, int w) { return (GAS u64*)(ws + WS_FLAG + (size_t)((bb * 3 + stage) * 8 + w) * 64); };
    auto partner = [&](int dj) { return (bid & ~24) | (((jj + dj) & 3) << 3); };
    auto hl1 = [&](int dj) { return 2 * ((jj + dj) & 3); };

    f32x16 acc[5];
    bf16x8 qf[8], pf[4], fr[8];
    f32x16 s0, s1;
#pragma unroll
    for (int d = 0; d < 4; ++d) zero16(acc[d]);

    auto nextstage = [](unsigned st) { return st == 8u * SLOT ? 4u * SLOT : (st == 4u * SLOT ? 6u * SLOT : (st == 6u * SLOT ? 2u * SLOT : 8u * SLOT)); };
    auto qbase = [&](unsigned st) { return QBase{opaque(smem + st + xb0), opaque(smem + st + xb1), opaque(smem + st + wqb)}; };
    if (grp == 0) {
        unsigned st = 8 * SLOT;
        unsigned dummy = 0;
        {
            const unsigned line = lw * 64 + lane;
            const char* p = codebase + (line < CODE_LINES ? line : 0) * 128;
            asm volatile("global_load_dword %0, %1, off" : "+v"(dummy) : "v"(p) : "memory");
        }
        BARX();
        QBase qc = qbase(st);
        qpre(fr, pf, qc);
#pragma unroll 1
        for (int i = 0; i < 10; ++i) {
            st = nextstage(st);
            const QBase qn = qbase(st);
            qsteps<true>(acc, fr, pf, qc, qn);
            qc = qn;
        }
        WAITV(0);
        PIN(dummy);
    } else {
        const rsrc_t sx = mkrsrc(x + (size_t)b * SQ * DM * 2, SQ * DM * 2);
        const rsrc_t sw = mkrsrc(wq + (size_t)h1 * 256 - BIAS, DM * 8192 - h1 * 256 + BIAS);
        const unsigned vx0 = (32 * lw + dr) * 1280 + dsub * 64 + xr0, vx1 = (32 * lw + 8 + dr) * 1280 + dsub * 64 + xr1 - 1024;
        const unsigned vw0 = BIAS + (16 * lw + dr) * 8192 + dsub * 64 + 16 * dsl;
        const int rot = ROT ? b + 3 * jj : 0;
        constexpr unsigned T0 = 6 * SLOT, T1 = 8 * SLOT, P1 = 2 * SLOT, P2 = 4 * SLOT;
        int ci = rot >= 10 ? rot - 10 : rot;
        auto issueQn = [&](int hd, unsigned st) {
            dma4<NT_W>(ldw + st + SLOT, vw0, vw0 - 896, vw0 + 65536 - 2048, vw0 + 65536 - 2944, sw, (unsigned)ci * 524288u + 256 * hd);
            dma4<NT_X>(ldw + st, vx0, vx1, vx0 + 20480 - 2048, vx1 + 20480 - 2048, sx, 128 * ci);
            ci = ci == 9 ? 0 : ci + 1;
        };
        issueQn(0, T1);
        WAITV(0); BARX();
        issueQn(0, P2); issueQn(0, T0);
        WAITV(8);  BARX(); issueQn(0, P1);
        WAITV(8);  BARX(); issueQn(0, T1);
        WAITV(8);  BARX(); issueQn(0, P2);
        WAITV(8);  BARX(); issueQn(0, T0); issueK(0, 0); issueV(0, 0);
        WAITV(16); BARX(); issueQn(0, P1);
        WAITV(16); BARX(); issueQn(0, T1);
        WAITV(8);  BARX(); issueQn(0, P2);
        WAITV(8);  BARX(); issueQn(1, T0);
        WAITV(8);  BARX(); issueK(1, P1); issueV(1, P1);
        BARX(); issueQn(1, T1);
        const QBase q0 = qbase(T0), q1 = qbase(T1);
        WAITV(16); BARX();
        qpre(fr, pf, q0);
        issueK(2, P2);
        qsteps<false>(acc, fr, pf, q0, q0);
        WAITV(4);
        unsigned pv = P2;
#pragma unroll 1
        for (int c = 1; c < 9; c += 2) {
            unsigned pk = nextpair(pv);
            BARX();
            qpre(fr, pf, q1);
            issueQn(1, T0);
            qsteps<false>(acc, fr, pf, q1, q1);
            issueV(c + 1, pv); issueK(c + 2, pk);
            pv = nextpair(pk);
            WAITV(8);
            BARX();
            qpre(fr, pf, q0);
            issueQn(1, T1);
            qsteps<false>(acc, fr, pf, q0, q0);
            issueV(c + 2, pk); issueK(c + 3, pv);
            WAITV(8);
        }
        BARX();
        qpre(fr, pf, q1);
        qsteps<false>(acc, fr, pf, q1, q1);
    }
    STAMP(1);
    asm volatile("" : "+v"(gen));
    const u64 tag = gen + 1;

    __builtin_amdgcn_s_setprio(PPRIO);
    MPAD4(acc);
    {
        float cexp = CEXP; PIN(cexp);
#pragma unroll
        for (int d = 0; d < 4; ++d) {
#pragma unroll
            for (int r = 0; r < 16; ++r) acc[d][r] = fmul_s(acc[d][r], cexp);
            qf[2 * d] = pack8(acc[d], 0); qf[2 * d + 1] = pack8(acc[d], 1); zero16(acc[d]);
        }
    }
    if (grp == 0) BARX();
    float la = 0.f, lb = 0.f, lc = 0.f, ld = 0.f;
    {
        f32x16 negm, p0; zero16(negm); zero16(s1); zero16(p0);
        unsigned pk = grp ? 4 * SLOT : 0;
        lptr kp0 = opaque(smem + pk + kb0), kp1 = opaque(smem + pk + kb1);
#pragma unroll
        for (int i = 16; i < 16 + LA; ++i) aload(i, fr, kp0, kp0, kp1);
        const GAS u64* fp = flagp(partner(1 + (lane % 3)), 0, qb);
        u64 fv = 0;
        const int tw = grp ? 3 : -1;
        float thr = -__builtin_inff(), dmin = -__builtin_inff();
        u32x4 pw[4];
#pragma unroll 1
        for (int t = 0; t < 8; ++t) {
            if (__builtin_expect(t == tw, 0)) asm volatile("global_load_dwordx2 %0, %1, off sc1" : "+v"(fv) : "v"(fp) : "memory");
            float c0, c1;
#pragma unroll
            for (int j = 0; j < 16; ++j) {
                const int i = 16 + j;
                SB();
                if (j < 8) s0 = MFMA32(fr[i & 7], qf[j], j == 0 ? negm : s0); else s1 = MFMA32(fr[i & 7], qf[j - 8], j == 8 ? negm : s1);
                SB();
                if (i + LA < 32) aload(i + LA, fr, kp0, kp0, kp1);
                if (j < 8) {
                    la = fadd_s(la, s1[2 * j]); lb = fadd_s(lb, s1[2 * j + 1]); lc = fadd_s(lc, p0[2 * j]); ld = fadd_s(ld, p0[2 * j + 1]);
                    PIN(la); PIN(lb); PIN(lc); PIN(ld);
                }
                if (j >= 10) {
                    const int e = 2 * (j - 10);
                    p0[e] = exp_s(s0[e]); p0[e + 1] = exp_s(s0[e + 1]); PIN(p0);
                    if (j == 10) { c0 = max3f(s0[0], s0[1], s0[2]); c1 = max3f(s0[3], s0[4], s0[5]); PIN(c0); PIN(c1); }
                    if (j == 11) { c0 = max3f(c0, s0[6], s0[7]); c1 = max3f(c1, s0[8], s0[9]); PIN(c0); PIN(c1); }
                    if (j == 12) { c0 = max3f(c0, s0[10], s0[11]); PIN(c0); }
                    if (j == 13) { c1 = max3f(c1, s0[12], s0[13]); PIN(c1); }
                    if (j == 14) { c0 = max3f(c0, s0[14], s0[15]); PIN(c0); }
                    if (j == 15) { c0 = max3f(c0, c1, c1); PIN(c0); }
                    pw[e >> 3][(e >> 1) & 3] = cvtpk(p0[e], p0[e + 1]); PIN(pw[e >> 3][(e >> 1) & 3]);
                }
            }
            WAITV(0);
            if (__builtin_expect(t == tw, 0)) { PIN(fv); if (!__all(fv == tag)) wait_flags(fp, tag); }
            STAMP(2 + 2 * t);
            BARX();
            const lptr vp = opaque(smem + pk + SLOT + trb);
            pk = nextpair(pk);
            kp0 = opaque(smem + pk + kb0); kp1 = opaque(smem + pk + kb1);
#pragma unroll
            for (int i = 0; i < LA; ++i) aload(i, fr, vp, vp, vp);
            PIN(s1);
            SB();
            {
                float c2 = max3f(s1[0], s1[1], s1[2]), c3 = max3f(s1[3], s1[4], s1[5]);
                c1 = max3f(s1[6], s1[7], s1[8]); c0 = max3f(c0, s1[9], s1[10]);
                c2 = max3f(c2, s1[11], s1[12]); c3 = max3f(c3, s1[13], s1[14]); c1 = max3f(c1, s1[15], c0);
                c0 = max3f(c1, c2, c3);
                auto r_ = __builtin_amdgcn_permlane32_swap(__float_as_uint(c0), __float_as_uint(c0), false, false);
                const float mx = max3f(__uint_as_float(r_[0]), __uint_as_float(r_[1]), __uint_as_float(r_[1]));
                if (__builtin_expect(__any(!(mx <= thr)), 0)) {
                    const float d = fmaxf(mx, dmin);
                    float f = __builtin_amdgcn_exp2f(fminf(-d, 0.f));
                    asm volatile("s_nop 1" : "+v"(f));
#pragma unroll
                    for (int r = 0; r < 16; ++r) { s0[r] = fsub_s(s0[r], d); s1[r] = fsub_s(s1[r], d); negm[r] = fsub_s(negm[r], d); }
                    la = fmul_s(la, f); lb = fmul_s(lb, f); lc = fmul_s(lc, f); ld = fmul_s(ld, f);
#pragma unroll
                    for (int dd = 0; dd < 4; ++dd)
#pragma unroll
                        for (int r = 0; r < 16; ++r) acc[dd][r] = fmul_s(acc[dd][r], f);
#pragma unroll
                    for (int e = 0; e < 12; ++e) p0[e] = __builtin_amdgcn_exp2f(s0[e]);
#pragma unroll
                    for (int e = 0; e < 12; e += 2) pw[e >> 3][(e >> 1) & 3] = cvtpk(p0[e], p0[e + 1]);
                }
                thr = SM_THR; dmin = 0.f;
            }
#pragma unroll
            for (int i = 0; i < 16; ++i) {
                SB();
                acc[i & 3] = MFMA32(fr[i & 7], __builtin_bit_cast(bf16x8, pw[i >> 2]), acc[i & 3]);
                SB();
                constexpr int pairs[16] = {6, 7, 8, 9, 10, 11, -1, 12, 13, 14, 15, -1, -1, -1, -1, -1};
                const int q = pairs[i];
                if (q >= 0) {
                    if (q < 8) { p0[2 * q] = __builtin_amdgcn_exp2f(s0[2 * q]); p0[2 * q + 1] = __builtin_amdgcn_exp2f(s0[2 * q + 1]); PIN(p0); }
                    else { s1[2 * q - 16] = __builtin_amdgcn_exp2f(s1[2 * q - 16]); s1[2 * q - 15] = __builtin_amdgcn_exp2f(s1[2 * q - 15]); PIN(s1); }
                }
                aload(i + LA, fr, vp, kp0, kp1);
                if (q >= 0) {
                    pw[q >> 2][q & 3] = q < 8 ? cvtpk(p0[2 * q], p0[2 * q + 1]) : cvtpk(s1[2 * q - 16], s1[2 * q - 15]);
                    PIN(pw[q >> 2][q & 3]);
                }
            }
            SB();
            STAMP(3 + 2 * t);
        }
#pragma unroll
        for (int j = 0; j < 8; ++j) { la = fadd_s(la, s1[2 * j]); lb = fadd_s(lb, s1[2 * j + 1]); lc = fadd_s(lc, p0[2 * j]); ld = fadd_s(ld, p0[2 * j + 1]); }
    }
    if (grp == 0) { __builtin_amdgcn_s_setprio(0); BARX(); }
    STAMP(18);

    {
        float inv = __builtin_amdgcn_rcpf(swapsum((la + lb) + (lc + ld)));
        asm volatile("s_nop 1" : "+v"(inv));
        MPAD4(acc);
#pragma unroll
        for (int d = 0; d < 4; ++d) {
#pragma unroll
            for (int r = 0; r < 16; ++r) acc[d][r] = fmul_s(acc[d][r], inv);
            qf[2 * d] = pack8(acc[d], 0); qf[2 * d + 1] = pack8(acc[d], 1);
            __builtin_amdgcn_raw_buffer_store_b128(__builtin_bit_cast(u32x4, qf[2 * d]), sof, vo16, (2 * jj + grp) * 32768 + qb * 8192 + d * 2048, 16);
            __builtin_amdgcn_raw_buffer_store_b128(__builtin_bit_cast(u32x4, qf[2 * d + 1]), sof, vo16, (2 * jj + grp) * 32768 + qb * 8192 + d * 2048 + 1024, 16);
            SB();
        }
    }
    const rsrc_t swo = mkrsrc(wo + (size_t)(8 * xq * 128) * 1280 + 320 * jj - BIAS, 1024 * 1280 - 320 * jj + BIAS);
    const unsigned vwA = BIAS + (16 * lw + dr) * 1280 + dsub * 64 + 16 * dsl, vwB = BIAS + (16 * lw + dr) * 1280 + 16 * dsl + (dsub ? 8 * 1280 : 256) - 2048;
    auto issueW = [&](int hl, int half, int buf) {
        const unsigned so = (unsigned)(hl * 128 + 64 * half) * 1280u, ld = lds0 + buf * WT + 5 * lw * 1024;
        dma4<NT_WO>(ld, vwA, vwA - 896, vwB, vwA + 10240 + 64 - 3072, swo, so);
        dma1<NT_WO>(ld + 4096, vwA, swo, so + 10240 + 192);
    };
    const lptr x1 = smem + (lw < 2 ? lw * 8192 : 7 * WT + (lw - 2) * 8192) + vo16;
    if (grp) {
#pragma unroll
        for (int f = 0; f < 8; ++f) *(LAS bf16x8*)(x1 + 1024 * f) = qf[f];
        __builtin_amdgcn_s_setprio(0);
        BAR();
        BARX();
        WAITV(0);
        if (lane == 0) __hip_atomic_store(flagp(bid, 1, qb), tag, __ATOMIC_RELAXED, __HIP_MEMORY_SCOPE_AGENT);
        STAMP(19);
        issueW(hl1(3) + 1, 0, 0); issueW(hl1(3) + 1, 1, 7);
        wait_flags(flagp(partner(1 + (lane % 3)), 1, qb), tag);
        WAITV(0);
        STAMP(21);
        BARX();
    } else {
        bf16x8 cur[4], nxt[4];
#pragma unroll
        for (int k = 0; k < 4; ++k) { cur[k] = qf[k]; nxt[k] = qf[4 + k]; }
        auto issueO = [&](bf16x8* f, int blk) { ld4_sc1(f, vo16, sof, hl1(blk >> 1) * 32768 + qb * 8192 + 4096 * (blk & 1)); };
        issueW(hl1(0), 0, 5);
        BARX();
        issueW(hl1(0), 1, 6); issueW(hl1(1), 0, 7);
        issueV(10, 2 * SLOT); issueK(11, 4 * SLOT);
        BARX();
        issueV(11, 4 * SLOT); issueK(12, 0);
        WAITV(8);
        if (lane == 0) __hip_atomic_store(flagp(bid, 0, qb), tag, __ATOMIC_RELAXED, __HIP_MEMORY_SCOPE_AGENT);
        BARX();
        oblock<true>(acc, cur, smem + 5 * WT + trb);
        issueV(12, 0); issueK(13, 2 * SLOT);
        WAITV(8);
        BARX();
        issueO(cur, 2);
        issueW(hl1(1), 1, 5);
        oblock(acc, nxt, smem + 6 * WT + trb);
        issueO(nxt, 3);
        issueV(13, 2 * SLOT); issueK(14, 4 * SLOT);
        WAITV(17);
        PIN4(cur);
        oblock(acc, cur, smem + 7 * WT + trb);
        issueO(cur, 4);
        WAITV(12);
        BARX();
        unsigned wdummy = 0;
        {
            const int r = 64 + 40 * b + (lane < 40 ? lane : 0);
            const char* p = wo + (size_t)((8 * xq + hl1(1 + (r >> 7)) + 1) * 128 + (r & 127)) * 1280 + 320 * jj + (lw == 3 ? 316 : 128 * lw);
            asm volatile("global_load_dword %0, %1, off" : "+v"(wdummy) : "v"(p) : "memory");
        }
        issueW(hl1(2), 0, 6); issueW(hl1(2), 1, 7);
        PIN4(nxt);
        oblock(acc, nxt, smem + 5 * WT + trb);
        issueO(nxt, 5);
        issueV(14, 4 * SLOT); issueK(15, 0);
        WAITV(17);
        PIN(wdummy);
        BARX();
        issueW(hl1(3), 0, 5);
        PIN4(cur);
        oblock(acc, cur, smem + 6 * WT + trb);
        issueO(cur, 6);
        issueV(15, 0);
        WAITV(13);
        BARX();
        issueW(hl1(3), 1, 6); issueW(hl1(0) + 1, 0, 2); issueW(hl1(0) + 1, 1, 3);
        PIN4(nxt);
        oblock(acc, nxt, smem + 7 * WT + trb);
        issueO(nxt, 7);
        WAITV(4);
        BARX();
        issueW(hl1(1) + 1, 0, 4);
        PIN4(cur);
        oblock(acc, cur, smem + 5 * WT + trb);
        WAITV(5);
        PIN4(nxt);
        oblock(acc, nxt, smem + 6 * WT + trb);
        WAITV(0);
        BARX();
        bf16x8 of2[24];
#pragma unroll
        for (int f = 0; f < 8; ++f) of2[f] = lds128(x1 + 1024 * f);
        BAR();
        issueW(hl1(1) + 1, 1, 5); issueW(hl1(2) + 1, 0, 6); issueW(hl1(2) + 1, 1, 1);
        oblock(acc, of2, smem + 2 * WT + trb);
        oblock(acc, of2 + 4, smem + 3 * WT + trb);
        WAITV(0);
        STAMP(20);
        BARX();
        const unsigned ob = qb * 8192 + 32768;
        auto ldo = [&](int k0) {
#pragma unroll
            for (int k = k0; k < k0 + 4; ++k)
                of2[k] = __builtin_bit_cast(bf16x8, __builtin_amdgcn_raw_buffer_load_b128(sof, vo16, (unsigned)hl1(1 + (k >> 3)) * 32768 + ob + (k & 7) * 1024, 16));
        };
        ldo(0); ldo(4); SB();
        oblock(acc, of2, smem + 4 * WT + trb);       ldo(8);  ldo(12); SB();
        oblock(acc, of2 + 4, smem + 5 * WT + trb);   ldo(16); SB();
        oblock(acc, of2 + 8, smem + 6 * WT + trb);   ldo(20); SB();
        oblock(acc, of2 + 12, smem + 1 * WT + trb);
        oblock(acc, of2 + 16, smem + 0 * WT + trb);
        oblock(acc, of2 + 20, smem + 7 * WT + trb);
        STAMP(22);
        const rsrc_t spb = mkrsrc(ws + WS_PB, 256 * PB_WG);
        f32x4 mine[5];
#define UNIT(u, e) acc[(u) >> 2][4 * ((u) & 3) + (e)]
#define UPK(u) cvtpk(UNIT(u, 0), UNIT(u, 1)), cvtpk(UNIT(u, 2), UNIT(u, 3))
#pragma unroll
        for (int g = 0; g < 4; ++g) {
            if (g == xq) {
#pragma unroll
                for (int n = 0; n < 5; ++n) mine[n] = (f32x4){UNIT(5 * g + n, 0), UNIT(5 * g + n, 1), UNIT(5 * g + n, 2), UNIT(5 * g + n, 3)};
            } else {
                const unsigned so = (unsigned)((bid & ~3) | g) * PB_WG + (xq * 4 + qb) * 5120;
                const u32x4 w01 = {UPK(5 * g), UPK(5 * g + 1)}, w23 = {UPK(5 * g + 2), UPK(5 * g + 3)};
                const u32x2 w4 = {UPK(5 * g + 4)};
                __builtin_amdgcn_raw_buffer_store_b128(w01, spb, vo16, so, 16);
                __builtin_amdgcn_raw_buffer_store_b128(w23, spb, vo16, so + 1024, 16);
                __builtin_amdgcn_raw_buffer_store_b64(w4, spb, lane * 8, so + 2048, 16);
            }
        }
        WAITV(0);
        if (lane == 0) __hip_atomic_store(flagp(bid, 2, qb), tag, __ATOMIC_RELAXED, __HIP_MEMORY_SCOPE_AGENT);
        STAMP(23);
        wait_flags(flagp((bid & ~3) | ((xq + 1 + (lane % 3)) & 3), 2, qb), tag);
        STAMP(24);
        {
            const rsrc_t sout = mkrsrc(out + ((size_t)b * SQ * (2 * DM) + br * DM + 160 * jj + 40 * xq) * 2, SQ * 2 * DM * 2 - (br * DM + 160 * jj + 40 * xq) * 2);
            const unsigned vop = rowq * (4 * DM) + 8 * h;
            u32x4 a01[3], a23[3]; u32x2 a4[3];
#pragma unroll
            for (int s = 0; s < 3; ++s) {
                const unsigned so = (unsigned)bid * PB_WG + (((xq + 1 + s) & 3) * 4 + qb) * 5120;
                a01[s] = __builtin_amdgcn_raw_buffer_load_b128(spb, vo16, so, 16);
                a23[s] = __builtin_amdgcn_raw_buffer_load_b128(spb, vo16, so + 1024, 16);
                a4[s] = __builtin_amdgcn_raw_buffer_load_b64(spb, lane * 8, so + 2048, 16);
            }
            auto up = [](unsigned lo, unsigned hi) { return (f32x4){__uint_as_float(lo << 16), __uint_as_float(lo & 0xffff0000u), __uint_as_float(hi << 16), __uint_as_float(hi & 0xffff0000u)}; };
#pragma unroll
            for (int s = 0; s < 3; ++s) {
                mine[0] += up(a01[s][0], a01[s][1]); mine[1] += up(a01[s][2], a01[s][3]);
                mine[2] += up(a23[s][0], a23[s][1]); mine[3] += up(a23[s][2], a23[s][3]); mine[4] += up(a4[s][0], a4[s][1]);
            }
#pragma unroll
            for (int n = 0; n < 5; ++n) __builtin_amdgcn_raw_buffer_store_b64((u32x2){cvtpk(mine[n][0], mine[n][1]), cvtpk(mine[n][2], mine[n][3])}, sout, vop, 16 * n, OUT_AUX);
        }
    }
    {
        if (xq == 0 && jj == 0 && wid == 0 && lane == 0)
            __hip_atomic_store((GAS u64*)(ws + WS_GEN + (br * NB + b) * 256), tag, __ATOMIC_RELAXED, __HIP_MEMORY_SCOPE_AGENT);
        STAMP(25);
    }
}

extern "C" void kernel_launch(void* const* d_in, const int* in_sizes, int n_in, void* d_out, int out_size,
                              void* d_ws, size_t ws_size, hipStream_t stream) {
    (void)hipFuncSetAttribute((const void*)fused_kernel, hipFuncAttributeMaxDynamicSharedMemorySize, LDS_BYTES);
    hipLaunchKernelGGL(fused_kernel, dim3(256), dim3(512), LDS_BYTES, stream,
                       (const char*)d_in[0], (const char*)d_in[1], (const char*)d_in[2], (const char*)d_in[3], (const char*)d_in[4],
                       (const char*)d_in[5], (const char*)d_in[6], (const char*)d_in[7], (const char*)d_in[8], (const char*)d_in[9],
                       (char*)d_ws + WS_SHIFT, (char*)d_out);
}
```

```cpp
#define PROBE 0
#define PROBE_G 0
#define PROBE_BID 77
#include <hip/hip_runtime.h>
#include <hip/hip_bf16.h>
#include <stdint.h>
#include <type_traits>

typedef short bf16x8 __attribute__((ext_vector_type(8)));
typedef short s16x4 __attribute__((ext_vector_type(4)));
typedef float f32x16 __attribute__((ext_vector_type(16)));
typedef float f32x4 __attribute__((ext_vector_type(4)));
typedef float f32x2 __attribute__((ext_vector_type(2)));
typedef __bf16 bf16x2_t __attribute__((ext_vector_type(2)));
typedef unsigned u32x4 __attribute__((ext_vector_type(4)));
typedef unsigned u32x2 __attribute__((ext_vector_type(2)));
typedef unsigned long long u64;
#define LAS __attribute__((address_space(3)))
#define GAS __attribute__((address_space(1)))
#define DI __device__ __forceinline__
typedef LAS char* lptr;
typedef __amdgpu_buffer_rsrc_t rsrc_t;

constexpr int NB = 8, SQ = 128, DM = 640, NH = 32, SK = 512, DH = 128;
constexpr float CEXP = 0.08838834764831845f * 1.4426950408889634f;
constexpr int SLOT = 16384;
constexpr int LDS_T = 6 * SLOT;
constexpr int WT = 20480;
constexpr int LDS_BYTES = 10 * SLOT;
constexpr size_t WS_GEN = 0, WS_FLAG = 8192, WS_OF = 1 << 20;
constexpr size_t OF_BYTES = (size_t)2 * NB * NH * 32768;
constexpr size_t WS_PB = WS_OF + OF_BYTES;
constexpr unsigned PB_WG = 4 * 8 * 3 * 1024;
#define NT_W 0
#define NT_X 0
#define NT_WO 0
#define PPRIO 2
#define PBF16 1
#define WS_SHIFT 0
#define POL 15
#define PRETOUCH 1
constexpr unsigned CODE_LINES = 180;
#define WOWARM 1
#define ROT 1
#define OUT_AUX 0
constexpr unsigned SPIN_CAP = 1u << 16;

#define MFMA32(a, b, c) __builtin_amdgcn_mfma_f32_32x32x16_bf16((a), (b), (c), 0, 0, 0)
#define WAITV(n) asm volatile("s_waitcnt vmcnt(" #n ")" ::: "memory")
#define SB() __builtin_amdgcn_sched_barrier(0)
#define BARX() do { SB(); asm volatile("s_barrier" ::: "memory"); SB(); } while (0)
#define BAR() do { SB(); asm volatile("s_waitcnt lgkmcnt(0)\n\ts_barrier" ::: "memory"); SB(); } while (0)

#define STAMP(idx) do { if ((idx) == 0) pt0 = __builtin_amdgcn_s_memrealtime(); if ((idx) == PROBE) ptx = __builtin_amdgcn_s_memrealtime(); } while (0)

DI unsigned cvtpk(float lo, float hi) {
    f32x2 v = {lo, hi};
    bf16x2_t b = __builtin_convertvector(v, bf16x2_t);
    return __builtin_bit_cast(unsigned, b);
}
DI bf16x8 pack8(const f32x16& x, int s) {
    u32x4 w = {cvtpk(x[8 * s + 0], x[8 * s + 1]), cvtpk(x[8 * s + 2], x[8 * s + 3]),
               cvtpk(x[8 * s + 4], x[8 * s + 5]), cvtpk(x[8 * s + 6], x[8 * s + 7])};
    return __builtin_bit_cast(bf16x8, w);
}
template <int NT> DI void dma4(unsigned lds, unsigned v0, unsigned v1, unsigned v2, unsigned v3, rsrc_t srd, unsigned soff) {
    if (NT) asm volatile("s_mov_b32 m0, %0\n\ts_nop 3\n\tbuffer_load_dwordx4 %1, %5, %6 offen nt lds\n\tbuffer_load_dwordx4 %2, %5, %6 offen offset:1024 nt lds\n\t"
                         "buffer_load_dwordx4 %3, %5, %6 offen offset:2048 nt lds\n\tbuffer_load_dwordx4 %4, %5, %6 offen offset:3072 nt lds"
                         :: "s"(lds), "v"(v0), "v"(v1), "v"(v2), "v"(v3), "s"(srd), "s"(soff) : "memory");
    else asm volatile("s_mov_b32 m0, %0\n\ts_nop 3\n\tbuffer_load_dwordx4 %1, %5, %6 offen lds\n\tbuffer_load_dwordx4 %2, %5, %6 offen offset:1024 lds\n\t"
                      "buffer_load_dwordx4 %3, %5, %6 offen offset:2048 lds\n\tbuffer_load_dwordx4 %4, %5, %6 offen offset:3072 lds"
                      :: "s"(lds), "v"(v0), "v"(v1), "v"(v2), "v"(v3), "s"(srd), "s"(soff) : "memory");
}
template <int NT> DI void dma1(unsigned lds, unsigned voff, rsrc_t srd, unsigned soff) {
    if (NT) asm volatile("s_mov_b32 m0, %0\n\ts_nop 3\n\tbuffer_load_dwordx4 %1, %2, %3 offen nt lds" :: "s"(lds), "v"(voff), "s"(srd), "s"(soff) : "memory");
    else asm volatile("s_mov_b32 m0, %0\n\ts_nop 3\n\tbuffer_load_dwordx4 %1, %2, %3 offen lds" :: "s"(lds), "v"(voff), "s"(srd), "s"(soff) : "memory");
}
DI void ld4_sc1(bf16x8* d, unsigned voff, rsrc_t srd, unsigned soff) {
    asm volatile("s_nop 4\n\tbuffer_load_dwordx4 %0, %4, %5, %6 offen sc1\n\tbuffer_load_dwordx4 %1, %4, %5, %6 offen offset:1024 sc1\n\t"
                 "buffer_load_dwordx4 %2, %4, %5, %6 offen offset:2048 sc1\n\tbuffer_load_dwordx4 %3, %4, %5, %6 offen offset:3072 sc1"
                 : "+v"(d[0]), "+v"(d[1]), "+v"(d[2]), "+v"(d[3]) : "v"(voff), "s"(srd), "s"(soff) : "memory");
}
constexpr unsigned BIAS = 4096;
#define WAITV_F(n, f) asm volatile("s_waitcnt vmcnt(" #n ")" : "+v"((f)[0]), "+v"((f)[1]), "+v"((f)[2]), "+v"((f)[3]) :: "memory")
DI bf16x8 lds128(lptr p) { return *(const LAS bf16x8*)p; }
DI s16x4 ldstr(lptr p) {
    typedef short v4i16_t __attribute__((ext_vector_type(4)));
    return __builtin_amdgcn_ds_read_tr16_b64_v4i16((LAS v4i16_t*)p);
}
DI bf16x8 cat(s16x4 a, s16x4 b) { return __builtin_shufflevector(a, b, 0, 1, 2, 3, 4, 5, 6, 7); }
DI float swapmax(float v) {
    auto r = __builtin_amdgcn_permlane32_swap(__float_as_uint(v), __float_as_uint(v), false, false);
    return fmaxf(__uint_as_float(r[0]), __uint_as_float(r[1]));
}
DI float swapsum(float v) {
    auto r = __builtin_amdgcn_permlane32_swap(__float_as_uint(v), __float_as_uint(v), false, false);
    return __uint_as_float(r[0]) + __uint_as_float(r[1]);
}
DI rsrc_t mkrsrc(const void* p, unsigned bytes) {
    return __builtin_amdgcn_make_buffer_rsrc((void*)p, (short)0, (int)bytes, 0x00020000);
}
DI void zero16(f32x16& v) {
#pragma unroll
    for (int r = 0; r < 16; ++r) v[r] = 0.f;
}
DI void wait_flags(const GAS u64* p, u64 tag) {
#pragma unroll 1
    for (unsigned it = 0; it < SPIN_CAP; ++it) {
        const u64 v = __hip_atomic_load(p, __ATOMIC_RELAXED, __HIP_MEMORY_SCOPE_AGENT);
        if (__all(v == tag)) break;
        __builtin_amdgcn_s_sleep(1);
    }
    asm volatile("" ::: "memory");
}

DI float max3f(float a, float b, float c) { float r; asm("v_max3_f32 %0, %1, %2, %3" : "=v"(r) : "v"(a), "v"(b), "v"(c)); return r; }
DI float fadd_s(float a, float b) { float r; asm("v_add_f32_e32 %0, %1, %2" : "=v"(r) : "v"(a), "v"(b)); return r; }
DI float fmul_s(float a, float b) { float r; asm("v_mul_f32_e32 %0, %1, %2" : "=v"(r) : "v"(a), "v"(b)); return r; }
DI float fsub_s(float a, float b) { float r; asm("v_sub_f32_e32 %0, %1, %2" : "=v"(r) : "v"(a), "v"(b)); return r; }
DI float exp_s(float a) { float r; asm("v_exp_f32_e32 %0, %1" : "=v"(r) : "v"(a)); return r; }
#define MPAD4(a) asm volatile("s_nop 7\n\ts_nop 7" : "+v"((a)[0]), "+v"((a)[1]), "+v"((a)[2]), "+v"((a)[3]))
#define PIN(x) asm volatile("" : "+v"(x))
#define PIN4(f) asm volatile("" : "+v"((f)[0]), "+v"((f)[1]), "+v"((f)[2]), "+v"((f)[3]) :: "memory")
DI lptr opaque(lptr p) { unsigned a = (unsigned)(uintptr_t)p; asm volatile("" : "+v"(a)); return (lptr)(uintptr_t)a; }
constexpr int LA = 6;
#define SM_THR 8.0f

struct QBase { lptr x0, x1, w; };
DI void qload(int i, bf16x8* wf, bf16x8* xf, const QBase& q) {
    const int s = i >> 2, d = i & 3;
    if (d == 0) xf[s] = lds128(((s & 1) ? q.x1 : q.x0) + 512 * (s >> 1));
    wf[i & 7] = cat(ldstr(q.w + 4096 * s + 512 * d), ldstr(q.w + 4096 * s + 512 * d + 256));
}
DI void qpre(bf16x8* wf, bf16x8* xf, const QBase& q) {
#pragma unroll
    for (int i = 0; i < LA; ++i) qload(i, wf, xf, q);
}
template <bool NEXT> DI void qsteps(f32x16* acc, bf16x8* wf, bf16x8* xf, const QBase& q, const QBase& qn) {
#pragma unroll
    for (int i = 0; i < 16; ++i) {
        SB();
        acc[i & 3] = MFMA32(wf[i & 7], xf[i >> 2], acc[i & 3]);
        PIN(acc[i & 3]);
        SB();
        if (NEXT && i + LA == 16) BARX();
        if (i + LA < 16) qload(i + LA, wf, xf, q); else if (NEXT) qload(i + LA - 16, wf, xf, qn);
    }
    SB();
}
template <bool FIRST = false> DI void oblock(f32x16* acc, const bf16x8* of, lptr wp_) {
    const lptr wp = opaque(wp_);
    bf16x8 wf[8];
    auto load = [&](int i) { wf[i & 7] = cat(ldstr(wp + 5120 * (i / 5) + 512 * (i % 5)), ldstr(wp + 5120 * (i / 5) + 2560 + 512 * (i % 5))); };
#pragma unroll
    for (int i = 0; i < LA; ++i) load(i);
#pragma unroll
    for (int i = 0; i < 20; ++i) {
        SB();
        if (FIRST && i < 5) { f32x16 z; zero16(z); acc[i] = MFMA32(wf[i & 7], of[0], z); }
        else acc[i % 5] = MFMA32(wf[i & 7], of[i / 5], acc[i % 5]);
        PIN(acc[i % 5]);
        SB();
        if (i + LA < 20) load(i + LA);
    }
    SB();
}
DI void aload(int i, bf16x8* fr, lptr vp, lptr kp0, lptr kp1) {
    if (i < 16) fr[i & 7] = cat(ldstr(vp + 4096 * (i >> 2) + 512 * (i & 3)), ldstr(vp + 4096 * (i >> 2) + 2048 + 512 * (i & 3)));
    else { const int s = i & 7; fr[i & 7] = lds128(((s & 1) ? kp1 : kp0) + 512 * (s >> 1) + 8192 * ((i - 16) >> 3)); }
}

__global__ __launch_bounds__(512, 2) void fused_kernel(
    const char* xA, const char* kA, const char* vA, const char* wqA, const char* woA,
    const char* xB, const char* kB, const char* vB, const char* wqB, const char* woB,
    char* ws, char* out)
{
    extern __shared__ __attribute__((aligned(16))) char smem_[];
    const lptr smem = (lptr)smem_;
    const unsigned lds0 = (unsigned)(uintptr_t)smem;
    const int tid = threadIdx.x, lane = tid & 63;
    const int wid = __builtin_amdgcn_readfirstlane(tid >> 6);
    const int bid = blockIdx.x;
    const int xg = bid & 7, ii = bid >> 3;
    const int br = xg >> 2, xq = xg & 3, b = ii >> 2, jj = ii & 3;
    const int h1 = 8 * xq + 2 * jj;
    const char* x = br ? xB : xA;
    const char* kc = br ? kB : kA;
    const char* vc = br ? vB : vA;
    const char* wq = br ? wqB : wqA;
    const char* wo = br ? woB : woA;
    const int qb = wid & 3, grp = wid >> 2, lw = wid & 3;
    const int l31 = lane & 31, h = lane >> 5;
    unsigned long long pt0 = 0, ptx = 0;
    STAMP(0);
    const char* codebase;
    asm volatile("s_getpc_b64 %0" : "=s"(codebase));

    u64 gen;
    {
        const char* gp = ws + WS_GEN + (br * NB + b) * 256;
        asm volatile("global_load_dwordx2 %0, %1, off sc1" : "=v"(gen) : "v"(gp) : "memory");
    }

    const rsrc_t sk = mkrsrc(kc + (size_t)(b * NH + h1) * SK * DH * 2 - BIAS, 2 * SK * DH * 2 + BIAS);
    const rsrc_t sv = mkrsrc(vc + (size_t)(b * NH + h1) * SK * DH * 2 - BIAS, 2 * SK * DH * 2 + BIAS);
    const rsrc_t sof = mkrsrc(ws + WS_OF + (size_t)((br * NB + b) * NH + 8 * xq) * 32768, 8 * 32768);

    const unsigned dsub = lane >> 5, dr = (lane >> 2) & 7, dsl = lane & 3;
    const unsigned xr0 = 16u * (dsl ^ ((dr >> 2) & 3)), xr1 = 16u * (dsl ^ ((2 + (dr >> 2)) & 3));
    const unsigned vk0 = BIAS + 4096 * lw + dr * 256 + dsub * 64 + xr0, vk2 = BIAS + 4096 * lw + dr * 256 + dsub * 64 + xr1;
    const unsigned vv0 = BIAS + 4096 * lw + dr * 256 + dsub * 64 + 16 * dsl;
    const unsigned ldw = lds0 + 4096 * lw;

    auto issueK = [&](int t, unsigned pair) {
        if ((POL & 1) == ((POL >> 2) & 1) ? (POL & 1) : ((POL >> (2 * br)) & 1)) dma4<1>(ldw + pair, vk0, vk0 - 896, vk2, vk2 - 896, sk, (unsigned)t * 16384u);
        else dma4<0>(ldw + pair, vk0, vk0 - 896, vk2, vk2 - 896, sk, (unsigned)t * 16384u); };
    auto issueV = [&](int t, unsigned pair) {
        if (((POL >> 1) & 1) == ((POL >> 3) & 1) ? ((POL >> 1) & 1) : ((POL >> (2 * br + 1)) & 1)) dma4<1>(ldw + pair + SLOT, vv0, vv0 - 896, vv0, vv0 - 896, sv, (unsigned)t * 16384u);
        else dma4<0>(ldw + pair + SLOT, vv0, vv0 - 896, vv0, vv0 - 896, sv, (unsigned)t * 16384u); };
    auto nextpair = [](unsigned p) { return (p == 4u * SLOT) ? 0u : p + 2u * SLOT; };

    const int G1 = (lane >> 4) & 1, tq = (lane & 15) >> 2, tp = lane & 3;
    const int sig = ((tp & 1) << 1) | (tp >> 1);
    const unsigned wqb = SLOT + 2048 * h + 64 * tq + 32 * G1 + 8 * sig;
    const unsigned rowq = 32 * qb + l31;
    const unsigned xsw = (rowq >> 2) & 3;
    const unsigned xb0 = 1024 * (rowq >> 3) + 64 * (rowq & 7) + 16 * ((0 + h) ^ xsw);
    const unsigned xb1 = 1024 * (rowq >> 3) + 64 * (rowq & 7) + 16 * ((2 + h) ^ xsw);
    const unsigned ksw = (l31 >> 2) & 3;
    const unsigned kb0 = 2048 * (l31 >> 3) + 64 * (l31 & 7) + 16 * ((0 + h) ^ ksw);
    const unsigned kb1 = 2048 * (l31 >> 3) + 64 * (l31 & 7) + 16 * ((2 + h) ^ ksw);
    const unsigned trb = 64 * (4 * h + tq) + 32 * G1 + 8 * tp;
    const unsigned vo16 = lane * 16;

    auto flagp = [&](int bb, int stage, int w) { return (GAS u64*)(ws + WS_FLAG + (size_t)((bb * 3 + stage) * 8 + w) * 64); };
    auto partner = [&](int dj) { return (bid & ~24) | (((jj + dj) & 3) << 3); };
    auto hl1 = [&](int dj) { return 2 * ((jj + dj) & 3); };

    f32x16 acc[5];
    bf16x8 qf[8], pf[4], fr[8];
    f32x16 s0, s1;
#pragma unroll
    for (int d = 0; d < 4; ++d) zero16(acc[d]);

    auto nextstage = [](unsigned st) { return st == 8u * SLOT ? 4u * SLOT : (st == 4u * SLOT ? 6u * SLOT : (st == 6u * SLOT ? 2u * SLOT : 8u * SLOT)); };
    auto qbase = [&](unsigned st) { return QBase{opaque(smem + st + xb0), opaque(smem + st + xb1), opaque(smem + st + wqb)}; };
    if (grp == 0) {
        unsigned st = 8 * SLOT;
        unsigned dummy = 0;
        {
            const unsigned line = lw * 64 + lane;
            const char* p = codebase + (line < CODE_LINES ? line : 0) * 128;
            asm volatile("global_load_dword %0, %1, off" : "+v"(dummy) : "v"(p) : "memory");
        }
        BARX();
        QBase qc = qbase(st);
        qpre(fr, pf, qc);
#pragma unroll 1
        for (int i = 0; i < 10; ++i) {
            st = nextstage(st);
            const QBase qn = qbase(st);
            qsteps<true>(acc, fr, pf, qc, qn);
            qc = qn;
        }
        WAITV(0);
        PIN(dummy);
    } else {
        const rsrc_t sx = mkrsrc(x + (size_t)b * SQ * DM * 2, SQ * DM * 2);
        const rsrc_t sw = mkrsrc(wq + (size_t)h1 * 256 - BIAS, DM * 8192 - h1 * 256 + BIAS);
        const unsigned vx0 = (32 * lw + dr) * 1280 + dsub * 64 + xr0, vx1 = (32 * lw + 8 + dr) * 1280 + dsub * 64 + xr1 - 1024;
        const unsigned vw0 = BIAS + (16 * lw + dr) * 8192 + dsub * 64 + 16 * dsl;
        const int rot = ROT ? b + 3 * jj : 0;
        constexpr unsigned T0 = 6 * SLOT, T1 = 8 * SLOT, P1 = 2 * SLOT, P2 = 4 * SLOT;
        int ci = rot >= 10 ? rot - 10 : rot;
        auto issueQn = [&](int hd, unsigned st) {
            dma4<NT_W>(ldw + st + SLOT, vw0, vw0 - 896, vw0 + 65536 - 2048, vw0 + 65536 - 2944, sw, (unsigned)ci * 524288u + 256 * hd);
            dma4<NT_X>(ldw + st, vx0, vx1, vx0 + 20480 - 2048, vx1 + 20480 - 2048, sx, 128 * ci);
            ci = ci == 9 ? 0 : ci + 1;
        };
        issueQn(0, T1);
        WAITV(0); BARX();
        issueQn(0, P2); issueQn(0, T0);
        WAITV(8);  BARX(); issueQn(0, P1);
        WAITV(8);  BARX(); issueQn(0, T1);
        WAITV(8);  BARX(); issueQn(0, P2);
        WAITV(8);  BARX(); issueQn(0, T0); issueK(0, 0); issueV(0, 0);
        WAITV(16); BARX(); issueQn(0, P1);
        WAITV(16); BARX(); issueQn(0, T1);
        WAITV(8);  BARX(); issueQn(0, P2);
        WAITV(8);  BARX(); issueQn(1, T0);
        WAITV(8);  BARX(); issueK(1, P1); issueV(1, P1);
        BARX(); issueQn(1, T1);
        const QBase q0 = qbase(T0), q1 = qbase(T1);
        WAITV(16); BARX();
        qpre(fr, pf, q0);
        issueK(2, P2);
        qsteps<false>(acc, fr, pf, q0, q0);
        WAITV(4);
        unsigned pv = P2;
#pragma unroll 1
        for (int c = 1; c < 9; c += 2) {
            unsigned pk = nextpair(pv);
            BARX();
            qpre(fr, pf, q1);
            issueQn(1, T0);
            qsteps<false>(acc, fr, pf, q1, q1);
            issueV(c + 1, pv); issueK(c + 2, pk);
            pv = nextpair(pk);
            WAITV(8);
            BARX();
            qpre(fr, pf, q0);
            issueQn(1, T1);
            qsteps<false>(acc, fr, pf, q0, q0);
            issueV(c + 2, pk); issueK(c + 3, pv);
            WAITV(8);
        }
        BARX();
        qpre(fr, pf, q1);
        qsteps<false>(acc, fr, pf, q1, q1);
    }
    STAMP(1);
    asm volatile("" : "+v"(gen));
    const u64 tag = gen + 1;

    __builtin_amdgcn_s_setprio(PPRIO);
    MPAD4(acc);
    {
        float cexp = CEXP; PIN(cexp);
#pragma unroll
        for (int d = 0; d < 4; ++d) {
#pragma unroll
            for (int r = 0; r < 16; ++r) acc[d][r] = fmul_s(acc[d][r], cexp);
            qf[2 * d] = pack8(acc[d], 0); qf[2 * d + 1] = pack8(acc[d], 1); zero16(acc[d]);
        }
    }
    if (grp == 0) BARX();
    float la = 0.f, lb = 0.f, lc = 0.f, ld = 0.f;
    {
        f32x16 negm, p0; zero16(negm); zero16(s1); zero16(p0);
        unsigned pk = grp ? 4 * SLOT : 0;
        lptr kp0 = opaque(smem + pk + kb0), kp1 = opaque(smem + pk + kb1);
#pragma unroll
        for (int i = 16; i < 16 + LA; ++i) aload(i, fr, kp0, kp0, kp1);
        const GAS u64* fp = flagp(partner(1 + (lane % 3)), 0, qb);
        u64 fv = 0;
        const int tw = grp ? 3 : -1;
        float thr = -__builtin_inff(), dmin = -__builtin_inff();
        u32x4 pw[4];
#pragma unroll 1
        for (int t = 0; t < 8; ++t) {
            if (__builtin_expect(t == tw, 0)) asm volatile("global_load_dwordx2 %0, %1, off sc1" : "+v"(fv) : "v"(fp) : "memory");
            float c0, c1;
#pragma unroll
            for (int j = 0; j < 16; ++j) {
                const int i = 16 + j;
                SB();
                if (j < 8) s0 = MFMA32(fr[i & 7], qf[j], j == 0 ? negm : s0); else s1 = MFMA32(fr[i & 7], qf[j - 8], j == 8 ? negm : s1);
                SB();
                if (i + LA < 32) aload(i + LA, fr, kp0, kp0, kp1);
                if (j < 8) {
                    la = fadd_s(la, s1[2 * j]); lb = fadd_s(lb, s1[2 * j + 1]); lc = fadd_s(lc, p0[2 * j]); ld = fadd_s(ld, p0[2 * j + 1]);
                    PIN(la); PIN(lb); PIN(lc); PIN(ld);
                }
                if (j >= 10) {
                    const int e = 2 * (j - 10);
                    p0[e] = exp_s(s0[e]); p0[e + 1] = exp_s(s0[e + 1]); PIN(p0);
                    if (j == 10) { c0 = max3f(s0[0], s0[1], s0[2]); c1 = max3f(s0[3], s0[4], s0[5]); PIN(c0); PIN(c1); }
                    if (j == 11) { c0 = max3f(c0, s0[6], s0[7]); c1 = max3f(c1, s0[8], s0[9]); PIN(c0); PIN(c1); }
                    if (j == 12) { c0 = max3f(c0, s0[10], s0[11]); PIN(c0); }
                    if (j == 13) { c1 = max3f(c1, s0[12], s0[13]); PIN(c1); }
                    if (j == 14) { c0 = max3f(c0, s0[14], s0[15]); PIN(c0); }
                    if (j == 15) { c0 = max3f(c0, c1, c1); PIN(c0); }
                    pw[e >> 3][(e >> 1) & 3] = cvtpk(p0[e], p0[e + 1]); PIN(pw[e >> 3][(e >> 1) & 3]);
                }
            }
            WAITV(0);
            if (__builtin_expect(t == tw, 0)) { PIN(fv); if (!__all(fv == tag)) wait_flags(fp, tag); }
            STAMP(2 + 2 * t);
            BARX();
            const lptr vp = opaque(smem + pk + SLOT + trb);
            pk = nextpair(pk);
            kp0 = opaque(smem + pk + kb0); kp1 = opaque(smem + pk + kb1);
#pragma unroll
            for (int i = 0; i < LA; ++i) aload(i, fr, vp, vp, vp);
            PIN(s1);
            SB();
            {
                float c2 = max3f(s1[0], s1[1], s1[2]), c3 = max3f(s1[3], s1[4], s1[5]);
                c1 = max3f(s1[6], s1[7], s1[8]); c0 = max3f(c0, s1[9], s1[10]);
                c2 = max3f(c2, s1[11], s1[12]); c3 = max3f(c3, s1[13], s1[14]); c1 = max3f(c1, s1[15], c0);
                c0 = max3f(c1, c2, c3);
                auto r_ = __builtin_amdgcn_permlane32_swap(__float_as_uint(c0), __float_as_uint(c0), false, false);
                const float mx = max3f(__uint_as_float(r_[0]), __uint_as_float(r_[1]), __uint_as_float(r_[1]));
                if (__builtin_expect(__any(!(mx <= thr)), 0)) {
                    const float d = fmaxf(mx, dmin);
                    float f = __builtin_amdgcn_exp2f(fminf(-d, 0.f));
                    asm volatile("s_nop 1" : "+v"(f));
#pragma unroll
                    for (int r = 0; r < 16; ++r) { s0[r] = fsub_s(s0[r], d); s1[r] = fsub_s(s1[r], d); negm[r] = fsub_s(negm[r], d); }
                    la = fmul_s(la, f); lb = fmul_s(lb, f); lc = fmul_s(lc, f); ld = fmul_s(ld, f);
#pragma unroll
                    for (int dd = 0; dd < 4; ++dd)
#pragma unroll
                        for (int r = 0; r < 16; ++r) acc[dd][r] = fmul_s(acc[dd][r], f);
#pragma unroll
                    for (int e = 0; e < 12; ++e) p0[e] = __builtin_amdgcn_exp2f(s0[e]);
#pragma unroll
                    for (int e = 0; e < 12; e += 2) pw[e >> 3][(e >> 1) & 3] = cvtpk(p0[e], p0[e + 1]);
                }
                thr = SM_THR; dmin = 0.f;
            }
#pragma unroll
            for (int i = 0; i < 16; ++i) {
                SB();
                acc[i & 3] = MFMA32(fr[i & 7], __builtin_bit_cast(bf16x8, pw[i >> 2]), acc[i & 3]);
                SB();
                constexpr int pairs[16] = {6, 7, 8, 9, 10, 11, -1, 12, 13, 14, 15, -1, -1, -1, -1, -1};
                const int q = pairs[i];
                if (q >= 0) {
                    if (q < 8) { p0[2 * q] = __builtin_amdgcn_exp2f(s0[2 * q]); p0[2 * q + 1] = __builtin_amdgcn_exp2f(s0[2 * q + 1]); PIN(p0); }
                    else { s1[2 * q - 16] = __builtin_amdgcn_exp2f(s1[2 * q - 16]); s1[2 * q - 15] = __builtin_amdgcn_exp2f(s1[2 * q - 15]); PIN(s1); }
                }
                aload(i + LA, fr, vp, kp0, kp1);
                if (q >= 0) {
                    pw[q >> 2][q & 3] = q < 8 ? cvtpk(p0[2 * q], p0[2 * q + 1]) : cvtpk(s1[2 * q - 16], s1[2 * q - 15]);
                    PIN(pw[q >> 2][q & 3]);
                }
            }
            SB();
            STAMP(3 + 2 * t);
        }
#pragma unroll
        for (int j = 0; j < 8; ++j) { la = fadd_s(la, s1[2 * j]); lb = fadd_s(lb, s1[2 * j + 1]); lc = fadd_s(lc, p0[2 * j]); ld = fadd_s(ld, p0[2 * j + 1]); }
    }
    if (grp == 0) { __builtin_amdgcn_s_setprio(0); BARX(); }
    STAMP(18);

    {
        float inv = __builtin_amdgcn_rcpf(swapsum((la + lb) + (lc + ld)));
        asm volatile("s_nop 1" : "+v"(inv));
        MPAD4(acc);
#pragma unroll
        for (int d = 0; d < 4; ++d) {
#pragma unroll
            for (int r = 0; r < 16; ++r) acc[d][r] = fmul_s(acc[d][r], inv);
            qf[2 * d] = pack8(acc[d], 0); qf[2 * d + 1] = pack8(acc[d], 1);
            __builtin_amdgcn_raw_buffer_store_b128(__builtin_bit_cast(u32x4, qf[2 * d]), sof, vo16, (2 * jj + grp) * 32768 + qb * 8192 + d * 2048, 16);
            __builtin_amdgcn_raw_buffer_store_b128(__builtin_bit_cast(u32x4, qf[2 * d + 1]), sof, vo16, (2 * jj + grp) * 32768 + qb * 8192 + d * 2048 + 1024, 16);
            SB();
        }
    }
    const rsrc_t swo = mkrsrc(wo + (size_t)(8 * xq * 128) * 1280 + 320 * jj - BIAS, 1024 * 1280 - 320 * jj + BIAS);
    const unsigned vwA = BIAS + (16 * lw + dr) * 1280 + dsub * 64 + 16 * dsl, vwB = BIAS + (16 * lw + dr) * 1280 + 16 * dsl + (dsub ? 8 * 1280 : 256) - 2048;
    auto issueW = [&](int hl, int half, int buf) {
        const unsigned so = (unsigned)(hl * 128 + 64 * half) * 1280u, ld = lds0 + buf * WT + 5 * lw * 1024;
        dma4<NT_WO>(ld, vwA, vwA - 896, vwB, vwA + 10240 + 64 - 3072, swo, so);
        dma1<NT_WO>(ld + 4096, vwA, swo, so + 10240 + 192);
    };
    const lptr x1 = smem + (lw < 2 ? lw * 8192 : 7 * WT + (lw - 2) * 8192) + vo16;
    if (grp) {
#pragma unroll
        for (int f = 0; f < 8; ++f) *(LAS bf16x8*)(x1 + 1024 * f) = qf[f];
        __builtin_amdgcn_s_setprio(0);
        BAR();
        BARX();
        WAITV(0);
        if (lane == 0) __hip_atomic_store(flagp(bid, 1, qb), tag, __ATOMIC_RELAXED, __HIP_MEMORY_SCOPE_AGENT);
        STAMP(19);
        issueW(hl1(3) + 1, 0, 0); issueW(hl1(3) + 1, 1, 7);
        wait_flags(flagp(partner(1 + (lane % 3)), 1, qb), tag);
        WAITV(0);
        STAMP(21);
        BARX();
    } else {
        bf16x8 cur[4], nxt[4];
#pragma unroll
        for (int k = 0; k < 4; ++k) { cur[k] = qf[k]; nxt[k] = qf[4 + k]; }
        auto issueO = [&](bf16x8* f, int blk) { ld4_sc1(f, vo16, sof, hl1(blk >> 1) * 32768 + qb * 8192 + 4096 * (blk & 1)); };
        issueW(hl1(0), 0, 5);
        BARX();
        issueW(hl1(0), 1, 6); issueW(hl1(1), 0, 7);
        issueV(10, 2 * SLOT); issueK(11, 4 * SLOT);
        BARX();
        issueV(11, 4 * SLOT); issueK(12, 0);
        WAITV(8);
        if (lane == 0) __hip_atomic_store(flagp(bid, 0, qb), tag, __ATOMIC_RELAXED, __HIP_MEMORY_SCOPE_AGENT);
        BARX();
        oblock<true>(acc, cur, smem + 5 * WT + trb);
        issueV(12, 0); issueK(13, 2 * SLOT);
        WAITV(8);
        BARX();
        issueO(cur, 2);
        issueW(hl1(1), 1, 5);
        oblock(acc, nxt, smem + 6 * WT + trb);
        issueO(nxt, 3);
        issueV(13, 2 * SLOT); issueK(14, 4 * SLOT);
        WAITV(17);
        PIN4(cur);
        oblock(acc, cur, smem + 7 * WT + trb);
        issueO(cur, 4);
        WAITV(12);
        BARX();
        unsigned wdummy = 0;
        {
            const int r = 64 + 40 * b + (lane < 40 ? lane : 0);
            const char* p = wo + (size_t)((8 * xq + hl1(1 + (r >> 7)) + 1) * 128 + (r & 127)) * 1280 + 320 * jj + (lw == 3 ? 316 : 128 * lw);
            asm volatile("global_load_dword %0, %1, off" : "+v"(wdummy) : "v"(p) : "memory");
        }
        issueW(hl1(2), 0, 6); issueW(hl1(2), 1, 7);
        PIN4(nxt);
        oblock(acc, nxt, smem + 5 * WT + trb);
        issueO(nxt, 5);
        issueV(14, 4 * SLOT); issueK(15, 0);
        WAITV(17);
        PIN(wdummy);
        BARX();
        issueW(hl1(3), 0, 5);
        PIN4(cur);
        oblock(acc, cur, smem + 6 * WT + trb);
        issueO(cur, 6);
        issueV(15, 0);
        WAITV(13);
        BARX();
        issueW(hl1(3), 1, 6); issueW(hl1(0) + 1, 0, 2); issueW(hl1(0) + 1, 1, 3);
        PIN4(nxt);
        oblock(acc, nxt, smem + 7 * WT + trb);
        issueO(nxt, 7);
        WAITV(4);
        BARX();
        issueW(hl1(1) + 1, 0, 4);
        PIN4(cur);
        oblock(acc, cur, smem + 5 * WT + trb);
        WAITV(5);
        PIN4(nxt);
        oblock(acc, nxt, smem + 6 * WT + trb);
        WAITV(0);
        BARX();
        bf16x8 of2[24];
#pragma unroll
        for (int f = 0; f < 8; ++f) of2[f] = lds128(x1 + 1024 * f);
        BAR();
        issueW(hl1(1) + 1, 1, 5); issueW(hl1(2) + 1, 0, 6); issueW(hl1(2) + 1, 1, 1);
        oblock(acc, of2, smem + 2 * WT + trb);
        oblock(acc, of2 + 4, smem + 3 * WT + trb);
        WAITV(0);
        STAMP(20);
        BARX();
        const unsigned ob = qb * 8192 + 32768;
        auto ldo = [&](int k0) {
#pragma unroll
            for (int k = k0; k < k0 + 4; ++k)
                of2[k] = __builtin_bit_cast(bf16x8, __builtin_amdgcn_raw_buffer_load_b128(sof, vo16, (unsigned)hl1(1 + (k >> 3)) * 32768 + ob + (k & 7) * 1024, 16));
        };
        ldo(0); ldo(4); SB();
        oblock(acc, of2, smem + 4 * WT + trb);       ldo(8);  ldo(12); SB();
        oblock(acc, of2 + 4, smem + 5 * WT + trb);   ldo(16); SB();
        oblock(acc, of2 + 8, smem + 6 * WT + trb);   ldo(20); SB();
        oblock(acc, of2 + 12, smem + 1 * WT + trb);
        oblock(acc, of2 + 16, smem + 0 * WT + trb);
        oblock(acc, of2 + 20, smem + 7 * WT + trb);
        STAMP(22);
        const rsrc_t spb = mkrsrc(ws + WS_PB, 256 * PB_WG);
        f32x4 mine[5];
#define UNIT(u, e) acc[(u) >> 2][4 * ((u) & 3) + (e)]
#define UPK(u) cvtpk(UNIT(u, 0), UNIT(u, 1)), cvtpk(UNIT(u, 2), UNIT(u, 3))
#pragma unroll
        for (int g = 0; g < 4; ++g) {
            if (g == xq) {
#pragma unroll
                for (int n = 0; n < 5; ++n) mine[n] = (f32x4){UNIT(5 * g + n, 0), UNIT(5 * g + n, 1), UNIT(5 * g + n, 2), UNIT(5 * g + n, 3)};
            } else {
                const unsigned so = (unsigned)((bid & ~3) | g) * PB_WG + (xq * 4 + qb) * 5120;
                const u32x4 w01 = {UPK(5 * g), UPK(5 * g + 1)}, w23 = {UPK(5 * g + 2), UPK(5 * g + 3)};
                const u32x2 w4 = {UPK(5 * g + 4)};
                __builtin_amdgcn_raw_buffer_store_b128(w01, spb, vo16, so, 16);
                __builtin_amdgcn_raw_buffer_store_b128(w23, spb, vo16, so + 1024, 16);
                __builtin_amdgcn_raw_buffer_store_b64(w4, spb, lane * 8, so + 2048, 16);
            }
        }
        WAITV(0);
        if (lane == 0) __hip_atomic_store(flagp(bid, 2, qb), tag, __ATOMIC_RELAXED, __HIP_MEMORY_SCOPE_AGENT);
        STAMP(23);
        wait_flags(flagp((bid & ~3) | ((xq + 1 + (lane % 3)) & 3), 2, qb), tag);
        STAMP(24);
        {
            const rsrc_t sout = mkrsrc(out + ((size_t)b * SQ * (2 * DM) + br * DM + 160 * jj + 40 * xq) * 2, SQ * 2 * DM * 2 - (br * DM + 160 * jj + 40 * xq) * 2);
            const unsigned vop = rowq * (4 * DM) + 8 * h;
            u32x4 a01[3], a23[3]; u32x2 a4[3];
#pragma unroll
            for (int s = 0; s < 3; ++s) {
                const unsigned so = (unsigned)bid * PB_WG + (((xq + 1 + s) & 3) * 4 + qb) * 5120;
                a01[s] = __builtin_amdgcn_raw_buffer_load_b128(spb, vo16, so, 16);
                a23[s] = __builtin_amdgcn_raw_buffer_load_b128(spb, vo16, so + 1024, 16);
                a4[s] = __builtin_amdgcn_raw_buffer_load_b64(spb, lane * 8, so + 2048, 16);
            }
            auto up = [](unsigned lo, unsigned hi) { return (f32x4){__uint_as_float(lo << 16), __uint_as_float(lo & 0xffff0000u), __uint_as_float(hi << 16), __uint_as_float(hi & 0xffff0000u)}; };
#pragma unroll
            for (int s = 0; s < 3; ++s) {
                mine[0] += up(a01[s][0], a01[s][1]); mine[1] += up(a01[s][2], a01[s][3]);
                mine[2] += up(a23[s][0], a23[s][1]); mine[3] += up(a23[s][2], a23[s][3]); mine[4] += up(a4[s][0], a4[s][1]);
            }
#pragma unroll
            for (int n = 0; n < 5; ++n) __builtin_amdgcn_raw_buffer_store_b64((u32x2){cvtpk(mine[n][0], mine[n][1]), cvtpk(mine[n][2], mine[n][3])}, sout, vop, 16 * n, OUT_AUX);
        }
    }
    {
        if (xq == 0 && jj == 0 && wid == 0 && lane == 0)
            __hip_atomic_store((GAS u64*)(ws + WS_GEN + (br * NB + b) * 256), tag, __ATOMIC_RELAXED, __HIP_MEMORY_SCOPE_AGENT);
        STAMP(25);
        if (bid == PROBE_BID && wid == 4 * PROBE_G) { const unsigned long long te = pt0 + 8000 + 4 * (ptx - pt0); while (__builtin_amdgcn_s_memrealtime() < te) __builtin_amdgcn_s_sleep(4); }
    }
}

extern "C" void kernel_launch(void* const* d_in, const int* in_sizes, int n_in, void* d_out, int out_size,
                              void* d_ws, size_t ws_size, hipStream_t stream) {
    (void)hipFuncSetAttribute((const void*)fused_kernel, hipFuncAttributeMaxDynamicSharedMemorySize, LDS_BYTES);
    hipLaunchKernelGGL(fused_kernel, dim3(256), dim3(512), LDS_BYTES, stream,
                       (const char*)d_in[0], (const char*)d_in[1], (const char*)d_in[2], (const char*)d_in[3], (const char*)d_in[4],
                       (const char*)d_in[5], (const char*)d_in[6], (const char*)d_in[7], (const char*)d_in[8], (const char*)d_in[9],
                       (char*)d_ws + WS_SHIFT, (char*)d_out);
}
```

```cpp
#include <hip/hip_runtime.h>
#include <hip/hip_bf16.h>
#include <stdint.h>
#include <type_traits>

typedef short bf16x8 __attribute__((ext_vector_type(8)));
typedef short s16x4 __attribute__((ext_vector_type(4)));
typedef float f32x16 __attribute__((ext_vector_type(16)));
typedef float f32x4 __attribute__((ext_vector_type(4)));
typedef float f32x2 __attribute__((ext_vector_type(2)));
typedef __bf16 bf16x2_t __attribute__((ext_vector_type(2)));
typedef unsigned u32x4 __attribute__((ext_vector_type(4)));
typedef unsigned u32x2 __attribute__((ext_vector_type(2)));
typedef unsigned long long u64;
#define LAS __attribute__((address_space(3)))
#define GAS __attribute__((address_space(1)))
#define DI __device__ __forceinline__
typedef LAS char* lptr;
typedef __amdgpu_buffer_rsrc_t rsrc_t;

constexpr int NB = 8, SQ = 128, DM = 640, NH = 32, SK = 512, DH = 128;
constexpr float CEXP = 0.08838834764831845f * 1.4426950408889634f;
constexpr int SLOT = 16384;
constexpr int LDS_T = 6 * SLOT;
constexpr int WT = 20480;
constexpr int LDS_BYTES = 10 * SLOT;
constexpr size_t WS_GEN = 0, WS_FLAG = 8192, WS_OF = 1 << 20;
constexpr size_t OF_BYTES = (size_t)2 * NB * NH * 32768;
constexpr size_t WS_PB = WS_OF + OF_BYTES;
constexpr unsigned PB_WG = 4 * 8 * 3 * 1024;
#define NT_W 0
#define NT_X 0
#define NT_WO 0
#define PPRIO 2
#define PBF16 1
#define WS_SHIFT 0
#define POL 15
#define PRETOUCH 1
constexpr unsigned CODE_LINES = 180;
#define WOWARM 1
#define ROT 1
#define OUT_AUX 0
#define PROBE_BID 77
constexpr unsigned SPIN_CAP = 1u << 16;

#define MFMA32(a, b, c) __builtin_amdgcn_mfma_f32_32x32x16_bf16((a), (b), (c), 0, 0, 0)
#define WAITV(n) asm volatile("s_waitcnt vmcnt(" #n ")" ::: "memory")
#define SB() __builtin_amdgcn_sched_barrier(0)
#define BARX() do { SB(); asm volatile("s_barrier" ::: "memory"); SB(); } while (0)
#define BAR() do { SB(); asm volatile("s_waitcnt lgkmcnt(0)\n\ts_barrier" ::: "memory"); SB(); } while (0)

#define STAMP(idx)

DI unsigned cvtpk(float lo, float hi) {
    f32x2 v = {lo, hi};
    bf16x2_t b = __builtin_convertvector(v, bf16x2_t);
    return __builtin_bit_cast(unsigned, b);
}
DI bf16x8 pack8(const f32x16& x, int s) {
    u32x4 w = {cvtpk(x[8 * s + 0], x[8 * s + 1]), cvtpk(x[8 * s + 2], x[8 * s + 3]),
               cvtpk(x[8 * s + 4], x[8 * s + 5]), cvtpk(x[8 * s + 6], x[8 * s + 7])};
    return __builtin_bit_cast(bf16x8, w);
}
template <int NT> DI void dma4(unsigned lds, unsigned v0, unsigned v1, unsigned v2, unsigned v3, rsrc_t srd, unsigned soff) {
    if (NT) asm volatile("s_mov_b32 m0, %0\n\ts_nop 3\n\tbuffer_load_dwordx4 %1, %5, %6 offen nt lds\n\tbuffer_load_dwordx4 %2, %5, %6 offen offset:1024 nt lds\n\t"
                         "buffer_load_dwordx4 %3, %5, %6 offen offset:2048 nt lds\n\tbuffer_load_dwordx4 %4, %5, %6 offen offset:3072 nt lds"
                         :: "s"(lds), "v"(v0), "v"(v1), "v"(v2), "v"(v3), "s"(srd), "s"(soff) : "memory");
    else asm volatile("s_mov_b32 m0, %0\n\ts_nop 3\n\tbuffer_load_dwordx4 %1, %5, %6 offen lds\n\tbuffer_load_dwordx4 %2, %5, %6 offen offset:1024 lds\n\t"
                      "buffer_load_dwordx4 %3, %5, %6 offen offset:2048 lds\n\tbuffer_load_dwordx4 %4, %5, %6 offen offset:3072 lds"
                      :: "s"(lds), "v"(v0), "v"(v1), "v"(v2), "v"(v3), "s"(srd), "s"(soff) : "memory");
}
template <int NT> DI void dma1(unsigned lds, unsigned voff, rsrc_t srd, unsigned soff) {
    if (NT) asm volatile("s_mov_b32 m0, %0\n\ts_nop 3\n\tbuffer_load_dwordx4 %1, %2, %3 offen nt lds" :: "s"(lds), "v"(voff), "s"(srd), "s"(soff) : "memory");
    else asm volatile("s_mov_b32 m0, %0\n\ts_nop 3\n\tbuffer_load_dwordx4 %1, %2, %3 offen lds" :: "s"(lds), "v"(voff), "s"(srd), "s"(soff) : "memory");
}
DI void ld4_sc1(bf16x8* d, unsigned voff, rsrc_t srd, unsigned soff) {
    asm volatile("s_nop 4\n\tbuffer_load_dwordx4 %0, %4, %5, %6 offen sc1\n\tbuffer_load_dwordx4 %1, %4, %5, %6 offen offset:1024 sc1\n\t"
                 "buffer_load_dwordx4 %2, %4, %5, %6 offen offset:2048 sc1\n\tbuffer_load_dwordx4 %3, %4, %5, %6 offen offset:3072 sc1"
                 : "+v"(d[0]), "+v"(d[1]), "+v"(d[2]), "+v"(d[3]) : "v"(voff), "s"(srd), "s"(soff) : "memory");
}
constexpr unsigned BIAS = 4096;
#define WAITV_F(n, f) asm volatile("s_waitcnt vmcnt(" #n ")" : "+v"((f)[0]), "+v"((f)[1]), "+v"((f)[2]), "+v"((f)[3]) :: "memory")
DI bf16x8 lds128(lptr p) { return *(const LAS bf16x8*)p; }
DI s16x4 ldstr(lptr p) {
    typedef short v4i16_t __attribute__((ext_vector_type(4)));
    return __builtin_amdgcn_ds_read_tr16_b64_v4i16((LAS v4i16_t*)p);
}
DI bf16x8 cat(s16x4 a, s16x4 b) { return __builtin_shufflevector(a, b, 0, 1, 2, 3, 4, 5, 6, 7); }
DI float swapmax(float v) {
    auto r = __builtin_amdgcn_permlane32_swap(__float_as_uint(v), __float_as_uint(v), false, false);
    return fmaxf(__uint_as_float(r[0]), __uint_as_float(r[1]));
}
DI float swapsum(float v) {
    auto r = __builtin_amdgcn_permlane32_swap(__float_as_uint(v), __float_as_uint(v), false, false);
    return __uint_as_float(r[0]) + __uint_as_float(r[1]);
}
DI rsrc_t mkrsrc(const void* p, unsigned bytes) {
    return __builtin_amdgcn_make_buffer_rsrc((void*)p, (short)0, (int)bytes, 0x00020000);
}
DI void zero16(f32x16& v) {
#pragma unroll
    for (int r = 0; r < 16; ++r) v[r] = 0.f;
}
DI void wait_flags(const GAS u64* p, u64 tag) {
#pragma unroll 1
    for (unsigned it = 0; it < SPIN_CAP; ++it) {
        const u64 v = __hip_atomic_load(p, __ATOMIC_RELAXED, __HIP_MEMORY_SCOPE_AGENT);
        if (__all(v == tag)) break;
        __builtin_amdgcn_s_sleep(1);
    }
    asm volatile("" ::: "memory");
}

DI float max3f(float a, float b, float c) { float r; asm("v_max3_f32 %0, %1, %2, %3" : "=v"(r) : "v"(a), "v"(b), "v"(c)); return r; }
DI float fadd_s(float a, float b) { float r; asm("v_add_f32_e32 %0, %1, %2" : "=v"(r) : "v"(a), "v"(b)); return r; }
DI float fmul_s(float a, float b) { float r; asm("v_mul_f32_e32 %0, %1, %2" : "=v"(r) : "v"(a), "v"(b)); return r; }
DI float fsub_s(float a, float b) { float r; asm("v_sub_f32_e32 %0, %1, %2" : "=v"(r) : "v"(a), "v"(b)); return r; }
DI float exp_s(float a) { float r; asm("v_exp_f32_e32 %0, %1" : "=v"(r) : "v"(a)); return r; }
#define MPAD4(a) asm volatile("s_nop 7\n\ts_nop 7" : "+v"((a)[0]), "+v"((a)[1]), "+v"((a)[2]), "+v"((a)[3]))
#define PIN(x) asm volatile("" : "+v"(x))
#define PIN4(f) asm volatile("" : "+v"((f)[0]), "+v"((f)[1]), "+v"((f)[2]), "+v"((f)[3]) :: "memory")
DI lptr opaque(lptr p) { unsigned a = (unsigned)(uintptr_t)p; asm volatile("" : "+v"(a)); return (lptr)(uintptr_t)a; }
constexpr int LA = 6;
#define SM_THR 8.0f

struct QBase { lptr x0, x1, w; };
DI void qload(int i, bf16x8* wf, bf16x8* xf, const QBase& q) {
    const int s = i >> 2, d = i & 3;
    if (d == 0) xf[s] = lds128(((s & 1) ? q.x1 : q.x0) + 512 * (s >> 1));
    wf[i & 7] = cat(ldstr(q.w + 4096 * s + 512 * d), ldstr(q.w + 4096 * s + 512 * d + 256));
}
DI void qpre(bf16x8* wf, bf16x8* xf, const QBase& q) {
#pragma unroll
    for (int i = 0; i < LA; ++i) qload(i, wf, xf, q);
}
template <bool NEXT> DI void qsteps(f32x16* acc, bf16x8* wf, bf16x8* xf, const QBase& q, const QBase& qn) {
#pragma unroll
    for (int i = 0; i < 16; ++i) {
        SB();
        acc[i & 3] = MFMA32(wf[i & 7], xf[i >> 2], acc[i & 3]);
        PIN(acc[i & 3]);
        SB();
        if (NEXT && i + LA == 16) BARX();
        if (i + LA < 16) qload(i + LA, wf, xf, q); else if (NEXT) qload(i + LA - 16, wf, xf, qn);
    }
    SB();
}
template <bool FIRST = false> DI void oblock(f32x16* acc, const bf16x8* of, lptr wp_) {
    const lptr wp = opaque(wp_);
    bf16x8 wf[8];
    auto load = [&](int i) { wf[i & 7] = cat(ldstr(wp + 5120 * (i / 5) + 512 * (i % 5)), ldstr(wp + 5120 * (i / 5) + 2560 + 512 * (i % 5))); };
#pragma unroll
    for (int i = 0; i < LA; ++i) load(i);
#pragma unroll
    for (int i = 0; i < 20; ++i) {
        SB();
        if (FIRST && i < 5) { f32x16 z; zero16(z); acc[i] = MFMA32(wf[i & 7], of[0], z); }
        else acc[i % 5] = MFMA32(wf[i & 7], of[i / 5], acc[i % 5]);
        PIN(acc[i % 5]);
        SB();
        if (i + LA < 20) load(i + LA);
    }
    SB();
}
DI void aload(int i, bf16x8* fr, lptr vp, lptr kp0, lptr kp1) {
    if (i < 16) fr[i & 7] = cat(ldstr(vp + 4096 * (i >> 2) + 512 * (i & 3)), ldstr(vp + 4096 * (i >> 2) + 2048 + 512 * (i & 3)));
    else { const int s = i & 7; fr[i & 7] = lds128(((s & 1) ? kp1 : kp0) + 512 * (s >> 1) + 8192 * ((i - 16) >> 3)); }
}

__global__ __launch_bounds__(512, 2) void fused_kernel(
    const char* xA, const char* kA, const char* vA, const char* wqA, const char* woA,
    const char* xB, const char* kB, const char* vB, const char* wqB, const char* woB,
    char* ws, char* out)
{
    extern __shared__ __attribute__((aligned(16))) char smem_[];
    const lptr smem = (lptr)smem_;
    const unsigned lds0 = (unsigned)(uintptr_t)smem;
    const int tid = threadIdx.x, lane = tid & 63;
    const int wid = __builtin_amdgcn_readfirstlane(tid >> 6);
    const int bid = blockIdx.x;
    const int xg = bid & 7, ii = bid >> 3;
    const int br = xg >> 2, xq = xg & 3, b = ii >> 2, jj = ii & 3;
    const int h1 = 8 * xq + 2 * jj;
    const char* x = br ? xB : xA;
    const char* kc = br ? kB : kA;
    const char* vc = br ? vB : vA;
    const char* wq = br ? wqB : wqA;
    const char* wo = br ? woB : woA;
    const int qb = wid & 3, grp = wid >> 2, lw = wid & 3;
    const int l31 = lane & 31, h = lane >> 5;
    STAMP(0);
    const char* codebase;
    asm volatile("s_getpc_b64 %0" : "=s"(codebase));

    u64 gen;
    {
        const char* gp = ws + WS_GEN + (br * NB + b) * 256;
        asm volatile("global_load_dwordx2 %0, %1, off sc1" : "=v"(gen) : "v"(gp) : "memory");
    }

    const rsrc_t sk = mkrsrc(kc + (size_t)(b * NH + h1) * SK * DH * 2 - BIAS, 2 * SK * DH * 2 + BIAS);
    const rsrc_t sv = mkrsrc(vc + (size_t)(b * NH + h1) * SK * DH * 2 - BIAS, 2 * SK * DH * 2 + BIAS);
    const rsrc_t sof = mkrsrc(ws + WS_OF + (size_t)((br * NB + b) * NH + 8 * xq) * 32768, 8 * 32768);

    const unsigned dsub = lane >> 5, dr = (lane >> 2) & 7, dsl = lane & 3;
    const unsigned xr0 = 16u * (dsl ^ ((dr >> 2) & 3)), xr1 = 16u * (dsl ^ ((2 + (dr >> 2)) & 3));
    const unsigned vk0 = BIAS + 4096 * lw + dr * 256 + dsub * 64 + xr0, vk2 = BIAS + 4096 * lw + dr * 256 + dsub * 64 + xr1;
    const unsigned vv0 = BIAS + 4096 * lw + dr * 256 + dsub * 64 + 16 * dsl;
    const unsigned ldw = lds0 + 4096 * lw;

    auto issueK = [&](int t, unsigned pair) {
        if ((POL & 1) == ((POL >> 2) & 1) ? (POL & 1) : ((POL >> (2 * br)) & 1)) dma4<1>(ldw + pair, vk0, vk0 - 896, vk2, vk2 - 896, sk, (unsigned)t * 16384u);
        else dma4<0>(ldw + pair, vk0, vk0 - 896, vk2, vk2 - 896, sk, (unsigned)t * 16384u); };
    auto issueV = [&](int t, unsigned pair) {
        if (((POL >> 1) & 1) == ((POL >> 3) & 1) ? ((POL >> 1) & 1) : ((POL >> (2 * br + 1)) & 1)) dma4<1>(ldw + pair + SLOT, vv0, vv0 - 896, vv0, vv0 - 896, sv, (unsigned)t * 16384u);
        else dma4<0>(ldw + pair + SLOT, vv0, vv0 - 896, vv0, vv0 - 896, sv, (unsigned)t * 16384u); };
    auto nextpair = [](unsigned p) { return (p == 4u * SLOT) ? 0u : p + 2u * SLOT; };

    const int G1 = (lane >> 4) & 1, tq = (lane & 15) >> 2, tp = lane & 3;
    const int sig = ((tp & 1) << 1) | (tp >> 1);
    const unsigned wqb = SLOT + 2048 * h + 64 * tq + 32 * G1 + 8 * sig;
    const unsigned rowq = 32 * qb + l31;
    const unsigned xsw = (rowq >> 2) & 3;
    const unsigned xb0 = 1024 * (rowq >> 3) + 64 * (rowq & 7) + 16 * ((0 + h) ^ xsw);
    const unsigned xb1 = 1024 * (rowq >> 3) + 64 * (rowq & 7) + 16 * ((2 + h) ^ xsw);
    const unsigned ksw = (l31 >> 2) & 3;
    const unsigned kb0 = 2048 * (l31 >> 3) + 64 * (l31 & 7) + 16 * ((0 + h) ^ ksw);
    const unsigned kb1 = 2048 * (l31 >> 3) + 64 * (l31 & 7) + 16 * ((2 + h) ^ ksw);
    const unsigned trb = 64 * (4 * h + tq) + 32 * G1 + 8 * tp;
    const unsigned vo16 = lane * 16;

    auto flagp = [&](int bb, int stage, int w) { return (GAS u64*)(ws + WS_FLAG + (size_t)((bb * 3 + stage) * 8 + w) * 64); };
    auto partner = [&](int dj) { return (bid & ~24) | (((jj + dj) & 3) << 3); };
    auto hl1 = [&](int dj) { return 2 * ((jj + dj) & 3); };

    f32x16 acc[5];
    bf16x8 qf[8], pf[4], fr[8];
    f32x16 s0, s1;
#pragma unroll
    for (int d = 0; d < 4; ++d) zero16(acc[d]);

    auto nextstage = [](unsigned st) { return st == 8u * SLOT ? 4u * SLOT : (st == 4u * SLOT ? 6u * SLOT : (st == 6u * SLOT ? 2u * SLOT : 8u * SLOT)); };
    auto qbase = [&](unsigned st) { return QBase{opaque(smem + st + xb0), opaque(smem + st + xb1), opaque(smem + st + wqb)}; };
    if (grp == 0) {
        unsigned st = 8 * SLOT;
        unsigned dummy = 0;
        {
            const unsigned line = lw * 64 + lane;
            const char* p = codebase + (line < CODE_LINES ? line : 0) * 128;
            asm volatile("global_load_dword %0, %1, off" : "+v"(dummy) : "v"(p) : "memory");
        }
        BARX();
        QBase qc = qbase(st);
        qpre(fr, pf, qc);
#pragma unroll 1
        for (int i = 0; i < 10; ++i) {
            st = nextstage(st);
            const QBase qn = qbase(st);
            qsteps<true>(acc, fr, pf, qc, qn);
            qc = qn;
        }
        WAITV(0);
        PIN(dummy);
    } else {
        const rsrc_t sx = mkrsrc(x + (size_t)b * SQ * DM * 2, SQ * DM * 2);
        const rsrc_t sw = mkrsrc(wq + (size_t)h1 * 256 - BIAS, DM * 8192 - h1 * 256 + BIAS);
        const unsigned vx0 = (32 * lw + dr) * 1280 + dsub * 64 + xr0, vx1 = (32 * lw + 8 + dr) * 1280 + dsub * 64 + xr1 - 1024;
        const unsigned vw0 = BIAS + (16 * lw + dr) * 8192 + dsub * 64 + 16 * dsl;
        const int rot = ROT ? b + 3 * jj : 0;
        constexpr unsigned T0 = 6 * SLOT, T1 = 8 * SLOT, P1 = 2 * SLOT, P2 = 4 * SLOT;
        int ci = rot >= 10 ? rot - 10 : rot;
        auto issueQn = [&](int hd, unsigned st) {
            dma4<NT_W>(ldw + st + SLOT, vw0, vw0 - 896, vw0 + 65536 - 2048, vw0 + 65536 - 2944, sw, (unsigned)ci * 524288u + 256 * hd);
            dma4<NT_X>(ldw + st, vx0, vx1, vx0 + 20480 - 2048, vx1 + 20480 - 2048, sx, 128 * ci);
            ci = ci == 9 ? 0 : ci + 1;
        };
        issueQn(0, T1);
        WAITV(0); BARX();
        issueQn(0, P2); issueQn(0, T0);
        WAITV(8);  BARX(); issueQn(0, P1);
        WAITV(8);  BARX(); issueQn(0, T1);
        WAITV(8);  BARX(); issueQn(0, P2);
        WAITV(8);  BARX(); issueQn(0, T0); issueK(0, 0); issueV(0, 0);
        WAITV(16); BARX(); issueQn(0, P1);
        WAITV(16); BARX(); issueQn(0, T1);
        WAITV(8);  BARX(); issueQn(0, P2);
        WAITV(8);  BARX(); issueQn(1, T0);
        WAITV(8);  BARX(); issueK(1, P1); issueV(1, P1);
        BARX(); issueQn(1, T1);
        const QBase q0 = qbase(T0), q1 = qbase(T1);
        WAITV(16); BARX();
        qpre(fr, pf, q0);
        issueK(2, P2);
        qsteps<false>(acc, fr, pf, q0, q0);
        WAITV(4);
        unsigned pv = P2;
#pragma unroll 1
        for (int c = 1; c < 9; c += 2) {
            unsigned pk = nextpair(pv);
            BARX();
            qpre(fr, pf, q1);
            issueQn(1, T0);
            qsteps<false>(acc, fr, pf, q1, q1);
            issueV(c + 1, pv); issueK(c + 2, pk);
            pv = nextpair(pk);
            WAITV(8);
            BARX();
            qpre(fr, pf, q0);
            issueQn(1, T1);
            qsteps<false>(acc, fr, pf, q0, q0);
            issueV(c + 2, pk); issueK(c + 3, pv);
            WAITV(8);
        }
        BARX();
        qpre(fr, pf, q1);
        qsteps<false>(acc, fr, pf, q1, q1);
    }
    STAMP(1);
    asm volatile("" : "+v"(gen));
    const u64 tag = gen + 1;

    __builtin_amdgcn_s_setprio(PPRIO);
    MPAD4(acc);
    {
        float cexp = CEXP; PIN(cexp);
#pragma unroll
        for (int d = 0; d < 4; ++d) {
#pragma unroll
            for (int r = 0; r < 16; ++r) acc[d][r] = fmul_s(acc[d][r], cexp);
            qf[2 * d] = pack8(acc[d], 0); qf[2 * d + 1] = pack8(acc[d], 1); zero16(acc[d]);
        }
    }
    if (grp == 0) BARX();
    float la = 0.f, lb = 0.f, lc = 0.f, ld = 0.f;
    {
        f32x16 negm, p0; zero16(negm); zero16(s1); zero16(p0);
        unsigned pk = grp ? 4 * SLOT : 0;
        lptr kp0 = opaque(smem + pk + kb0), kp1 = opaque(smem + pk + kb1);
#pragma unroll
        for (int i = 16; i < 16 + LA; ++i) aload(i, fr, kp0, kp0, kp1);
        const GAS u64* fp = flagp(partner(1 + (lane % 3)), 0, qb);
        u64 fv = 0;
        const int tw = grp ? 3 : -1;
        float thr = -__builtin_inff(), dmin = -__builtin_inff();
        u32x4 pw[4];
#pragma unroll 1
        for (int t = 0; t < 8; ++t) {
            if (__builtin_expect(t == tw, 0)) asm volatile("global_load_dwordx2 %0, %1, off sc1" : "+v"(fv) : "v"(fp) : "memory");
            float c0, c1;
#pragma unroll
            for (int j = 0; j < 16; ++j) {
                const int i = 16 + j;
                SB();
                if (j < 8) s0 = MFMA32(fr[i & 7], qf[j], j == 0 ? negm : s0); else s1 = MFMA32(fr[i & 7], qf[j - 8], j == 8 ? negm : s1);
                SB();
                if (i + LA < 32) aload(i + LA, fr, kp0, kp0, kp1);
                if (j < 8) {
                    la = fadd_s(la, s1[2 * j]); lb = fadd_s(lb, s1[2 * j + 1]); lc = fadd_s(lc, p0[2 * j]); ld = fadd_s(ld, p0[2 * j + 1]);
                    PIN(la); PIN(lb); PIN(lc); PIN(ld);
                }
                if (j >= 10) {
                    const int e = 2 * (j - 10);
                    p0[e] = exp_s(s0[e]); p0[e + 1] = exp_s(s0[e + 1]); PIN(p0);
                    if (j == 10) { c0 = max3f(s0[0], s0[1], s0[2]); c1 = max3f(s0[3], s0[4], s0[5]); PIN(c0); PIN(c1); }
                    if (j == 11) { c0 = max3f(c0, s0[6], s0[7]); c1 = max3f(c1, s0[8], s0[9]); PIN(c0); PIN(c1); }
                    if (j == 12) { c0 = max3f(c0, s0[10], s0[11]); PIN(c0); }
                    if (j == 13) { c1 = max3f(c1, s0[12], s0[13]); PIN(c1); }
                    if (j == 14) { c0 = max3f(c0, s0[14], s0[15]); PIN(c0); }
                    if (j == 15) { c0 = max3f(c0, c1, c1); PIN(c0); }
                    pw[e >> 3][(e >> 1) & 3] = cvtpk(p0[e], p0[e + 1]); PIN(pw[e >> 3][(e >> 1) & 3]);
                }
            }
            WAITV(0);
            if (__builtin_expect(t == tw, 0)) { PIN(fv); if (!__all(fv == tag)) wait_flags(fp, tag); }
            STAMP(2 + 2 * t);
            BARX();
            const lptr vp = opaque(smem + pk + SLOT + trb);
            pk = nextpair(pk);
            kp0 = opaque(smem + pk + kb0); kp1 = opaque(smem + pk + kb1);
#pragma unroll
            for (int i = 0; i < LA; ++i) aload(i, fr, vp, vp, vp);
            PIN(s1);
            SB();
            {
                float c2 = max3f(s1[0], s1[1], s1[2]), c3 = max3f(s1[3], s1[4], s1[5]);
                c1 = max3f(s1[6], s1[7], s1[8]); c0 = max3f(c0, s1[9], s1[10]);
                c2 = max3f(c2, s1[11], s1[12]); c3 = max3f(c3, s1[13], s1[14]); c1 = max3f(c1, s1[15], c0);
                c0 = max3f(c1, c2, c3);
                auto r_ = __builtin_amdgcn_permlane32_swap(__float_as_uint(c0), __float_as_uint(c0), false, false);
                const float mx = max3f(__uint_as_float(r_[0]), __uint_as_float(r_[1]), __uint_as_float(r_[1]));
                if (__builtin_expect(__any(!(mx <= thr)), 0)) {
                    const float d = fmaxf(mx, dmin);
                    float f = __builtin_amdgcn_exp2f(fminf(-d, 0.f));
                    asm volatile("s_nop 1" : "+v"(f));
#pragma unroll
                    for (int r = 0; r < 16; ++r) { s0[r] = fsub_s(s0[r], d); s1[r] = fsub_s(s1[r], d); negm[r] = fsub_s(negm[r], d); }
                    la = fmul_s(la, f); lb = fmul_s(lb, f); lc = fmul_s(lc, f); ld = fmul_s(ld, f);
#pragma unroll
                    for (int dd = 0; dd < 4; ++dd)
#pragma unroll
                        for (int r = 0; r < 16; ++r) acc[dd][r] = fmul_s(acc[dd][r], f);
#pragma unroll
                    for (int e = 0; e < 12; ++e) p0[e] = __builtin_amdgcn_exp2f(s0[e]);
#pragma unroll
                    for (int e = 0; e < 12; e += 2) pw[e >> 3][(e >> 1) & 3] = cvtpk(p0[e], p0[e + 1]);
                }
                thr = SM_THR; dmin = 0.f;
            }
#pragma unroll
            for (int i = 0; i < 16; ++i) {
                SB();
                acc[i & 3] = MFMA32(fr[i & 7], __builtin_bit_cast(bf16x8, pw[i >> 2]), acc[i & 3]);
                SB();
                constexpr int pairs[16] = {6, 7, 8, 9, 10, 11, -1, 12, 13, 14, 15, -1, -1, -1, -1, -1};
                const int q = pairs[i];
                if (q >= 0) {
                    if (q < 8) { p0[2 * q] = __builtin_amdgcn_exp2f(s0[2 * q]); p0[2 * q + 1] = __builtin_amdgcn_exp2f(s0[2 * q + 1]); PIN(p0); }
                    else { s1[2 * q - 16] = __builtin_amdgcn_exp2f(s1[2 * q - 16]); s1[2 * q - 15] = __builtin_amdgcn_exp2f(s1[2 * q - 15]); PIN(s1); }
                }
                aload(i + LA, fr, vp, kp0, kp1);
                if (q >= 0) {
                    pw[q >> 2][q & 3] = q < 8 ? cvtpk(p0[2 * q], p0[2 * q + 1]) : cvtpk(s1[2 * q - 16], s1[2 * q - 15]);
                    PIN(pw[q >> 2][q & 3]);
                }
            }
            SB();
            STAMP(3 + 2 * t);
        }
#pragma unroll
        for (int j = 0; j < 8; ++j) { la = fadd_s(la, s1[2 * j]); lb = fadd_s(lb, s1[2 * j + 1]); lc = fadd_s(lc, p0[2 * j]); ld = fadd_s(ld, p0[2 * j + 1]); }
    }
    if (grp == 0) { __builtin_amdgcn_s_setprio(0); BARX(); }
    STAMP(18);

    {
        float inv = __builtin_amdgcn_rcpf(swapsum((la + lb) + (lc + ld)));
        asm volatile("s_nop 1" : "+v"(inv));
        MPAD4(acc);
#pragma unroll
        for (int d = 0; d < 4; ++d) {
#pragma unroll
            for (int r = 0; r < 16; ++r) acc[d][r] = fmul_s(acc[d][r], inv);
            qf[2 * d] = pack8(acc[d], 0); qf[2 * d + 1] = pack8(acc[d], 1);
            __builtin_amdgcn_raw_buffer_store_b128(__builtin_bit_cast(u32x4, qf[2 * d]), sof, vo16, (2 * jj + grp) * 32768 + qb * 8192 + d * 2048, 16);
            __builtin_amdgcn_raw_buffer_store_b128(__builtin_bit_cast(u32x4, qf[2 * d + 1]), sof, vo16, (2 * jj + grp) * 32768 + qb * 8192 + d * 2048 + 1024, 16);
            SB();
        }
    }
    const rsrc_t swo = mkrsrc(wo + (size_t)(8 * xq * 128) * 1280 + 320 * jj - BIAS, 1024 * 1280 - 320 * jj + BIAS);
    const unsigned vwA = BIAS + (16 * lw + dr) * 1280 + dsub * 64 + 16 * dsl, vwB = BIAS + (16 * lw + dr) * 1280 + 16 * dsl + (dsub ? 8 * 1280 : 256) - 2048;
    auto issueW = [&](int hl, int half, int buf) {
        const unsigned so = (unsigned)(hl * 128 + 64 * half) * 1280u, ld = lds0 + buf * WT + 5 * lw * 1024;
        dma4<NT_WO>(ld, vwA, vwA - 896, vwB, vwA + 10240 + 64 - 3072, swo, so);
        dma1<NT_WO>(ld + 4096, vwA, swo, so + 10240 + 192);
    };
    const lptr x1 = smem + (lw < 2 ? lw * 8192 : 7 * WT + (lw - 2) * 8192) + vo16;
    if (grp) {
#pragma unroll
        for (int f = 0; f < 8; ++f) *(LAS bf16x8*)(x1 + 1024 * f) = qf[f];
        __builtin_amdgcn_s_setprio(0);
        BAR();
        WAITV(0);
        if (lane == 0) __hip_atomic_store(flagp(bid, 1, qb), tag, __ATOMIC_RELAXED, __HIP_MEMORY_SCOPE_AGENT);
        STAMP(19);
        wait_flags(flagp(partner(1 + (lane % 3)), 1, qb), tag);
        STAMP(21);
        BARX();
        issueW(hl1(3) + 1, 0, 0); issueW(hl1(3) + 1, 1, 7);
        WAITV(0);
        BARX();
    } else {
        bf16x8 cur[4], nxt[4];
#pragma unroll
        for (int k = 0; k < 4; ++k) { cur[k] = qf[k]; nxt[k] = qf[4 + k]; }
        auto issueO = [&](bf16x8* f, int blk) { ld4_sc1(f, vo16, sof, hl1(blk >> 1) * 32768 + qb * 8192 + 4096 * (blk & 1)); };
        issueW(hl1(0), 0, 5);
        BARX();
        issueW(hl1(0), 1, 6); issueW(hl1(1), 0, 7);
        issueV(10, 2 * SLOT); issueK(11, 4 * SLOT);
        WAITV(18);
        if (lane == 0) __hip_atomic_store(flagp(bid, 0, qb), tag, __ATOMIC_RELAXED, __HIP_MEMORY_SCOPE_AGENT);
        BARX();
        oblock<true>(acc, cur, smem + 5 * WT + trb);
        issueV(11, 4 * SLOT); issueK(12, 0);
        unsigned wdummy = 0;
        {
            const int r = 64 + 40 * b + (lane < 40 ? lane : 0);
            const char* p = wo + (size_t)((8 * xq + hl1(1 + (r >> 7)) + 1) * 128 + (r & 127)) * 1280 + 320 * jj + (lw == 3 ? 316 : 128 * lw);
            asm volatile("global_load_dword %0, %1, off" : "+v"(wdummy) : "v"(p) : "memory");
        }
        WAITV(9);
        BARX();
        issueW(hl1(1), 1, 5);
        oblock(acc, nxt, smem + 6 * WT + trb);
        issueV(12, 0); issueK(13, 2 * SLOT);
        WAITV(13);
        PIN(wdummy);
        BARX();
        issueO(cur, 2); issueO(nxt, 3);
        issueW(hl1(2), 0, 6);
        issueV(13, 2 * SLOT); issueK(14, 4 * SLOT);
        WAITV(17);
        PIN4(cur);
        oblock(acc, cur, smem + 7 * WT + trb);
        issueO(cur, 4);
        WAITV(17);
        BARX();
        issueW(hl1(2), 1, 7);
        PIN4(nxt);
        oblock(acc, nxt, smem + 5 * WT + trb);
        issueO(nxt, 5);
        issueV(14, 4 * SLOT); issueK(15, 0);
        WAITV(17);
        BARX();
        issueW(hl1(3), 0, 5);
        PIN4(cur);
        oblock(acc, cur, smem + 6 * WT + trb);
        issueO(cur, 6);
        issueV(15, 0);
        WAITV(13);
        BARX();
        issueW(hl1(3), 1, 6); issueW(hl1(0) + 1, 0, 2); issueW(hl1(0) + 1, 1, 3);
        PIN4(nxt);
        oblock(acc, nxt, smem + 7 * WT + trb);
        issueO(nxt, 7);
        WAITV(14);
        BARX();
        issueW(hl1(1) + 1, 0, 4);
        PIN4(cur);
        oblock(acc, cur, smem + 5 * WT + trb);
        WAITV(5);
        PIN4(nxt);
        oblock(acc, nxt, smem + 6 * WT + trb);
        BARX();
        bf16x8 of2[24];
#pragma unroll
        for (int f = 0; f < 8; ++f) of2[f] = lds128(x1 + 1024 * f);
        issueW(hl1(1) + 1, 1, 5); issueW(hl1(2) + 1, 0, 6); issueW(hl1(2) + 1, 1, 1);
        oblock(acc, of2, smem + 2 * WT + trb);
        oblock(acc, of2 + 4, smem + 3 * WT + trb);
        WAITV(0);
        STAMP(20);
        BARX();
        const unsigned ob = qb * 8192 + 32768;
        auto ldo = [&](int k0) {
#pragma unroll
            for (int k = k0; k < k0 + 4; ++k)
                of2[k] = __builtin_bit_cast(bf16x8, __builtin_amdgcn_raw_buffer_load_b128(sof, vo16, (unsigned)hl1(1 + (k >> 3)) * 32768 + ob + (k & 7) * 1024, 16));
        };
        ldo(0); ldo(4); SB();
        oblock(acc, of2, smem + 4 * WT + trb);       ldo(8);  ldo(12); SB();
        oblock(acc, of2 + 4, smem + 5 * WT + trb);   ldo(16); SB();
        oblock(acc, of2 + 8, smem + 6 * WT + trb);   ldo(20); SB();
        oblock(acc, of2 + 12, smem + 1 * WT + trb);
        BARX();
        oblock(acc, of2 + 16, smem + 0 * WT + trb);
        oblock(acc, of2 + 20, smem + 7 * WT + trb);
        STAMP(22);
        const rsrc_t spb = mkrsrc(ws + WS_PB, 256 * PB_WG);
        f32x4 mine[5];
#define UNIT(u, e) acc[(u) >> 2][4 * ((u) & 3) + (e)]
#define UPK(u) cvtpk(UNIT(u, 0), UNIT(u, 1)), cvtpk(UNIT(u, 2), UNIT(u, 3))
#pragma unroll
        for (int g = 0; g < 4; ++g) {
            if (g == xq) {
#pragma unroll
                for (int n = 0; n < 5; ++n) mine[n] = (f32x4){UNIT(5 * g + n, 0), UNIT(5 * g + n, 1), UNIT(5 * g + n, 2), UNIT(5 * g + n, 3)};
            } else {
                const unsigned so = (unsigned)((bid & ~3) | g) * PB_WG + (xq * 4 + qb) * 5120;
                const u32x4 w01 = {UPK(5 * g), UPK(5 * g + 1)}, w23 = {UPK(5 * g + 2), UPK(5 * g + 3)};
                const u32x2 w4 = {UPK(5 * g + 4)};
                __builtin_amdgcn_raw_buffer_store_b128(w01, spb, vo16, so, 16);
                __builtin_amdgcn_raw_buffer_store_b128(w23, spb, vo16, so + 1024, 16);
                __builtin_amdgcn_raw_buffer_store_b64(w4, spb, lane * 8, so + 2048, 16);
            }
        }
        WAITV(0);
        if (lane == 0) __hip_atomic_store(flagp(bid, 2, qb), tag, __ATOMIC_RELAXED, __HIP_MEMORY_SCOPE_AGENT);
        STAMP(23);
        wait_flags(flagp((bid & ~3) | ((xq + 1 + (lane % 3)) & 3), 2, qb), tag);
        STAMP(24);
        {
            const rsrc_t sout = mkrsrc(out + ((size_t)b * SQ * (2 * DM) + br * DM + 160 * jj + 40 * xq) * 2, SQ * 2 * DM * 2 - (br * DM + 160 * jj + 40 * xq) * 2);
            const unsigned vop = rowq * (4 * DM) + 8 * h;
            u32x4 a01[3], a23[3]; u32x2 a4[3];
#pragma unroll
            for (int s = 0; s < 3; ++s) {
                const unsigned so = (unsigned)bid * PB_WG + (((xq + 1 + s) & 3) * 4 + qb) * 5120;
                a01[s] = __builtin_amdgcn_raw_buffer_load_b128(spb, vo16, so, 16);
                a23[s] = __builtin_amdgcn_raw_buffer_load_b128(spb, vo16, so + 1024, 16);
                a4[s] = __builtin_amdgcn_raw_buffer_load_b64(spb, lane * 8, so + 2048, 16);
            }
            auto up = [](unsigned lo, unsigned hi) { return (f32x4){__uint_as_float(lo << 16), __uint_as_float(lo & 0xffff0000u), __uint_as_float(hi << 16), __uint_as_float(hi & 0xffff0000u)}; };
#pragma unroll
            for (int s = 0; s < 3; ++s) {
                mine[0] += up(a01[s][0], a01[s][1]); mine[1] += up(a01[s][2], a01[s][3]);
                mine[2] += up(a23[s][0], a23[s][1]); mine[3] += up(a23[s][2], a23[s][3]); mine[4] += up(a4[s][0], a4[s][1]);
            }
#pragma unroll
            for (int n = 0; n < 5; ++n) __builtin_amdgcn_raw_buffer_store_b64((u32x2){cvtpk(mine[n][0], mine[n][1]), cvtpk(mine[n][2], mine[n][3])}, sout, vop, 16 * n, OUT_AUX);
        }
    }
    {
        if (xq == 0 && jj == 0 && wid == 0 && lane == 0)
            __hip_atomic_store((GAS u64*)(ws + WS_GEN + (br * NB + b) * 256), tag, __ATOMIC_RELAXED, __HIP_MEMORY_SCOPE_AGENT);
        STAMP(25);
    }
}

extern "C" void kernel_launch(void* const* d_in, const int* in_sizes, int n_in, void* d_out, int out_size,
                              void* d_ws, size_t ws_size, hipStream_t stream) {
    (void)hipFuncSetAttribute((const void*)fused_kernel, hipFuncAttributeMaxDynamicSharedMemorySize, LDS_BYTES);
    hipLaunchKernelGGL(fused_kernel, dim3(256), dim3(512), LDS_BYTES, stream,
                       (const char*)d_in[0], (const char*)d_in[1], (const char*)d_in[2], (const char*)d_in[3], (const char*)d_in[4],
                       (const char*)d_in[5], (const char*)d_in[6], (const char*)d_in[7], (const char*)d_in[8], (const char*)d_in[9],
                       (char*)d_ws + WS_SHIFT, (char*)d_out);
}
```

```cpp
#define PPRIO 1
#include <hip/hip_runtime.h>
#include <hip/hip_bf16.h>
#include <stdint.h>
#include <type_traits>

typedef short bf16x8 __attribute__((ext_vector_type(8)));
typedef short s16x4 __attribute__((ext_vector_type(4)));
typedef float f32x16 __attribute__((ext_vector_type(16)));
typedef float f32x4 __attribute__((ext_vector_type(4)));
typedef float f32x2 __attribute__((ext_vector_type(2)));
typedef __bf16 bf16x2_t __attribute__((ext_vector_type(2)));
typedef unsigned u32x4 __attribute__((ext_vector_type(4)));
typedef unsigned u32x2 __attribute__((ext_vector_type(2)));
typedef unsigned long long u64;
#define LAS __attribute__((address_space(3)))
#define GAS __attribute__((address_space(1)))
#define DI __device__ __forceinline__
typedef LAS char* lptr;
typedef __amdgpu_buffer_rsrc_t rsrc_t;

constexpr int NB = 8, SQ = 128, DM = 640, NH = 32, SK = 512, DH = 128;
constexpr float CEXP = 0.08838834764831845f * 1.4426950408889634f;
constexpr int SLOT = 16384;
constexpr int LDS_T = 6 * SLOT;
constexpr int WT = 20480;
constexpr int LDS_BYTES = 10 * SLOT;
constexpr size_t WS_GEN = 0, WS_FLAG = 8192, WS_OF = 1 << 20;
constexpr size_t OF_BYTES = (size_t)2 * NB * NH * 32768;
constexpr size_t WS_PB = WS_OF + OF_BYTES;
constexpr unsigned PB_WG = 4 * 8 * 3 * 1024;
#define NT_W 0
#define NT_X 0
#define NT_WO 0
#define PBF16 1
#define WS_SHIFT 0
#define POL 15
#define PRETOUCH 1
constexpr unsigned CODE_LINES = 180;
#define WOWARM 1
#define ROT 1
#define OUT_AUX 0
#define PROBE_BID 77
constexpr unsigned SPIN_CAP = 1u << 16;

#define MFMA32(a, b, c) __builtin_amdgcn_mfma_f32_32x32x16_bf16((a), (b), (c), 0, 0, 0)
#define WAITV(n) asm volatile("s_waitcnt vmcnt(" #n ")" ::: "memory")
#define SB() __builtin_amdgcn_sched_barrier(0)
#define BARX() do { SB(); asm volatile("s_barrier" ::: "memory"); SB(); } while (0)
#define BAR() do { SB(); asm volatile("s_waitcnt lgkmcnt(0)\n\ts_barrier" ::: "memory"); SB(); } while (0)

#define STAMP(idx)

DI unsigned cvtpk(float lo, float hi) {
    f32x2 v = {lo, hi};
    bf16x2_t b = __builtin_convertvector(v, bf16x2_t);
    return __builtin_bit_cast(unsigned, b);
}
DI bf16x8 pack8(const f32x16& x, int s) {
    u32x4 w = {cvtpk(x[8 * s + 0], x[8 * s + 1]), cvtpk(x[8 * s + 2], x[8 * s + 3]),
               cvtpk(x[8 * s + 4], x[8 * s + 5]), cvtpk(x[8 * s + 6], x[8 * s + 7])};
    return __builtin_bit_cast(bf16x8, w);
}
template <int NT> DI void dma4(unsigned lds, unsigned v0, unsigned v1, unsigned v2, unsigned v3, rsrc_t srd, unsigned soff) {
    if (NT) asm volatile("s_mov_b32 m0, %0\n\ts_nop 3\n\tbuffer_load_dwordx4 %1, %5, %6 offen nt lds\n\tbuffer_load_dwordx4 %2, %5, %6 offen offset:1024 nt lds\n\t"
                         "buffer_load_dwordx4 %3, %5, %6 offen offset:2048 nt lds\n\tbuffer_load_dwordx4 %4, %5, %6 offen offset:3072 nt lds"
                         :: "s"(lds), "v"(v0), "v"(v1), "v"(v2), "v"(v3), "s"(srd), "s"(soff) : "memory");
    else asm volatile("s_mov_b32 m0, %0\n\ts_nop 3\n\tbuffer_load_dwordx4 %1, %5, %6 offen lds\n\tbuffer_load_dwordx4 %2, %5, %6 offen offset:1024 lds\n\t"
                      "buffer_load_dwordx4 %3, %5, %6 offen offset:2048 lds\n\tbuffer_load_dwordx4 %4, %5, %6 offen offset:3072 lds"
                      :: "s"(lds), "v"(v0), "v"(v1), "v"(v2), "v"(v3), "s"(srd), "s"(soff) : "memory");
}
template <int NT> DI void dma1(unsigned lds, unsigned voff, rsrc_t srd, unsigned soff) {
    if (NT) asm volatile("s_mov_b32 m0, %0\n\ts_nop 3\n\tbuffer_load_dwordx4 %1, %2, %3 offen nt lds" :: "s"(lds), "v"(voff), "s"(srd), "s"(soff) : "memory");
    else asm volatile("s_mov_b32 m0, %0\n\ts_nop 3\n\tbuffer_load_dwordx4 %1, %2, %3 offen lds" :: "s"(lds), "v"(voff), "s"(srd), "s"(soff) : "memory");
}
DI void ld4_sc1(bf16x8* d, unsigned voff, rsrc_t srd, unsigned soff) {
    asm volatile("s_nop 4\n\tbuffer_load_dwordx4 %0, %4, %5, %6 offen sc1\n\tbuffer_load_dwordx4 %1, %4, %5, %6 offen offset:1024 sc1\n\t"
                 "buffer_load_dwordx4 %2, %4, %5, %6 offen offset:2048 sc1\n\tbuffer_load_dwordx4 %3, %4, %5, %6 offen offset:3072 sc1"
                 : "+v"(d[0]), "+v"(d[1]), "+v"(d[2]), "+v"(d[3]) : "v"(voff), "s"(srd), "s"(soff) : "memory");
}
constexpr unsigned BIAS = 4096;
#define WAITV_F(n, f) asm volatile("s_waitcnt vmcnt(" #n ")" : "+v"((f)[0]), "+v"((f)[1]), "+v"((f)[2]), "+v"((f)[3]) :: "memory")
DI bf16x8 lds128(lptr p) { return *(const LAS bf16x8*)p; }
DI s16x4 ldstr(lptr p) {
    typedef short v4i16_t __attribute__((ext_vector_type(4)));
    return __builtin_amdgcn_ds_read_tr16_b64_v4i16((LAS v4i16_t*)p);
}
DI bf16x8 cat(s16x4 a, s16x4 b) { return __builtin_shufflevector(a, b, 0, 1, 2, 3, 4, 5, 6, 7); }
DI float swapmax(float v) {
    auto r = __builtin_amdgcn_permlane32_swap(__float_as_uint(v), __float_as_uint(v), false, false);
    return fmaxf(__uint_as_float(r[0]), __uint_as_float(r[1]));
}
DI float swapsum(float v) {
    auto r = __builtin_amdgcn_permlane32_swap(__float_as_uint(v), __float_as_uint(v), false, false);
    return __uint_as_float(r[0]) + __uint_as_float(r[1]);
}
DI rsrc_t mkrsrc(const void* p, unsigned bytes) {
    return __builtin_amdgcn_make_buffer_rsrc((void*)p, (short)0, (int)bytes, 0x00020000);
}
DI void zero16(f32x16& v) {
#pragma unroll
    for (int r = 0; r < 16; ++r) v[r] = 0.f;
}
DI void wait_flags(const GAS u64* p, u64 tag) {
#pragma unroll 1
    for (unsigned it = 0; it < SPIN_CAP; ++it) {
        const u64 v = __hip_atomic_load(p, __ATOMIC_RELAXED, __HIP_MEMORY_SCOPE_AGENT);
        if (__all(v == tag)) break;
        __builtin_amdgcn_s_sleep(1);
    }
    asm volatile("" ::: "memory");
}

DI float max3f(float a, float b, float c) { float r; asm("v_max3_f32 %0, %1, %2, %3" : "=v"(r) : "v"(a), "v"(b), "v"(c)); return r; }
DI float fadd_s(float a, float b) { float r; asm("v_add_f32_e32 %0, %1, %2" : "=v"(r) : "v"(a), "v"(b)); return r; }
DI float fmul_s(float a, float b) { float r; asm("v_mul_f32_e32 %0, %1, %2" : "=v"(r) : "v"(a), "v"(b)); return r; }
DI float fsub_s(float a, float b) { float r; asm("v_sub_f32_e32 %0, %1, %2" : "=v"(r) : "v"(a), "v"(b)); return r; }
DI float exp_s(float a) { float r; asm("v_exp_f32_e32 %0, %1" : "=v"(r) : "v"(a)); return r; }
#define MPAD4(a) asm volatile("s_nop 7\n\ts_nop 7" : "+v"((a)[0]), "+v"((a)[1]), "+v"((a)[2]), "+v"((a)[3]))
#define PIN(x) asm volatile("" : "+v"(x))
#define PIN4(f) asm volatile("" : "+v"((f)[0]), "+v"((f)[1]), "+v"((f)[2]), "+v"((f)[3]) :: "memory")
DI lptr opaque(lptr p) { unsigned a = (unsigned)(uintptr_t)p; asm volatile("" : "+v"(a)); return (lptr)(uintptr_t)a; }
#define LA_ 6
constexpr int LA = LA_;
#define SM_THR 8.0f

struct QBase { lptr x0, x1, w; };
DI void qload(int i, bf16x8* wf, bf16x8* xf, const QBase& q) {
    const int s = i >> 2, d = i & 3;
    if (d == 0) xf[s] = lds128(((s & 1) ? q.x1 : q.x0) + 512 * (s >> 1));
    wf[i & 7] = cat(ldstr(q.w + 4096 * s + 512 * d), ldstr(q.w + 4096 * s + 512 * d + 256));
}
DI void qpre(bf16x8* wf, bf16x8* xf, const QBase& q) {
#pragma unroll
    for (int i = 0; i < LA; ++i) qload(i, wf, xf, q);
}
template <bool NEXT> DI void qsteps(f32x16* acc, bf16x8* wf, bf16x8* xf, const QBase& q, const QBase& qn) {
#pragma unroll
    for (int i = 0; i < 16; ++i) {
        SB();
        acc[i & 3] = MFMA32(wf[i & 7], xf[i >> 2], acc[i & 3]);
        PIN(acc[i & 3]);
        SB();
        if (NEXT && i + LA == 16) BARX();
        if (i + LA < 16) qload(i + LA, wf, xf, q); else if (NEXT) qload(i + LA - 16, wf, xf, qn);
    }
    SB();
}
template <bool FIRST = false> DI void oblock(f32x16* acc, const bf16x8* of, lptr wp_) {
    const lptr wp = opaque(wp_);
    bf16x8 wf[8];
    auto load = [&](int i) { wf[i & 7] = cat(ldstr(wp + 5120 * (i / 5) + 512 * (i % 5)), ldstr(wp + 5120 * (i / 5) + 2560 + 512 * (i % 5))); };
#pragma unroll
    for (int i = 0; i < LA; ++i) load(i);
#pragma unroll
    for (int i = 0; i < 20; ++i) {
        SB();
        if (FIRST && i < 5) { f32x16 z; zero16(z); acc[i] = MFMA32(wf[i & 7], of[0], z); }
        else acc[i % 5] = MFMA32(wf[i & 7], of[i / 5], acc[i % 5]);
        PIN(acc[i % 5]);
        SB();
        if (i + LA < 20) load(i + LA);
    }
    SB();
}
DI void aload(int i, bf16x8* fr, lptr vp, lptr kp0, lptr kp1) {
    if (i < 16) fr[i & 7] = cat(ldstr(vp + 4096 * (i >> 2) + 512 * (i & 3)), ldstr(vp + 4096 * (i >> 2) + 2048 + 512 * (i & 3)));
    else { const int s = i & 7; fr[i & 7] = lds128(((s & 1) ? kp1 : kp0) + 512 * (s >> 1) + 8192 * ((i - 16) >> 3)); }
}

__global__ __launch_bounds__(512, 2) void fused_kernel(
    const char* xA, const char* kA, const char* vA, const char* wqA, const char* woA,
    const char* xB, const char* kB, const char* vB, const char* wqB, const char* woB,
    char* ws, char* out)
{
    extern __shared__ __attribute__((aligned(16))) char smem_[];
    const lptr smem = (lptr)smem_;
    const unsigned lds0 = (unsigned)(uintptr_t)smem;
    const int tid = threadIdx.x, lane = tid & 63;
    const int wid = __builtin_amdgcn_readfirstlane(tid >> 6);
    const int bid = blockIdx.x;
    const int xg = bid & 7, ii = bid >> 3;
    const int br = xg >> 2, xq = xg & 3, b = ii >> 2, jj = ii & 3;
    const int h1 = 8 * xq + 2 * jj;
    const char* x = br ? xB : xA;
    const char* kc = br ? kB : kA;
    const char* vc = br ? vB : vA;
    const char* wq = br ? wqB : wqA;
    const char* wo = br ? woB : woA;
    const int qb = wid & 3, grp = wid >> 2, lw = wid & 3;
    const int l31 = lane & 31, h = lane >> 5;
    STAMP(0);
    const char* codebase;
    asm volatile("s_getpc_b64 %0" : "=s"(codebase));

    u64 gen;
    {
        const char* gp = ws + WS_GEN + (br * NB + b) * 256;
        asm volatile("global_load_dwordx2 %0, %1, off sc1" : "=v"(gen) : "v"(gp) : "memory");
    }

    const rsrc_t sk = mkrsrc(kc + (size_t)(b * NH + h1) * SK * DH * 2 - BIAS, 2 * SK * DH * 2 + BIAS);
    const rsrc_t sv = mkrsrc(vc + (size_t)(b * NH + h1) * SK * DH * 2 - BIAS, 2 * SK * DH * 2 + BIAS);
    const rsrc_t sof = mkrsrc(ws + WS_OF + (size_t)((br * NB + b) * NH + 8 * xq) * 32768, 8 * 32768);

    const unsigned dsub = lane >> 5, dr = (lane >> 2) & 7, dsl = lane & 3;
    const unsigned xr0 = 16u * (dsl ^ ((dr >> 2) & 3)), xr1 = 16u * (dsl ^ ((2 + (dr >> 2)) & 3));
    const unsigned vk0 = BIAS + 4096 * lw + dr * 256 + dsub * 64 + xr0, vk2 = BIAS + 4096 * lw + dr * 256 + dsub * 64 + xr1;
    const unsigned vv0 = BIAS + 4096 * lw + dr * 256 + dsub * 64 + 16 * dsl;
    const unsigned ldw = lds0 + 4096 * lw;

    auto issueK = [&](int t, unsigned pair) {
        if ((POL & 1) == ((POL >> 2) & 1) ? (POL & 1) : ((POL >> (2 * br)) & 1)) dma4<1>(ldw + pair, vk0, vk0 - 896, vk2, vk2 - 896, sk, (unsigned)t * 16384u);
        else dma4<0>(ldw + pair, vk0, vk0 - 896, vk2, vk2 - 896, sk, (unsigned)t * 16384u); };
    auto issueV = [&](int t, unsigned pair) {
        if (((POL >> 1) & 1) == ((POL >> 3) & 1) ? ((POL >> 1) & 1) : ((POL >> (2 * br + 1)) & 1)) dma4<1>(ldw + pair + SLOT, vv0, vv0 - 896, vv0, vv0 - 896, sv, (unsigned)t * 16384u);
        else dma4<0>(ldw + pair + SLOT, vv0, vv0 - 896, vv0, vv0 - 896, sv, (unsigned)t * 16384u); };
    auto nextpair = [](unsigned p) { return (p == 4u * SLOT) ? 0u : p + 2u * SLOT; };

    const int G1 = (lane >> 4) & 1, tq = (lane & 15) >> 2, tp = lane & 3;
    const int sig = ((tp & 1) << 1) | (tp >> 1);
    const unsigned wqb = SLOT + 2048 * h + 64 * tq + 32 * G1 + 8 * sig;
    const unsigned rowq = 32 * qb + l31;
    const unsigned xsw = (rowq >> 2) & 3;
    const unsigned xb0 = 1024 * (rowq >> 3) + 64 * (rowq & 7) + 16 * ((0 + h) ^ xsw);
    const unsigned xb1 = 1024 * (rowq >> 3) + 64 * (rowq & 7) + 16 * ((2 + h) ^ xsw);
    const unsigned ksw = (l31 >> 2) & 3;
    const unsigned kb0 = 2048 * (l31 >> 3) + 64 * (l31 & 7) + 16 * ((0 + h) ^ ksw);
    const unsigned kb1 = 2048 * (l31 >> 3) + 64 * (l31 & 7) + 16 * ((2 + h) ^ ksw);
    const unsigned trb = 64 * (4 * h + tq) + 32 * G1 + 8 * tp;
    const unsigned vo16 = lane * 16;

    auto flagp = [&](int bb, int stage, int w) { return (GAS u64*)(ws + WS_FLAG + (size_t)((bb * 3 + stage) * 8 + w) * 64); };
    auto partner = [&](int dj) { return (bid & ~24) | (((jj + dj) & 3) << 3); };
    auto hl1 = [&](int dj) { return 2 * ((jj + dj) & 3); };

    f32x16 acc[5];
    bf16x8 qf[8], pf[4], fr[8];
    f32x16 s0, s1;
#pragma unroll
    for (int d = 0; d < 4; ++d) zero16(acc[d]);

    auto nextstage = [](unsigned st) { return st == 8u * SLOT ? 4u * SLOT : (st == 4u * SLOT ? 6u * SLOT : (st == 6u * SLOT ? 2u * SLOT : 8u * SLOT)); };
    auto qbase = [&](unsigned st) { return QBase{opaque(smem + st + xb0), opaque(smem + st + xb1), opaque(smem + st + wqb)}; };
    if (grp == 0) {
        unsigned st = 8 * SLOT;
        unsigned dummy = 0;
        {
            const unsigned line = lw * 64 + lane;
            const char* p = codebase + (line < CODE_LINES ? line : 0) * 128;
            asm volatile("global_load_dword %0, %1, off" : "+v"(dummy) : "v"(p) : "memory");
        }
        BARX();
        QBase qc = qbase(st);
        qpre(fr, pf, qc);
#pragma unroll 1
        for (int i = 0; i < 10; ++i) {
            st = nextstage(st);
            const QBase qn = qbase(st);
            qsteps<true>(acc, fr, pf, qc, qn);
            qc = qn;
        }
        WAITV(0);
        PIN(dummy);
    } else {
        const rsrc_t sx = mkrsrc(x + (size_t)b * SQ * DM * 2, SQ * DM * 2);
        const rsrc_t sw = mkrsrc(wq + (size_t)h1 * 256 - BIAS, DM * 8192 - h1 * 256 + BIAS);
        const unsigned vx0 = (32 * lw + dr) * 1280 + dsub * 64 + xr0, vx1 = (32 * lw + 8 + dr) * 1280 + dsub * 64 + xr1 - 1024;
        const unsigned vw0 = BIAS + (16 * lw + dr) * 8192 + dsub * 64 + 16 * dsl;
        const int rot = ROT ? b + 3 * jj : 0;
        constexpr unsigned T0 = 6 * SLOT, T1 = 8 * SLOT, P1 = 2 * SLOT, P2 = 4 * SLOT;
        int ci = rot >= 10 ? rot - 10 : rot;
        auto issueQn = [&](int hd, unsigned st) {
            dma4<NT_W>(ldw + st + SLOT, vw0, vw0 - 896, vw0 + 65536 - 2048, vw0 + 65536 - 2944, sw, (unsigned)ci * 524288u + 256 * hd);
            dma4<NT_X>(ldw + st, vx0, vx1, vx0 + 20480 - 2048, vx1 + 20480 - 2048, sx, 128 * ci);
            ci = ci == 9 ? 0 : ci + 1;
        };
        issueQn(0, T1);
        WAITV(0); BARX();
        issueQn(0, P2); issueQn(0, T0);
        WAITV(8);  BARX(); issueQn(0, P1);
        WAITV(8);  BARX(); issueQn(0, T1);
        WAITV(8);  BARX(); issueQn(0, P2);
        WAITV(8);  BARX(); issueQn(0, T0); issueK(0, 0); issueV(0, 0);
        WAITV(16); BARX(); issueQn(0, P1);
        WAITV(16); BARX(); issueQn(0, T1);
        WAITV(8);  BARX(); issueQn(0, P2);
        WAITV(8);  BARX(); issueQn(1, T0);
        WAITV(8);  BARX(); issueK(1, P1); issueV(1, P1);
        BARX(); issueQn(1, T1);
        const QBase q0 = qbase(T0), q1 = qbase(T1);
        WAITV(16); BARX();
        qpre(fr, pf, q0);
        issueK(2, P2);
        qsteps<false>(acc, fr, pf, q0, q0);
        WAITV(4);
        unsigned pv = P2;
#pragma unroll 1
        for (int c = 1; c < 9; c += 2) {
            unsigned pk = nextpair(pv);
            BARX();
            qpre(fr, pf, q1);
            issueQn(1, T0);
            qsteps<false>(acc, fr, pf, q1, q1);
            issueV(c + 1, pv); issueK(c + 2, pk);
            pv = nextpair(pk);
            WAITV(8);
            BARX();
            qpre(fr, pf, q0);
            issueQn(1, T1);
            qsteps<false>(acc, fr, pf, q0, q0);
            issueV(c + 2, pk); issueK(c + 3, pv);
            WAITV(8);
        }
        BARX();
        qpre(fr, pf, q1);
        qsteps<false>(acc, fr, pf, q1, q1);
    }
    STAMP(1);
    asm volatile("" : "+v"(gen));
    const u64 tag = gen + 1;

    __builtin_amdgcn_s_setprio(PPRIO);
    MPAD4(acc);
    {
        float cexp = CEXP; PIN(cexp);
#pragma unroll
        for (int d = 0; d < 4; ++d) {
#pragma unroll
            for (int r = 0; r < 16; ++r) acc[d][r] = fmul_s(acc[d][r], cexp);
            qf[2 * d] = pack8(acc[d], 0); qf[2 * d + 1] = pack8(acc[d], 1); zero16(acc[d]);
        }
    }
    if (grp == 0) BARX();
    float la = 0.f, lb = 0.f, lc = 0.f, ld = 0.f;
    {
        f32x16 negm, p0; zero16(negm); zero16(s1); zero16(p0);
        unsigned pk = grp ? 4 * SLOT : 0;
        lptr kp0 = opaque(smem + pk + kb0), kp1 = opaque(smem + pk + kb1);
#pragma unroll
        for (int i = 16; i < 16 + LA; ++i) aload(i, fr, kp0, kp0, kp1);
        const GAS u64* fp = flagp(partner(1 + (lane % 3)), 0, qb);
        u64 fv = 0;
        const int tw = grp ? 3 : -1;
        float thr = -__builtin_inff(), dmin = -__builtin_inff();
        u32x4 pw[4];
#pragma unroll 1
        for (int t = 0; t < 8; ++t) {
            if (__builtin_expect(t == tw, 0)) asm volatile("global_load_dwordx2 %0, %1, off sc1" : "+v"(fv) : "v"(fp) : "memory");
            float c0, c1;
#pragma unroll
            for (int j = 0; j < 16; ++j) {
                const int i = 16 + j;
                SB();
                if (j < 8) s0 = MFMA32(fr[i & 7], qf[j], j == 0 ? negm : s0); else s1 = MFMA32(fr[i & 7], qf[j - 8], j == 8 ? negm : s1);
                SB();
                if (i + LA < 32) aload(i + LA, fr, kp0, kp0, kp1);
                if (j < 8) {
                    la = fadd_s(la, s1[2 * j]); lb = fadd_s(lb, s1[2 * j + 1]); lc = fadd_s(lc, p0[2 * j]); ld = fadd_s(ld, p0[2 * j + 1]);
                    PIN(la); PIN(lb); PIN(lc); PIN(ld);
                }
                if (j >= 10) {
                    const int e = 2 * (j - 10);
                    p0[e] = exp_s(s0[e]); p0[e + 1] = exp_s(s0[e + 1]); PIN(p0);
                    if (j == 10) { c0 = max3f(s0[0], s0[1], s0[2]); c1 = max3f(s0[3], s0[4], s0[5]); PIN(c0); PIN(c1); }
                    if (j == 11) { c0 = max3f(c0, s0[6], s0[7]); c1 = max3f(c1, s0[8], s0[9]); PIN(c0); PIN(c1); }
                    if (j == 12) { c0 = max3f(c0, s0[10], s0[11]); PIN(c0); }
                    if (j == 13) { c1 = max3f(c1, s0[12], s0[13]); PIN(c1); }
                    if (j == 14) { c0 = max3f(c0, s0[14], s0[15]); PIN(c0); }
                    if (j == 15) { c0 = max3f(c0, c1, c1); PIN(c0); }
                    pw[e >> 3][(e >> 1) & 3] = cvtpk(p0[e], p0[e + 1]); PIN(pw[e >> 3][(e >> 1) & 3]);
                }
            }
            WAITV(0);
            if (__builtin_expect(t == tw, 0)) { PIN(fv); if (!__all(fv == tag)) wait_flags(fp, tag); }
            STAMP(2 + 2 * t);
            BARX();
            const lptr vp = opaque(smem + pk + SLOT + trb);
            pk = nextpair(pk);
            kp0 = opaque(smem + pk + kb0); kp1 = opaque(smem + pk + kb1);
#pragma unroll
            for (int i = 0; i < LA; ++i) aload(i, fr, vp, vp, vp);
            PIN(s1);
            SB();
            {
                float c2 = max3f(s1[0], s1[1], s1[2]), c3 = max3f(s1[3], s1[4], s1[5]);
                c1 = max3f(s1[6], s1[7], s1[8]); c0 = max3f(c0, s1[9], s1[10]);
                c2 = max3f(c2, s1[11], s1[12]); c3 = max3f(c3, s1[13], s1[14]); c1 = max3f(c1, s1[15], c0);
                c0 = max3f(c1, c2, c3);
                auto r_ = __builtin_amdgcn_permlane32_swap(__float_as_uint(c0), __float_as_uint(c0), false, false);
                const float mx = max3f(__uint_as_float(r_[0]), __uint_as_float(r_[1]), __uint_as_float(r_[1]));
                if (__builtin_expect(__any(!(mx <= thr)), 0)) {
                    const float d = fmaxf(mx, dmin);
                    float f = __builtin_amdgcn_exp2f(fminf(-d, 0.f));
                    asm volatile("s_nop 1" : "+v"(f));
#pragma unroll
                    for (int r = 0; r < 16; ++r) { s0[r] = fsub_s(s0[r], d); s1[r] = fsub_s(s1[r], d); negm[r] = fsub_s(negm[r], d); }
                    la = fmul_s(la, f); lb = fmul_s(lb, f); lc = fmul_s(lc, f); ld = fmul_s(ld, f);
#pragma unroll
                    for (int dd = 0; dd < 4; ++dd)
#pragma unroll
                        for (int r = 0; r < 16; ++r) acc[dd][r] = fmul_s(acc[dd][r], f);
#pragma unroll
                    for (int e = 0; e < 12; ++e) p0[e] = __builtin_amdgcn_exp2f(s0[e]);
#pragma unroll
                    for (int e = 0; e < 12; e += 2) pw[e >> 3][(e >> 1) & 3] = cvtpk(p0[e], p0[e + 1]);
                }
                thr = SM_THR; dmin = 0.f;
            }
#pragma unroll
            for (int i = 0; i < 16; ++i) {
                SB();
                acc[i & 3] = MFMA32(fr[i & 7], __builtin_bit_cast(bf16x8, pw[i >> 2]), acc[i & 3]);
                SB();
                constexpr int pairs[16] = {6, 7, 8, 9, 10, 11, -1, 12, 13, 14, 15, -1, -1, -1, -1, -1};
                const int q = pairs[i];
                if (q >= 0) {
                    if (q < 8) { p0[2 * q] = __builtin_amdgcn_exp2f(s0[2 * q]); p0[2 * q + 1] = __builtin_amdgcn_exp2f(s0[2 * q + 1]); PIN(p0); }
                    else { s1[2 * q - 16] = __builtin_amdgcn_exp2f(s1[2 * q - 16]); s1[2 * q - 15] = __builtin_amdgcn_exp2f(s1[2 * q - 15]); PIN(s1); }
                }
                aload(i + LA, fr, vp, kp0, kp1);
                if (q >= 0) {
                    pw[q >> 2][q & 3] = q < 8 ? cvtpk(p0[2 * q], p0[2 * q + 1]) : cvtpk(s1[2 * q - 16], s1[2 * q - 15]);
                    PIN(pw[q >> 2][q & 3]);
                }
            }
            SB();
            STAMP(3 + 2 * t);
        }
#pragma unroll
        for (int j = 0; j < 8; ++j) { la = fadd_s(la, s1[2 * j]); lb = fadd_s(lb, s1[2 * j + 1]); lc = fadd_s(lc, p0[2 * j]); ld = fadd_s(ld, p0[2 * j + 1]); }
    }
    if (grp == 0) { __builtin_amdgcn_s_setprio(0); BARX(); }
    STAMP(18);

    {
        float inv = __builtin_amdgcn_rcpf(swapsum((la + lb) + (lc + ld)));
        asm volatile("s_nop 1" : "+v"(inv));
        MPAD4(acc);
#pragma unroll
        for (int d = 0; d < 4; ++d) {
#pragma unroll
            for (int r = 0; r < 16; ++r) acc[d][r] = fmul_s(acc[d][r], inv);
            qf[2 * d] = pack8(acc[d], 0); qf[2 * d + 1] = pack8(acc[d], 1);
            __builtin_amdgcn_raw_buffer_store_b128(__builtin_bit_cast(u32x4, qf[2 * d]), sof, vo16, (2 * jj + grp) * 32768 + qb * 8192 + d * 2048, 16);
            __builtin_amdgcn_raw_buffer_store_b128(__builtin_bit_cast(u32x4, qf[2 * d + 1]), sof, vo16, (2 * jj + grp) * 32768 + qb * 8192 + d * 2048 + 1024, 16);
            SB();
        }
    }
    const rsrc_t swo = mkrsrc(wo + (size_t)(8 * xq * 128) * 1280 + 320 * jj - BIAS, 1024 * 1280 - 320 * jj + BIAS);
    const unsigned vwA = BIAS + (16 * lw + dr) * 1280 + dsub * 64 + 16 * dsl, vwB = BIAS + (16 * lw + dr) * 1280 + 16 * dsl + (dsub ? 8 * 1280 : 256) - 2048;
    auto issueW = [&](int hl, int half, int buf) {
        const unsigned so = (unsigned)(hl * 128 + 64 * half) * 1280u, ld = lds0 + buf * WT + 5 * lw * 1024;
        dma4<NT_WO>(ld, vwA, vwA - 896, vwB, vwA + 10240 + 64 - 3072, swo, so);
        dma1<NT_WO>(ld + 4096, vwA, swo, so + 10240 + 192);
    };
    const lptr x1 = smem + (lw < 2 ? lw * 8192 : 7 * WT + (lw - 2) * 8192) + vo16;
    if (grp) {
#pragma unroll
        for (int f = 0; f < 8; ++f) *(LAS bf16x8*)(x1 + 1024 * f) = qf[f];
        __builtin_amdgcn_s_setprio(0);
        BAR();
        BARX();
        WAITV(0);
        if (lane == 0) __hip_atomic_store(flagp(bid, 1, qb), tag, __ATOMIC_RELAXED, __HIP_MEMORY_SCOPE_AGENT);
        STAMP(19);
        issueW(hl1(3) + 1, 0, 0); issueW(hl1(3) + 1, 1, 7);
        wait_flags(flagp(partner(1 + (lane % 3)), 1, qb), tag);
        WAITV(0);
        STAMP(21);
        BARX();
    } else {
        bf16x8 cur[4], nxt[4];
#pragma unroll
        for (int k = 0; k < 4; ++k) { cur[k] = qf[k]; nxt[k] = qf[4 + k]; }
        auto issueO = [&](bf16x8* f, int blk) { ld4_sc1(f, vo16, sof, hl1(blk >> 1) * 32768 + qb * 8192 + 4096 * (blk & 1)); };
        issueW(hl1(0), 0, 5);
        BARX();
        issueW(hl1(0), 1, 6); issueW(hl1(1), 0, 7);
        issueV(10, 2 * SLOT); issueK(11, 4 * SLOT);
        BARX();
        issueV(11, 4 * SLOT); issueK(12, 0);
        WAITV(8);
        if (lane == 0) __hip_atomic_store(flagp(bid, 0, qb), tag, __ATOMIC_RELAXED, __HIP_MEMORY_SCOPE_AGENT);
        BARX();
        oblock<true>(acc, cur, smem + 5 * WT + trb);
        issueV(12, 0); issueK(13, 2 * SLOT);
        WAITV(8);
        BARX();
        issueO(cur, 2);
        issueW(hl1(1), 1, 5);
        oblock(acc, nxt, smem + 6 * WT + trb);
        issueO(nxt, 3);
        issueV(13, 2 * SLOT); issueK(14, 4 * SLOT);
        WAITV(17);
        PIN4(cur);
        oblock(acc, cur, smem + 7 * WT + trb);
        issueO(cur, 4);
        WAITV(12);
        BARX();
        unsigned wdummy = 0;
        {
            const int r = 64 + 40 * b + (lane < 40 ? lane : 0);
            const char* p = wo + (size_t)((8 * xq + hl1(1 + (r >> 7)) + 1) * 128 + (r & 127)) * 1280 + 320 * jj + (lw == 3 ? 316 : 128 * lw);
            asm volatile("global_load_dword %0, %1, off" : "+v"(wdummy) : "v"(p) : "memory");
        }
        issueW(hl1(2), 0, 6); issueW(hl1(2), 1, 7);
        PIN4(nxt);
        oblock(acc, nxt, smem + 5 * WT + trb);
        issueO(nxt, 5);
        issueV(14, 4 * SLOT); issueK(15, 0);
        WAITV(17);
        PIN(wdummy);
        BARX();
        issueW(hl1(3), 0, 5);
        PIN4(cur);
        oblock(acc, cur, smem + 6 * WT + trb);
        issueO(cur, 6);
        issueV(15, 0);
        WAITV(13);
        BARX();
        issueW(hl1(3), 1, 6); issueW(hl1(0) + 1, 0, 2); issueW(hl1(0) + 1, 1, 3);
        PIN4(nxt);
        oblock(acc, nxt, smem + 7 * WT + trb);
        issueO(nxt, 7);
        WAITV(4);
        BARX();
        issueW(hl1(1) + 1, 0, 4);
        PIN4(cur);
        oblock(acc, cur, smem + 5 * WT + trb);
        WAITV(5);
        PIN4(nxt);
        oblock(acc, nxt, smem + 6 * WT + trb);
        WAITV(0);
        BARX();
        bf16x8 of2[24];
#pragma unroll
        for (int f = 0; f < 8; ++f) of2[f] = lds128(x1 + 1024 * f);
        BAR();
        issueW(hl1(1) + 1, 1, 5); issueW(hl1(2) + 1, 0, 6); issueW(hl1(2) + 1, 1, 1);
        oblock(acc, of2, smem + 2 * WT + trb);
        oblock(acc, of2 + 4, smem + 3 * WT + trb);
        WAITV(0);
        STAMP(20);
        BARX();
        const unsigned ob = qb * 8192 + 32768;
        auto ldo = [&](int k0) {
#pragma unroll
            for (int k = k0; k < k0 + 4; ++k)
                of2[k] = __builtin_bit_cast(bf16x8, __builtin_amdgcn_raw_buffer_load_b128(sof, vo16, (unsigned)hl1(1 + (k >> 3)) * 32768 + ob + (k & 7) * 1024, 16));
        };
        ldo(0); ldo(4); SB();
        oblock(acc, of2, smem + 4 * WT + trb);       ldo(8);  ldo(12); SB();
        oblock(acc, of2 + 4, smem + 5 * WT + trb);   ldo(16); SB();
        oblock(acc, of2 + 8, smem + 6 * WT + trb);   ldo(20); SB();
        oblock(acc, of2 + 12, smem + 1 * WT + trb);
        oblock(acc, of2 + 16, smem + 0 * WT + trb);
        oblock(acc, of2 + 20, smem + 7 * WT + trb);
        STAMP(22);
        const rsrc_t spb = mkrsrc(ws + WS_PB, 256 * PB_WG);
        f32x4 mine[5];
#define UNIT(u, e) acc[(u) >> 2][4 * ((u) & 3) + (e)]
#define UPK(u) cvtpk(UNIT(u, 0), UNIT(u, 1)), cvtpk(UNIT(u, 2), UNIT(u, 3))
#pragma unroll
        for (int g = 0; g < 4; ++g) {
            if (g == xq) {
#pragma unroll
                for (int n = 0; n < 5; ++n) mine[n] = (f32x4){UNIT(5 * g + n, 0), UNIT(5 * g + n, 1), UNIT(5 * g + n, 2), UNIT(5 * g + n, 3)};
            } else {
                const unsigned so = (unsigned)((bid & ~3) | g) * PB_WG + (xq * 4 + qb) * 5120;
                const u32x4 w01 = {UPK(5 * g), UPK(5 * g + 1)}, w23 = {UPK(5 * g + 2), UPK(5 * g + 3)};
                const u32x2 w4 = {UPK(5 * g + 4)};
                __builtin_amdgcn_raw_buffer_store_b128(w01, spb, vo16, so, 16);
                __builtin_amdgcn_raw_buffer_store_b128(w23, spb, vo16, so + 1024, 16);
                __builtin_amdgcn_raw_buffer_store_b64(w4, spb, lane * 8, so + 2048, 16);
            }
        }
        WAITV(0);
        if (lane == 0) __hip_atomic_store(flagp(bid, 2, qb), tag, __ATOMIC_RELAXED, __HIP_MEMORY_SCOPE_AGENT);
        STAMP(23);
        wait_flags(flagp((bid & ~3) | ((xq + 1 + (lane % 3)) & 3), 2, qb), tag);
        STAMP(24);
        {
            const rsrc_t sout = mkrsrc(out + ((size_t)b * SQ * (2 * DM) + br * DM + 160 * jj + 40 * xq) * 2, SQ * 2 * DM * 2 - (br * DM + 160 * jj + 40 * xq) * 2);
            const unsigned vop = rowq * (4 * DM) + 8 * h;
            u32x4 a01[3], a23[3]; u32x2 a4[3];
#pragma unroll
            for (int s = 0; s < 3; ++s) {
                const unsigned so = (unsigned)bid * PB_WG + (((xq + 1 + s) & 3) * 4 + qb) * 5120;
                a01[s] = __builtin_amdgcn_raw_buffer_load_b128(spb, vo16, so, 16);
                a23[s] = __builtin_amdgcn_raw_buffer_load_b128(spb, vo16, so + 1024, 16);
                a4[s] = __builtin_amdgcn_raw_buffer_load_b64(spb, lane * 8, so + 2048, 16);
            }
            auto up = [](unsigned lo, unsigned hi) { return (f32x4){__uint_as_float(lo << 16), __uint_as_float(lo & 0xffff0000u), __uint_as_float(hi << 16), __uint_as_float(hi & 0xffff0000u)}; };
#pragma unroll
            for (int s = 0; s < 3; ++s) {
                mine[0] += up(a01[s][0], a01[s][1]); mine[1] += up(a01[s][2], a01[s][3]);
                mine[2] += up(a23[s][0], a23[s][1]); mine[3] += up(a23[s][2], a23[s][3]); mine[4] += up(a4[s][0], a4[s][1]);
            }
#pragma unroll
            for (int n = 0; n < 5; ++n) __builtin_amdgcn_raw_buffer_store_b64((u32x2){cvtpk(mine[n][0], mine[n][1]), cvtpk(mine[n][2], mine[n][3])}, sout, vop, 16 * n, OUT_AUX);
        }
    }
    {
        if (xq == 0 && jj == 0 && wid == 0 && lane == 0)
            __hip_atomic_store((GAS u64*)(ws + WS_GEN + (br * NB + b) * 256), tag, __ATOMIC_RELAXED, __HIP_MEMORY_SCOPE_AGENT);
        STAMP(25);
    }
}

extern "C" void kernel_launch(void* const* d_in, const int* in_sizes, int n_in, void* d_out, int out_size,
                              void* d_ws, size_t ws_size, hipStream_t stream) {
    (void)hipFuncSetAttribute((const void*)fused_kernel, hipFuncAttributeMaxDynamicSharedMemorySize, LDS_BYTES);
    hipLaunchKernelGGL(fused_kernel, dim3(256), dim3(512), LDS_BYTES, stream,
                       (const char*)d_in[0], (const char*)d_in[1], (const char*)d_in[2], (const char*)d_in[3], (const char*)d_in[4],
                       (const char*)d_in[5], (const char*)d_in[6], (const char*)d_in[7], (const char*)d_in[8], (const char*)d_in[9],
                       (char*)d_ws + WS_SHIFT, (char*)d_out);
}
```

```cpp
#include <hip/hip_runtime.h>
#include <hip/hip_bf16.h>
#include <stdint.h>
#include <type_traits>

typedef short bf16x8 __attribute__((ext_vector_type(8)));
typedef short s16x4 __attribute__((ext_vector_type(4)));
typedef float f32x16 __attribute__((ext_vector_type(16)));
typedef float f32x4 __attribute__((ext_vector_type(4)));
typedef float f32x2 __attribute__((ext_vector_type(2)));
typedef __bf16 bf16x2_t __attribute__((ext_vector_type(2)));
typedef unsigned u32x4 __attribute__((ext_vector_type(4)));
typedef unsigned u32x2 __attribute__((ext_vector_type(2)));
typedef unsigned long long u64;
#define LAS __attribute__((address_space(3)))
#define GAS __attribute__((address_space(1)))
#define DI __device__ __forceinline__
typedef LAS char* lptr;
typedef __amdgpu_buffer_rsrc_t rsrc_t;

constexpr int NB = 8, SQ = 128, DM = 640, NH = 32, SK = 512, DH = 128;
constexpr float CEXP = 0.08838834764831845f * 1.4426950408889634f;
constexpr int SLOT = 16384;
constexpr int LDS_T = 6 * SLOT;
constexpr int WT = 20480;
constexpr int LDS_BYTES = 10 * SLOT;
constexpr size_t WS_GEN = 0, WS_FLAG = 8192, WS_OF = 1 << 20;
constexpr size_t OF_BYTES = (size_t)2 * NB * NH * 32768;
constexpr size_t WS_PB = WS_OF + OF_BYTES;
constexpr unsigned PB_WG = 4 * 8 * 3 * 1024;
#define NT_W 0
#define NT_X 0
#define NT_WO 0
#define PPRIO 2
#define PBF16 1
#define WS_SHIFT 0
#define POL 15
#define PRETOUCH 1
#define CL 180
constexpr unsigned CODE_LINES = CL;
#define WOWARM 1
#define ROT 1
#define OUT_AUX 0
#define PROBE_BID 77
constexpr unsigned SPIN_CAP = 1u << 16;

#define MFMA32(a, b, c) __builtin_amdgcn_mfma_f32_32x32x16_bf16((a), (b), (c), 0, 0, 0)
#define WAITV(n) asm volatile("s_waitcnt vmcnt(" #n ")" ::: "memory")
#define SB() __builtin_amdgcn_sched_barrier(0)
#define BARX() do { SB(); asm volatile("s_barrier" ::: "memory"); SB(); } while (0)
#define BAR() do { SB(); asm volatile("s_waitcnt lgkmcnt(0)\n\ts_barrier" ::: "memory"); SB(); } while (0)

#define STAMP(idx)

DI unsigned cvtpk(float lo, float hi) {
    f32x2 v = {lo, hi};
    bf16x2_t b = __builtin_convertvector(v, bf16x2_t);
    return __builtin_bit_cast(unsigned, b);
}
DI bf16x8 pack8(const f32x16& x, int s) {
    u32x4 w = {cvtpk(x[8 * s + 0], x[8 * s + 1]), cvtpk(x[8 * s + 2], x[8 * s + 3]),
               cvtpk(x[8 * s + 4], x[8 * s + 5]), cvtpk(x[8 * s + 6], x[8 * s + 7])};
    return __builtin_bit_cast(bf16x8, w);
}
template <int NT> DI void dma4(unsigned lds, unsigned v0, unsigned v1, unsigned v2, unsigned v3, rsrc_t srd, unsigned soff) {
    if (NT) asm volatile("s_mov_b32 m0, %0\n\ts_nop 3\n\tbuffer_load_dwordx4 %1, %5, %6 offen nt lds\n\tbuffer_load_dwordx4 %2, %5, %6 offen offset:1024 nt lds\n\t"
                         "buffer_load_dwordx4 %3, %5, %6 offen offset:2048 nt lds\n\tbuffer_load_dwordx4 %4, %5, %6 offen offset:3072 nt lds"
                         :: "s"(lds), "v"(v0), "v"(v1), "v"(v2), "v"(v3), "s"(srd), "s"(soff) : "memory");
    else asm volatile("s_mov_b32 m0, %0\n\ts_nop 3\n\tbuffer_load_dwordx4 %1, %5, %6 offen lds\n\tbuffer_load_dwordx4 %2, %5, %6 offen offset:1024 lds\n\t"
                      "buffer_load_dwordx4 %3, %5, %6 offen offset:2048 lds\n\tbuffer_load_dwordx4 %4, %5, %6 offen offset:3072 lds"
                      :: "s"(lds), "v"(v0), "v"(v1), "v"(v2), "v"(v3), "s"(srd), "s"(soff) : "memory");
}
template <int NT> DI void dma1(unsigned lds, unsigned voff, rsrc_t srd, unsigned soff) {
    if (NT) asm volatile("s_mov_b32 m0, %0\n\ts_nop 3\n\tbuffer_load_dwordx4 %1, %2, %3 offen nt lds" :: "s"(lds), "v"(voff), "s"(srd), "s"(soff) : "memory");
    else asm volatile("s_mov_b32 m0, %0\n\ts_nop 3\n\tbuffer_load_dwordx4 %1, %2, %3 offen lds" :: "s"(lds), "v"(voff), "s"(srd), "s"(soff) : "memory");
}
DI void ld4_sc1(bf16x8* d, unsigned voff, rsrc_t srd, unsigned soff) {
    asm volatile("s_nop 4\n\tbuffer_load_dwordx4 %0, %4, %5, %6 offen sc1\n\tbuffer_load_dwordx4 %1, %4, %5, %6 offen offset:1024 sc1\n\t"
                 "buffer_load_dwordx4 %2, %4, %5, %6 offen offset:2048 sc1\n\tbuffer_load_dwordx4 %3, %4, %5, %6 offen offset:3072 sc1"
                 : "+v"(d[0]), "+v"(d[1]), "+v"(d[2]), "+v"(d[3]) : "v"(voff), "s"(srd), "s"(soff) : "memory");
}
constexpr unsigned BIAS = 4096;
#define WAITV_F(n, f) asm volatile("s_waitcnt vmcnt(" #n ")" : "+v"((f)[0]), "+v"((f)[1]), "+v"((f)[2]), "+v"((f)[3]) :: "memory")
DI bf16x8 lds128(lptr p) { return *(const LAS bf16x8*)p; }
DI s16x4 ldstr(lptr p) {
    typedef short v4i16_t __attribute__((ext_vector_type(4)));
    return __builtin_amdgcn_ds_read_tr16_b64_v4i16((LAS v4i16_t*)p);
}
DI bf16x8 cat(s16x4 a, s16x4 b) { return __builtin_shufflevector(a, b, 0, 1, 2, 3, 4, 5, 6, 7); }
DI float swapmax(float v) {
    auto r = __builtin_amdgcn_permlane32_swap(__float_as_uint(v), __float_as_uint(v), false, false);
    return fmaxf(__uint_as_float(r[0]), __uint_as_float(r[1]));
}
DI float swapsum(float v) {
    auto r = __builtin_amdgcn_permlane32_swap(__float_as_uint(v), __float_as_uint(v), false, false);
    return __uint_as_float(r[0]) + __uint_as_float(r[1]);
}
DI rsrc_t mkrsrc(const void* p, unsigned bytes) {
    return __builtin_amdgcn_make_buffer_rsrc((void*)p, (short)0, (int)bytes, 0x00020000);
}
DI void zero16(f32x16& v) {
#pragma unroll
    for (int r = 0; r < 16; ++r) v[r] = 0.f;
}
DI void wait_flags(const GAS u64* p, u64 tag) {
#pragma unroll 1
    for (unsigned it = 0; it < SPIN_CAP; ++it) {
        const u64 v = __hip_atomic_load(p, __ATOMIC_RELAXED, __HIP_MEMORY_SCOPE_AGENT);
        if (__all(v == tag)) break;
        __builtin_amdgcn_s_sleep(1);
    }
    asm volatile("" ::: "memory");
}

DI float max3f(float a, float b, float c) { float r; asm("v_max3_f32 %0, %1, %2, %3" : "=v"(r) : "v"(a), "v"(b), "v"(c)); return r; }
DI float fadd_s(float a, float b) { float r; asm("v_add_f32_e32 %0, %1, %2" : "=v"(r) : "v"(a), "v"(b)); return r; }
DI float fmul_s(float a, float b) { float r; asm("v_mul_f32_e32 %0, %1, %2" : "=v"(r) : "v"(a), "v"(b)); return r; }
DI float fsub_s(float a, float b) { float r; asm("v_sub_f32_e32 %0, %1, %2" : "=v"(r) : "v"(a), "v"(b)); return r; }
DI float exp_s(float a) { float r; asm("v_exp_f32_e32 %0, %1" : "=v"(r) : "v"(a)); return r; }
#define MPAD4(a) asm volatile("s_nop 7\n\ts_nop 7" : "+v"((a)[0]), "+v"((a)[1]), "+v"((a)[2]), "+v"((a)[3]))
#define PIN(x) asm volatile("" : "+v"(x))
#define PIN4(f) asm volatile("" : "+v"((f)[0]), "+v"((f)[1]), "+v"((f)[2]), "+v"((f)[3]) :: "memory")
DI lptr opaque(lptr p) { unsigned a = (unsigned)(uintptr_t)p; asm volatile("" : "+v"(a)); return (lptr)(uintptr_t)a; }
#define LA_ 6
constexpr int LA = LA_;
#define SM_THR 8.0f

struct QBase { lptr x0, x1, w; };
DI void qload(int i, bf16x8* wf, bf16x8* xf, const QBase& q) {
    const int s = i >> 2, d = i & 3;
    if (d == 0) xf[s] = lds128(((s & 1) ? q.x1 : q.x0) + 512 * (s >> 1));
    wf[i & 7] = cat(ldstr(q.w + 4096 * s + 512 * d), ldstr(q.w + 4096 * s + 512 * d + 256));
}
DI void qpre(bf16x8* wf, bf16x8* xf, const QBase& q) {
#pragma unroll
    for (int i = 0; i < LA; ++i) qload(i, wf, xf, q);
}
template <bool NEXT> DI void qsteps(f32x16* acc, bf16x8* wf, bf16x8* xf, const QBase& q, const QBase& qn) {
#pragma unroll
    for (int i = 0; i < 16; ++i) {
        SB();
        acc[i & 3] = MFMA32(wf[i & 7], xf[i >> 2], acc[i & 3]);
        PIN(acc[i & 3]);
        SB();
        if (NEXT && i + LA == 16) { WAITV(0); BARX(); }
        if (i + LA < 16) qload(i + LA, wf, xf, q); else if (NEXT) qload(i + LA - 16, wf, xf, qn);
    }
    SB();
}
template <bool FIRST = false> DI void oblock(f32x16* acc, const bf16x8* of, lptr wp_) {
    const lptr wp = opaque(wp_);
    bf16x8 wf[8];
    auto load = [&](int i) { wf[i & 7] = cat(ldstr(wp + 5120 * (i / 5) + 512 * (i % 5)), ldstr(wp + 5120 * (i / 5) + 2560 + 512 * (i % 5))); };
#pragma unroll
    for (int i = 0; i < LA; ++i) load(i);
#pragma unroll
    for (int i = 0; i < 20; ++i) {
        SB();
        if (FIRST && i < 5) { f32x16 z; zero16(z); acc[i] = MFMA32(wf[i & 7], of[0], z); }
        else acc[i % 5] = MFMA32(wf[i & 7], of[i / 5], acc[i % 5]);
        PIN(acc[i % 5]);
        SB();
        if (i + LA < 20) load(i + LA);
    }
    SB();
}
DI void aload(int i, bf16x8* fr, lptr vp, lptr kp0, lptr kp1) {
    if (i < 16) fr[i & 7] = cat(ldstr(vp + 4096 * (i >> 2) + 512 * (i & 3)), ldstr(vp + 4096 * (i >> 2) + 2048 + 512 * (i & 3)));
    else { const int s = i & 7; fr[i & 7] = lds128(((s & 1) ? kp1 : kp0) + 512 * (s >> 1) + 8192 * ((i - 16) >> 3)); }
}

__global__ __launch_bounds__(512, 2) void fused_kernel(
    const char* xA, const char* kA, const char* vA, const char* wqA, const char* woA,
    const char* xB, const char* kB, const char* vB, const char* wqB, const char* woB,
    char* ws, char* out)
{
    extern __shared__ __attribute__((aligned(16))) char smem_[];
    const lptr smem = (lptr)smem_;
    const unsigned lds0 = (unsigned)(uintptr_t)smem;
    const int tid = threadIdx.x, lane = tid & 63;
    const int wid = __builtin_amdgcn_readfirstlane(tid >> 6);
    const int bid = blockIdx.x;
    const int xg = bid & 7, ii = bid >> 3;
    const int br = xg >> 2, xq = xg & 3, b = ii >> 2, jj = ii & 3;
    const int h1 = 8 * xq + 2 * jj;
    const char* x = br ? xB : xA;
    const char* kc = br ? kB : kA;
    const char* vc = br ? vB : vA;
    const char* wq = br ? wqB : wqA;
    const char* wo = br ? woB : woA;
    const int qb = wid & 3, grp = wid >> 2, lw = wid & 3;
    const int l31 = lane & 31, h = lane >> 5;
    STAMP(0);
    const char* codebase;
    asm volatile("s_getpc_b64 %0" : "=s"(codebase));

    u64 gen;
    {
        const char* gp = ws + WS_GEN + (br * NB + b) * 256;
        asm volatile("global_load_dwordx2 %0, %1, off sc1" : "=v"(gen) : "v"(gp) : "memory");
    }

    const rsrc_t sk = mkrsrc(kc + (size_t)(b * NH + h1) * SK * DH * 2 - BIAS, 2 * SK * DH * 2 + BIAS);
    const rsrc_t sv = mkrsrc(vc + (size_t)(b * NH + h1) * SK * DH * 2 - BIAS, 2 * SK * DH * 2 + BIAS);
    const rsrc_t sof = mkrsrc(ws + WS_OF + (size_t)((br * NB + b) * NH + 8 * xq) * 32768, 8 * 32768);

    const unsigned dsub = lane >> 5, dr = (lane >> 2) & 7, dsl = lane & 3;
    const unsigned xr0 = 16u * (dsl ^ ((dr >> 2) & 3)), xr1 = 16u * (dsl ^ ((2 + (dr >> 2)) & 3));
    const unsigned vk0 = BIAS + 4096 * lw + dr * 256 + dsub * 64 + xr0, vk2 = BIAS + 4096 * lw + dr * 256 + dsub * 64 + xr1;
    const unsigned vv0 = BIAS + 4096 * lw + dr * 256 + dsub * 64 + 16 * dsl;
    const unsigned ldw = lds0 + 4096 * lw;

    auto issueK = [&](int t, unsigned pair) {
        if ((POL & 1) == ((POL >> 2) & 1) ? (POL & 1) : ((POL >> (2 * br)) & 1)) dma4<1>(ldw + pair, vk0, vk0 - 896, vk2, vk2 - 896, sk, (unsigned)t * 16384u);
        else dma4<0>(ldw + pair, vk0, vk0 - 896, vk2, vk2 - 896, sk, (unsigned)t * 16384u); };
    auto issueV = [&](int t, unsigned pair) {
        if (((POL >> 1) & 1) == ((POL >> 3) & 1) ? ((POL >> 1) & 1) : ((POL >> (2 * br + 1)) & 1)) dma4<1>(ldw + pair + SLOT, vv0, vv0 - 896, vv0, vv0 - 896, sv, (unsigned)t * 16384u);
        else dma4<0>(ldw + pair + SLOT, vv0, vv0 - 896, vv0, vv0 - 896, sv, (unsigned)t * 16384u); };
    auto nextpair = [](unsigned p) { return (p == 4u * SLOT) ? 0u : p + 2u * SLOT; };

    const int G1 = (lane >> 4) & 1, tq = (lane & 15) >> 2, tp = lane & 3;
    const int sig = ((tp & 1) << 1) | (tp >> 1);
    const unsigned wqb = SLOT + 2048 * h + 64 * tq + 32 * G1 + 8 * sig;
    const unsigned rowq = 32 * qb + l31;
    const unsigned xsw = (rowq >> 2) & 3;
    const unsigned xb0 = 1024 * (rowq >> 3) + 64 * (rowq & 7) + 16 * ((0 + h) ^ xsw);
    const unsigned xb1 = 1024 * (rowq >> 3) + 64 * (rowq & 7) + 16 * ((2 + h) ^ xsw);
    const unsigned ksw = (l31 >> 2) & 3;
    const unsigned kb0 = 2048 * (l31 >> 3) + 64 * (l31 & 7) + 16 * ((0 + h) ^ ksw);
    const unsigned kb1 = 2048 * (l31 >> 3) + 64 * (l31 & 7) + 16 * ((2 + h) ^ ksw);
    const unsigned trb = 64 * (4 * h + tq) + 32 * G1 + 8 * tp;
    const unsigned vo16 = lane * 16;

    auto flagp = [&](int bb, int stage, int w) { return (GAS u64*)(ws + WS_FLAG + (size_t)((bb * 3 + stage) * 8 + w) * 64); };
    auto partner = [&](int dj) { return (bid & ~24) | (((jj + dj) & 3) << 3); };
    auto hl1 = [&](int dj) { return 2 * ((jj + dj) & 3); };

    f32x16 acc[5];
    bf16x8 qf[8], pf[4], fr[8];
    f32x16 s0, s1;
#pragma unroll
    for (int d = 0; d < 4; ++d) zero16(acc[d]);

    auto nextstage = [](unsigned st) { return st == 8u * SLOT ? 4u * SLOT : (st == 4u * SLOT ? 6u * SLOT : (st == 6u * SLOT ? 2u * SLOT : 8u * SLOT)); };
    auto qbase = [&](unsigned st) { return QBase{opaque(smem + st + xb0), opaque(smem + st + xb1), opaque(smem + st + wqb)}; };
    const rsrc_t sx = mkrsrc(x + (size_t)b * SQ * DM * 2, SQ * DM * 2);
    const rsrc_t sw = mkrsrc(wq + (size_t)h1 * 256 - BIAS, DM * 8192 - h1 * 256 + BIAS);
    const unsigned vx0 = (32 * lw + dr) * 1280 + dsub * 64 + xr0, vx1 = (32 * lw + 8 + dr) * 1280 + dsub * 64 + xr1 - 1024;
    const unsigned vw0 = BIAS + (16 * lw + dr) * 8192 + dsub * 64 + 16 * dsl;
    const int rot = ROT ? b + 3 * jj : 0;
    constexpr unsigned T0 = 6 * SLOT, T1 = 8 * SLOT, P1 = 2 * SLOT, P2 = 4 * SLOT;
    int ci = rot >= 10 ? rot - 10 : rot;
    auto issueQn = [&](int hd, unsigned st) {
        dma4<NT_W>(ldw + st + SLOT, vw0, vw0 - 896, vw0 + 65536 - 2048, vw0 + 65536 - 2944, sw, (unsigned)ci * 524288u + 256 * hd);
        dma4<NT_X>(ldw + st, vx0, vx1, vx0 + 20480 - 2048, vx1 + 20480 - 2048, sx, 128 * ci);
        ci = ci == 9 ? 0 : ci + 1;
    };

    if (grp == 0) {
        unsigned st = 8 * SLOT;
        unsigned dummy = 0;
        {
            const unsigned line = lw * 64 + lane;
            const char* p = codebase + (line < CODE_LINES ? line : 0) * 128;
            asm volatile("global_load_dword %0, %1, off" : "+v"(dummy) : "v"(p) : "memory");
        }
        __builtin_amdgcn_s_sleep(4);
        ci = ci == 9 ? 0 : ci + 1;
        issueQn(0, P2);
        BARX();
        QBase qc = qbase(st);
        qpre(fr, pf, qc);
#pragma unroll 1
        for (int i = 0; i < 10; ++i) {
            st = nextstage(st);
            const QBase qn = qbase(st);
            qsteps<true>(acc, fr, pf, qc, qn);
            qc = qn;
        }
        WAITV(0);
        PIN(dummy);
    } else {
        issueQn(0, T1);
        WAITV(0); BARX();
        ci = ci == 9 ? 0 : ci + 1;
        issueQn(0, T0);
        BARX(); issueQn(0, P1);
        WAITV(8);  BARX(); issueQn(0, T1);
        WAITV(8);  BARX(); issueQn(0, P2);
        WAITV(8);  BARX(); issueQn(0, T0); issueK(0, 0); issueV(0, 0);
        WAITV(16); BARX(); issueQn(0, P1);
        WAITV(16); BARX(); issueQn(0, T1);
        WAITV(8);  BARX(); issueQn(0, P2);
        WAITV(8);  BARX(); issueQn(1, T0);
        WAITV(8);  BARX(); issueK(1, P1); issueV(1, P1);
        BARX(); issueQn(1, T1);
        const QBase q0 = qbase(T0), q1 = qbase(T1);
        WAITV(16); BARX();
        qpre(fr, pf, q0);
        issueK(2, P2);
        qsteps<false>(acc, fr, pf, q0, q0);
        WAITV(4);
        unsigned pv = P2;
#pragma unroll 1
        for (int c = 1; c < 9; c += 2) {
            unsigned pk = nextpair(pv);
            BARX();
            qpre(fr, pf, q1);
            issueQn(1, T0);
            qsteps<false>(acc, fr, pf, q1, q1);
            issueV(c + 1, pv); issueK(c + 2, pk);
            pv = nextpair(pk);
            WAITV(8);
            BARX();
            qpre(fr, pf, q0);
            issueQn(1, T1);
            qsteps<false>(acc, fr, pf, q0, q0);
            issueV(c + 2, pk); issueK(c + 3, pv);
            WAITV(8);
        }
        BARX();
        qpre(fr, pf, q1);
        qsteps<false>(acc, fr, pf, q1, q1);
    }
    STAMP(1);
    asm volatile("" : "+v"(gen));
    const u64 tag = gen + 1;

    __builtin_amdgcn_s_setprio(PPRIO);
    MPAD4(acc);
    {
        float cexp = CEXP; PIN(cexp);
#pragma unroll
        for (int d = 0; d < 4; ++d) {
#pragma unroll
            for (int r = 0; r < 16; ++r) acc[d][r] = fmul_s(acc[d][r], cexp);
            qf[2 * d] = pack8(acc[d], 0); qf[2 * d + 1] = pack8(acc[d], 1); zero16(acc[d]);
        }
    }
    if (grp == 0) BARX();
    float la = 0.f, lb = 0.f, lc = 0.f, ld = 0.f;
    {
        f32x16 negm, p0; zero16(negm); zero16(s1); zero16(p0);
        unsigned pk = grp ? 4 * SLOT : 0;
        lptr kp0 = opaque(smem + pk + kb0), kp1 = opaque(smem + pk + kb1);
#pragma unroll
        for (int i = 16; i < 16 + LA; ++i) aload(i, fr, kp0, kp0, kp1);
        const GAS u64* fp = flagp(partner(1 + (lane % 3)), 0, qb);
        u64 fv = 0;
        const int tw = grp ? 3 : -1;
        float thr = -__builtin_inff(), dmin = -__builtin_inff();
        u32x4 pw[4];
#pragma unroll 1
        for (int t = 0; t < 8; ++t) {
            if (__builtin_expect(t == tw, 0)) asm volatile("global_load_dwordx2 %0, %1, off sc1" : "+v"(fv) : "v"(fp) : "memory");
            float c0, c1;
#pragma unroll
            for (int j = 0; j < 16; ++j) {
                const int i = 16 + j;
                SB();
                if (j < 8) s0 = MFMA32(fr[i & 7], qf[j], j == 0 ? negm : s0); else s1 = MFMA32(fr[i & 7], qf[j - 8], j == 8 ? negm : s1);
                SB();
                if (i + LA < 32) aload(i + LA, fr, kp0, kp0, kp1);
                if (j < 8) {
                    la = fadd_s(la, s1[2 * j]); lb = fadd_s(lb, s1[2 * j + 1]); lc = fadd_s(lc, p0[2 * j]); ld = fadd_s(ld, p0[2 * j + 1]);
                    PIN(la); PIN(lb); PIN(lc); PIN(ld);
                }
                if (j >= 10) {
                    const int e = 2 * (j - 10);
                    p0[e] = exp_s(s0[e]); p0[e + 1] = exp_s(s0[e + 1]); PIN(p0);
                    if (j == 10) { c0 = max3f(s0[0], s0[1], s0[2]); c1 = max3f(s0[3], s0[4], s0[5]); PIN(c0); PIN(c1); }
                    if (j == 11) { c0 = max3f(c0, s0[6], s0[7]); c1 = max3f(c1, s0[8], s0[9]); PIN(c0); PIN(c1); }
                    if (j == 12) { c0 = max3f(c0, s0[10], s0[11]); PIN(c0); }
                    if (j == 13) { c1 = max3f(c1, s0[12], s0[13]); PIN(c1); }
                    if (j == 14) { c0 = max3f(c0, s0[14], s0[15]); PIN(c0); }
                    if (j == 15) { c0 = max3f(c0, c1, c1); PIN(c0); }
                    pw[e >> 3][(e >> 1) & 3] = cvtpk(p0[e], p0[e + 1]); PIN(pw[e >> 3][(e >> 1) & 3]);
                }
            }
            WAITV(0);
            if (__builtin_expect(t == tw, 0)) { PIN(fv); if (!__all(fv == tag)) wait_flags(fp, tag); }
            STAMP(2 + 2 * t);
            BARX();
            const lptr vp = opaque(smem + pk + SLOT + trb);
            pk = nextpair(pk);
            kp0 = opaque(smem + pk + kb0); kp1 = opaque(smem + pk + kb1);
#pragma unroll
            for (int i = 0; i < LA; ++i) aload(i, fr, vp, vp, vp);
            PIN(s1);
            SB();
            {
                float c2 = max3f(s1[0], s1[1], s1[2]), c3 = max3f(s1[3], s1[4], s1[5]);
                c1 = max3f(s1[6], s1[7], s1[8]); c0 = max3f(c0, s1[9], s1[10]);
                c2 = max3f(c2, s1[11], s1[12]); c3 = max3f(c3, s1[13], s1[14]); c1 = max3f(c1, s1[15], c0);
                c0 = max3f(c1, c2, c3);
                auto r_ = __builtin_amdgcn_permlane32_swap(__float_as_uint(c0), __float_as_uint(c0), false, false);
                const float mx = max3f(__uint_as_float(r_[0]), __uint_as_float(r_[1]), __uint_as_float(r_[1]));
                if (__builtin_expect(__any(!(mx <= thr)), 0)) {
                    const float d = fmaxf(mx, dmin);
                    float f = __builtin_amdgcn_exp2f(fminf(-d, 0.f));
                    asm volatile("s_nop 1" : "+v"(f));
#pragma unroll
                    for (int r = 0; r < 16; ++r) { s0[r] = fsub_s(s0[r], d); s1[r] = fsub_s(s1[r], d); negm[r] = fsub_s(negm[r], d); }
                    la = fmul_s(la, f); lb = fmul_s(lb, f); lc = fmul_s(lc, f); ld = fmul_s(ld, f);
#pragma unroll
                    for (int dd = 0; dd < 4; ++dd)
#pragma unroll
                        for (int r = 0; r < 16; ++r) acc[dd][r] = fmul_s(acc[dd][r], f);
#pragma unroll
                    for (int e = 0; e < 12; ++e) p0[e] = __builtin_amdgcn_exp2f(s0[e]);
#pragma unroll
                    for (int e = 0; e < 12; e += 2) pw[e >> 3][(e >> 1) & 3] = cvtpk(p0[e], p0[e + 1]);
                }
                thr = SM_THR; dmin = 0.f;
            }
#pragma unroll
            for (int i = 0; i < 16; ++i) {
                SB();
                acc[i & 3] = MFMA32(fr[i & 7], __builtin_bit_cast(bf16x8, pw[i >> 2]), acc[i & 3]);
                SB();
                constexpr int pairs[16] = {6, 7, 8, 9, 10, 11, -1, 12, 13, 14, 15, -1, -1, -1, -1, -1};
                const int q = pairs[i];
                if (q >= 0) {
                    if (q < 8) { p0[2 * q] = __builtin_amdgcn_exp2f(s0[2 * q]); p0[2 * q + 1] = __builtin_amdgcn_exp2f(s0[2 * q + 1]); PIN(p0); }
                    else { s1[2 * q - 16] = __builtin_amdgcn_exp2f(s1[2 * q - 16]); s1[2 * q - 15] = __builtin_amdgcn_exp2f(s1[2 * q - 15]); PIN(s1); }
                }
                aload(i + LA, fr, vp, kp0, kp1);
                if (q >= 0) {
                    pw[q >> 2][q & 3] = q < 8 ? cvtpk(p0[2 * q], p0[2 * q + 1]) : cvtpk(s1[2 * q - 16], s1[2 * q - 15]);
                    PIN(pw[q >> 2][q & 3]);
                }
            }
            SB();
            STAMP(3 + 2 * t);
        }
#pragma unroll
        for (int j = 0; j < 8; ++j) { la = fadd_s(la, s1[2 * j]); lb = fadd_s(lb, s1[2 * j + 1]); lc = fadd_s(lc, p0[2 * j]); ld = fadd_s(ld, p0[2 * j + 1]); }
    }
    if (grp == 0) { __builtin_amdgcn_s_setprio(0); BARX(); }
    STAMP(18);

    {
        float inv = __builtin_amdgcn_rcpf(swapsum((la + lb) + (lc + ld)));
        asm volatile("s_nop 1" : "+v"(inv));
        MPAD4(acc);
#pragma unroll
        for (int d = 0; d < 4; ++d) {
#pragma unroll
            for (int r = 0; r < 16; ++r) acc[d][r] = fmul_s(acc[d][r], inv);
            qf[2 * d] = pack8(acc[d], 0); qf[2 * d + 1] = pack8(acc[d], 1);
            __builtin_amdgcn_raw_buffer_store_b128(__builtin_bit_cast(u32x4, qf[2 * d]), sof, vo16, (2 * jj + grp) * 32768 + qb * 8192 + d * 2048, 16);
            __builtin_amdgcn_raw_buffer_store_b128(__builtin_bit_cast(u32x4, qf[2 * d + 1]), sof, vo16, (2 * jj + grp) * 32768 + qb * 8192 + d * 2048 + 1024, 16);
            SB();
        }
    }
    const rsrc_t swo = mkrsrc(wo + (size_t)(8 * xq * 128) * 1280 + 320 * jj - BIAS, 1024 * 1280 - 320 * jj + BIAS);
    const unsigned vwA = BIAS + (16 * lw + dr) * 1280 + dsub * 64 + 16 * dsl, vwB = BIAS + (16 * lw + dr) * 1280 + 16 * dsl + (dsub ? 8 * 1280 : 256) - 2048;
    auto issueW = [&](int hl, int half, int buf) {
        const unsigned so = (unsigned)(hl * 128 + 64 * half) * 1280u, ld = lds0 + buf * WT + 5 * lw * 1024;
        dma4<NT_WO>(ld, vwA, vwA - 896, vwB, vwA + 10240 + 64 - 3072, swo, so);
        dma1<NT_WO>(ld + 4096, vwA, swo, so + 10240 + 192);
    };
    const lptr x1 = smem + (lw < 2 ? lw * 8192 : 7 * WT + (lw - 2) * 8192) + vo16;
    if (grp) {
#pragma unroll
        for (int f = 0; f < 8; ++f) *(LAS bf16x8*)(x1 + 1024 * f) = qf[f];
        __builtin_amdgcn_s_setprio(0);
        BAR();
        BARX();
        WAITV(0);
        if (lane == 0) __hip_atomic_store(flagp(bid, 1, qb), tag, __ATOMIC_RELAXED, __HIP_MEMORY_SCOPE_AGENT);
        STAMP(19);
        issueW(hl1(3) + 1, 0, 0); issueW(hl1(3) + 1, 1, 7);
        wait_flags(flagp(partner(1 + (lane % 3)), 1, qb), tag);
        WAITV(0);
        STAMP(21);
        BARX();
    } else {
        bf16x8 cur[4], nxt[4];
#pragma unroll
        for (int k = 0; k < 4; ++k) { cur[k] = qf[k]; nxt[k] = qf[4 + k]; }
        auto issueO = [&](bf16x8* f, int blk) { ld4_sc1(f, vo16, sof, hl1(blk >> 1) * 32768 + qb * 8192 + 4096 * (blk & 1)); };
        issueW(hl1(0), 0, 5);
        BARX();
        issueW(hl1(0), 1, 6); issueW(hl1(1), 0, 7);
        issueV(10, 2 * SLOT); issueK(11, 4 * SLOT);
        BARX();
        issueV(11, 4 * SLOT); issueK(12, 0);
        WAITV(8);
        if (lane == 0) __hip_atomic_store(flagp(bid, 0, qb), tag, __ATOMIC_RELAXED, __HIP_MEMORY_SCOPE_AGENT);
        BARX();
        oblock<true>(acc, cur, smem + 5 * WT + trb);
        issueV(12, 0); issueK(13, 2 * SLOT);
        WAITV(8);
        BARX();
        issueO(cur, 2);
        issueW(hl1(1), 1, 5);
        oblock(acc, nxt, smem + 6 * WT + trb);
        issueO(nxt, 3);
        issueV(13, 2 * SLOT); issueK(14, 4 * SLOT);
        WAITV(17);
        PIN4(cur);
        oblock(acc, cur, smem + 7 * WT + trb);
        issueO(cur, 4);
        WAITV(12);
        BARX();
        unsigned wdummy = 0;
        {
            const int r = 64 + 40 * b + (lane < 40 ? lane : 0);
            const char* p = wo + (size_t)((8 * xq + hl1(1 + (r >> 7)) + 1) * 128 + (r & 127)) * 1280 + 320 * jj + (lw == 3 ? 316 : 128 * lw);
            asm volatile("global_load_dword %0, %1, off" : "+v"(wdummy) : "v"(p) : "memory");
        }
        issueW(hl1(2), 0, 6); issueW(hl1(2), 1, 7);
        PIN4(nxt);
        oblock(acc, nxt, smem + 5 * WT + trb);
        issueO(nxt, 5);
        issueV(14, 4 * SLOT); issueK(15, 0);
        WAITV(17);
        PIN(wdummy);
        BARX();
        issueW(hl1(3), 0, 5);
        PIN4(cur);
        oblock(acc, cur, smem + 6 * WT + trb);
        issueO(cur, 6);
        issueV(15, 0);
        WAITV(13);
        BARX();
        issueW(hl1(3), 1, 6); issueW(hl1(0) + 1, 0, 2); issueW(hl1(0) + 1, 1, 3);
        PIN4(nxt);
        oblock(acc, nxt, smem + 7 * WT + trb);
        issueO(nxt, 7);
        WAITV(4);
        BARX();
        issueW(hl1(1) + 1, 0, 4);
        PIN4(cur);
        oblock(acc, cur, smem + 5 * WT + trb);
        WAITV(5);
        PIN4(nxt);
        oblock(acc, nxt, smem + 6 * WT + trb);
        WAITV(0);
        BARX();
        bf16x8 of2[24];
#pragma unroll
        for (int f = 0; f < 8; ++f) of2[f] = lds128(x1 + 1024 * f);
        BAR();
        issueW(hl1(1) + 1, 1, 5); issueW(hl1(2) + 1, 0, 6); issueW(hl1(2) + 1, 1, 1);
        oblock(acc, of2, smem + 2 * WT + trb);
        oblock(acc, of2 + 4, smem + 3 * WT + trb);
        WAITV(0);
        STAMP(20);
        BARX();
        const unsigned ob = qb * 8192 + 32768;
        auto ldo = [&](int k0) {
#pragma unroll
            for (int k = k0; k < k0 + 4; ++k)
                of2[k] = __builtin_bit_cast(bf16x8, __builtin_amdgcn_raw_buffer_load_b128(sof, vo16, (unsigned)hl1(1 + (k >> 3)) * 32768 + ob + (k & 7) * 1024, 16));
        };
        ldo(0); ldo(4); SB();
        oblock(acc, of2, smem + 4 * WT + trb);       ldo(8);  ldo(12); SB();
        oblock(acc, of2 + 4, smem + 5 * WT + trb);   ldo(16); SB();
        oblock(acc, of2 + 8, smem + 6 * WT + trb);   ldo(20); SB();
        oblock(acc, of2 + 12, smem + 1 * WT + trb);
        oblock(acc, of2 + 16, smem + 0 * WT + trb);
        oblock(acc, of2 + 20, smem + 7 * WT + trb);
        STAMP(22);
        const rsrc_t spb = mkrsrc(ws + WS_PB, 256 * PB_WG);
        f32x4 mine[5];
#define UNIT(u, e) acc[(u) >> 2][4 * ((u) & 3) + (e)]
#define UPK(u) cvtpk(UNIT(u, 0), UNIT(u, 1)), cvtpk(UNIT(u, 2), UNIT(u, 3))
#pragma unroll
        for (int g = 0; g < 4; ++g) {
            if (g == xq) {
#pragma unroll
                for (int n = 0; n < 5; ++n) mine[n] = (f32x4){UNIT(5 * g + n, 0), UNIT(5 * g + n, 1), UNIT(5 * g + n, 2), UNIT(5 * g + n, 3)};
            } else {
                const unsigned so = (unsigned)((bid & ~3) | g) * PB_WG + (xq * 4 + qb) * 5120;
                const u32x4 w01 = {UPK(5 * g), UPK(5 * g + 1)}, w23 = {UPK(5 * g + 2), UPK(5 * g + 3)};
                const u32x2 w4 = {UPK(5 * g + 4)};
                __builtin_amdgcn_raw_buffer_store_b128(w01, spb, vo16, so, 16);
                __builtin_amdgcn_raw_buffer_store_b128(w23, spb, vo16, so + 1024, 16);
                __builtin_amdgcn_raw_buffer_store_b64(w4, spb, lane * 8, so + 2048, 16);
            }
        }
        WAITV(0);
        if (lane == 0) __hip_atomic_store(flagp(bid, 2, qb), tag, __ATOMIC_RELAXED, __HIP_MEMORY_SCOPE_AGENT);
        STAMP(23);
        wait_flags(flagp((bid & ~3) | ((xq + 1 + (lane % 3)) & 3), 2, qb), tag);
        STAMP(24);
        {
            const rsrc_t sout = mkrsrc(out + ((size_t)b * SQ * (2 * DM) + br * DM + 160 * jj + 40 * xq) * 2, SQ * 2 * DM * 2 - (br * DM + 160 * jj + 40 * xq) * 2);
            const unsigned vop = rowq * (4 * DM) + 8 * h;
            u32x4 a01[3], a23[3]; u32x2 a4[3];
#pragma unroll
            for (int s = 0; s < 3; ++s) {
                const unsigned so = (unsigned)bid * PB_WG + (((xq + 1 + s) & 3) * 4 + qb) * 5120;
                a01[s] = __builtin_amdgcn_raw_buffer_load_b128(spb, vo16, so, 16);
                a23[s] = __builtin_amdgcn_raw_buffer_load_b128(spb, vo16, so + 1024, 16);
                a4[s] = __builtin_amdgcn_raw_buffer_load_b64(spb, lane * 8, so + 2048, 16);
            }
            auto up = [](unsigned lo, unsigned hi) { return (f32x4){__uint_as_float(lo << 16), __uint_as_float(lo & 0xffff0000u), __uint_as_float(hi << 16), __uint_as_float(hi & 0xffff0000u)}; };
#pragma unroll
            for (int s = 0; s < 3; ++s) {
                mine[0] += up(a01[s][0], a01[s][1]); mine[1] += up(a01[s][2], a01[s][3]);
                mine[2] += up(a23[s][0], a23[s][1]); mine[3] += up(a23[s][2], a23[s][3]); mine[4] += up(a4[s][0], a4[s][1]);
            }
#pragma unroll
            for (int n = 0; n < 5; ++n) __builtin_amdgcn_raw_buffer_store_b64((u32x2){cvtpk(mine[n][0], mine[n][1]), cvtpk(mine[n][2], mine[n][3])}, sout, vop, 16 * n, OUT_AUX);
        }
    }
    {
        if (xq == 0 && jj == 0 && wid == 0 && lane == 0)
            __hip_atomic_store((GAS u64*)(ws + WS_GEN + (br * NB + b) * 256), tag, __ATOMIC_RELAXED, __HIP_MEMORY_SCOPE_AGENT);
        STAMP(25);
    }
}

extern "C" void kernel_launch(void* const* d_in, const int* in_sizes, int n_in, void* d_out, int out_size,
                              void* d_ws, size_t ws_size, hipStream_t stream) {
    (void)hipFuncSetAttribute((const void*)fused_kernel, hipFuncAttributeMaxDynamicSharedMemorySize, LDS_BYTES);
    hipLaunchKernelGGL(fused_kernel, dim3(256), dim3(512), LDS_BYTES, stream,
                       (const char*)d_in[0], (const char*)d_in[1], (const char*)d_in[2], (const char*)d_in[3], (const char*)d_in[4],
                       (const char*)d_in[5], (const char*)d_in[6], (const char*)d_in[7], (const char*)d_in[8], (const char*)d_in[9],
                       (char*)d_ws + WS_SHIFT, (char*)d_out);
}
```

```cpp
#include <hip/hip_runtime.h>
#include <hip/hip_bf16.h>
#include <stdint.h>
#include <type_traits>

typedef short bf16x8 __attribute__((ext_vector_type(8)));
typedef short s16x4 __attribute__((ext_vector_type(4)));
typedef float f32x16 __attribute__((ext_vector_type(16)));
typedef float f32x4 __attribute__((ext_vector_type(4)));
typedef float f32x2 __attribute__((ext_vector_type(2)));
typedef __bf16 bf16x2_t __attribute__((ext_vector_type(2)));
typedef unsigned u32x4 __attribute__((ext_vector_type(4)));
typedef unsigned u32x2 __attribute__((ext_vector_type(2)));
typedef unsigned long long u64;
#define LAS __attribute__((address_space(3)))
#define GAS __attribute__((address_space(1)))
#define DI __device__ __forceinline__
typedef LAS char* lptr;
typedef __amdgpu_buffer_rsrc_t rsrc_t;

constexpr int NB = 8, SQ = 128, DM = 640, NH = 32, SK = 512, DH = 128;
constexpr float CEXP = 0.08838834764831845f * 1.4426950408889634f;
constexpr int SLOT = 16384;
constexpr int LDS_T = 6 * SLOT;
constexpr int WT = 20480;
constexpr int LDS_BYTES = 10 * SLOT;
constexpr size_t WS_GEN = 0, WS_FLAG = 8192, WS_OF = 1 << 20;
constexpr size_t OF_BYTES = (size_t)2 * NB * NH * 32768;
constexpr size_t WS_PB = WS_OF + OF_BYTES;
constexpr unsigned PB_WG = 4 * 8 * 3 * 1024;
#define NT_W 0
#define NT_X 0
#define NT_WO 0
#define PPRIO 2
#define PBF16 1
#define WS_SHIFT 0
#define POL 15
#define PRETOUCH 1
#define CL 180
constexpr unsigned CODE_LINES = CL;
#define WOWARM 1
#define ROT 1
#define OUT_AUX 0
#define PROBE_BID 77
constexpr unsigned SPIN_CAP = 1u << 16;

#define MFMA32(a, b, c) __builtin_amdgcn_mfma_f32_32x32x16_bf16((a), (b), (c), 0, 0, 0)
#define WAITV(n) asm volatile("s_waitcnt vmcnt(" #n ")" ::: "memory")
#define SB() __builtin_amdgcn_sched_barrier(0)
#define BARX() do { SB(); asm volatile("s_barrier" ::: "memory"); SB(); } while (0)
#define BAR() do { SB(); asm volatile("s_waitcnt lgkmcnt(0)\n\ts_barrier" ::: "memory"); SB(); } while (0)

#define STAMP(idx)

DI unsigned cvtpk(float lo, float hi) {
    f32x2 v = {lo, hi};
    bf16x2_t b = __builtin_convertvector(v, bf16x2_t);
    return __builtin_bit_cast(unsigned, b);
}
DI bf16x8 pack8(const f32x16& x, int s) {
    u32x4 w = {cvtpk(x[8 * s + 0], x[8 * s + 1]), cvtpk(x[8 * s + 2], x[8 * s + 3]),
               cvtpk(x[8 * s + 4], x[8 * s + 5]), cvtpk(x[8 * s + 6], x[8 * s + 7])};
    return __builtin_bit_cast(bf16x8, w);
}
template <int NT> DI void dma4(unsigned lds, unsigned v0, unsigned v1, unsigned v2, unsigned v3, rsrc_t srd, unsigned soff) {
    if (NT) asm volatile("s_mov_b32 m0, %0\n\ts_nop 3\n\tbuffer_load_dwordx4 %1, %5, %6 offen nt lds\n\tbuffer_load_dwordx4 %2, %5, %6 offen offset:1024 nt lds\n\t"
                         "buffer_load_dwordx4 %3, %5, %6 offen offset:2048 nt lds\n\tbuffer_load_dwordx4 %4, %5, %6 offen offset:3072 nt lds"
                         :: "s"(lds), "v"(v0), "v"(v1), "v"(v2), "v"(v3), "s"(srd), "s"(soff) : "memory");
    else asm volatile("s_mov_b32 m0, %0\n\ts_nop 3\n\tbuffer_load_dwordx4 %1, %5, %6 offen lds\n\tbuffer_load_dwordx4 %2, %5, %6 offen offset:1024 lds\n\t"
                      "buffer_load_dwordx4 %3, %5, %6 offen offset:2048 lds\n\tbuffer_load_dwordx4 %4, %5, %6 offen offset:3072 lds"
                      :: "s"(lds), "v"(v0), "v"(v1), "v"(v2), "v"(v3), "s"(srd), "s"(soff) : "memory");
}
template <int NT> DI void dma1(unsigned lds, unsigned voff, rsrc_t srd, unsigned soff) {
    if (NT) asm volatile("s_mov_b32 m0, %0\n\ts_nop 3\n\tbuffer_load_dwordx4 %1, %2, %3 offen nt lds" :: "s"(lds), "v"(voff), "s"(srd), "s"(soff) : "memory");
    else asm volatile("s_mov_b32 m0, %0\n\ts_nop 3\n\tbuffer_load_dwordx4 %1, %2, %3 offen lds" :: "s"(lds), "v"(voff), "s"(srd), "s"(soff) : "memory");
}
DI void ld4_sc1(bf16x8* d, unsigned voff, rsrc_t srd, unsigned soff) {
    asm volatile("s_nop 4\n\tbuffer_load_dwordx4 %0, %4, %5, %6 offen sc1\n\tbuffer_load_dwordx4 %1, %4, %5, %6 offen offset:1024 sc1\n\t"
                 "buffer_load_dwordx4 %2, %4, %5, %6 offen offset:2048 sc1\n\tbuffer_load_dwordx4 %3, %4, %5, %6 offen offset:3072 sc1"
                 : "+v"(d[0]), "+v"(d[1]), "+v"(d[2]), "+v"(d[3]) : "v"(voff), "s"(srd), "s"(soff) : "memory");
}
constexpr unsigned BIAS = 4096;
#define WAITV_F(n, f) asm volatile("s_waitcnt vmcnt(" #n ")" : "+v"((f)[0]), "+v"((f)[1]), "+v"((f)[2]), "+v"((f)[3]) :: "memory")
DI bf16x8 lds128(lptr p) { return *(const LAS bf16x8*)p; }
DI s16x4 ldstr(lptr p) {
    typedef short v4i16_t __attribute__((ext_vector_type(4)));
    return __builtin_amdgcn_ds_read_tr16_b64_v4i16((LAS v4i16_t*)p);
}
DI bf16x8 cat(s16x4 a, s16x4 b) { return __builtin_shufflevector(a, b, 0, 1, 2, 3, 4, 5, 6, 7); }
DI float swapmax(float v) {
    auto r = __builtin_amdgcn_permlane32_swap(__float_as_uint(v), __float_as_uint(v), false, false);
    return fmaxf(__uint_as_float(r[0]), __uint_as_float(r[1]));
}
DI float swapsum(float v) {
    auto r = __builtin_amdgcn_permlane32_swap(__float_as_uint(v), __float_as_uint(v), false, false);
    return __uint_as_float(r[0]) + __uint_as_float(r[1]);
}
DI rsrc_t mkrsrc(const void* p, unsigned bytes) {
    return __builtin_amdgcn_make_buffer_rsrc((void*)p, (short)0, (int)bytes, 0x00020000);
}
DI void zero16(f32x16& v) {
#pragma unroll
    for (int r = 0; r < 16; ++r) v[r] = 0.f;
}
DI void wait_flags(const GAS u64* p, u64 tag) {
#pragma unroll 1
    for (unsigned it = 0; it < SPIN_CAP; ++it) {
        const u64 v = __hip_atomic_load(p, __ATOMIC_RELAXED, __HIP_MEMORY_SCOPE_AGENT);
        if (__all(v == tag)) break;
        __builtin_amdgcn_s_sleep(1);
    }
    asm volatile("" ::: "memory");
}

DI float max3f(float a, float b, float c) { float r; asm("v_max3_f32 %0, %1, %2, %3" : "=v"(r) : "v"(a), "v"(b), "v"(c)); return r; }
DI float fadd_s(float a, float b) { float r; asm("v_add_f32_e32 %0, %1, %2" : "=v"(r) : "v"(a), "v"(b)); return r; }
DI float fmul_s(float a, float b) { float r; asm("v_mul_f32_e32 %0, %1, %2" : "=v"(r) : "v"(a), "v"(b)); return r; }
DI float fsub_s(float a, float b) { float r; asm("v_sub_f32_e32 %0, %1, %2" : "=v"(r) : "v"(a), "v"(b)); return r; }
DI float exp_s(float a) { float r; asm("v_exp_f32_e32 %0, %1" : "=v"(r) : "v"(a)); return r; }
#define MPAD4(a) asm volatile("s_nop 7\n\ts_nop 7" : "+v"((a)[0]), "+v"((a)[1]), "+v"((a)[2]), "+v"((a)[3]))
#define PIN(x) asm volatile("" : "+v"(x))
#define PIN4(f) asm volatile("" : "+v"((f)[0]), "+v"((f)[1]), "+v"((f)[2]), "+v"((f)[3]) :: "memory")
DI lptr opaque(lptr p) { unsigned a = (unsigned)(uintptr_t)p; asm volatile("" : "+v"(a)); return (lptr)(uintptr_t)a; }
#define LA_ 6
constexpr int LA = LA_;
#define SM_THR 8.0f

struct QBase { lptr x0, x1, w; };
DI void qload(int i, bf16x8* wf, bf16x8* xf, const QBase& q) {
    const int s = i >> 2, d = i & 3;
    if (d == 0) xf[s] = lds128(((s & 1) ? q.x1 : q.x0) + 512 * (s >> 1));
    wf[i & 7] = cat(ldstr(q.w + 4096 * s + 512 * d), ldstr(q.w + 4096 * s + 512 * d + 256));
}
DI void qpre(bf16x8* wf, bf16x8* xf, const QBase& q) {
#pragma unroll
    for (int i = 0; i < LA; ++i) qload(i, wf, xf, q);
}
template <bool NEXT> DI void qsteps(f32x16* acc, bf16x8* wf, bf16x8* xf, const QBase& q, const QBase& qn) {
#pragma unroll
    for (int i = 0; i < 16; ++i) {
        SB();
        acc[i & 3] = MFMA32(wf[i & 7], xf[i >> 2], acc[i & 3]);
        PIN(acc[i & 3]);
        SB();
        if (NEXT && i + LA == 16) BARX();
        if (i + LA < 16) qload(i + LA, wf, xf, q); else if (NEXT) qload(i + LA - 16, wf, xf, qn);
    }
    SB();
}
template <bool FIRST = false> DI void oblock(f32x16* acc, const bf16x8* of, lptr wp_) {
    const lptr wp = opaque(wp_);
    bf16x8 wf[8];
    auto load = [&](int i) { wf[i & 7] = cat(ldstr(wp + 5120 * (i / 5) + 512 * (i % 5)), ldstr(wp + 5120 * (i / 5) + 2560 + 512 * (i % 5))); };
#pragma unroll
    for (int i = 0; i < LA; ++i) load(i);
#pragma unroll
    for (int i = 0; i < 20; ++i) {
        SB();
        if (FIRST && i < 5) { f32x16 z; zero16(z); acc[i] = MFMA32(wf[i & 7], of[0], z); }
        else acc[i % 5] = MFMA32(wf[i & 7], of[i / 5], acc[i % 5]);
        PIN(acc[i % 5]);
        SB();
        if (i + LA < 20) load(i + LA);
    }
    SB();
}
DI void aload(int i, bf16x8* fr, lptr vp, lptr kp0, lptr kp1) {
    if (i < 16) fr[i & 7] = cat(ldstr(vp + 4096 * (i >> 2) + 512 * (i & 3)), ldstr(vp + 4096 * (i >> 2) + 2048 + 512 * (i & 3)));
    else { const int s = i & 7; fr[i & 7] = lds128(((s & 1) ? kp1 : kp0) + 512 * (s >> 1) + 8192 * ((i - 16) >> 3)); }
}

__global__ __launch_bounds__(512, 2) void fused_kernel(
    const char* xA, const char* kA, const char* vA, const char* wqA, const char* woA,
    const char* xB, const char* kB, const char* vB, const char* wqB, const char* woB,
    char* ws, char* out)
{
    extern __shared__ __attribute__((aligned(16))) char smem_[];
    const lptr smem = (lptr)smem_;
    const unsigned lds0 = (unsigned)(uintptr_t)smem;
    const int tid = threadIdx.x, lane = tid & 63;
    const int wid = __builtin_amdgcn_readfirstlane(tid >> 6);
    const int bid = blockIdx.x;
    const int xg = bid & 7, ii = bid >> 3;
    const int br = xg >> 2, xq = xg & 3, b = ii >> 2, jj = ii & 3;
    const int h1 = 8 * xq + 2 * jj;
    const char* x = br ? xB : xA;
    const char* kc = br ? kB : kA;
    const char* vc = br ? vB : vA;
    const char* wq = br ? wqB : wqA;
    const char* wo = br ? woB : woA;
    const int qb = wid & 3, grp = wid >> 2, lw = wid & 3;
    const int l31 = lane & 31, h = lane >> 5;
    STAMP(0);
    const char* codebase;
    asm volatile("s_getpc_b64 %0" : "=s"(codebase));

    u64 gen;
    {
        const char* gp = ws + WS_GEN + (br * NB + b) * 256;
        asm volatile("global_load_dwordx2 %0, %1, off sc1" : "=v"(gen) : "v"(gp) : "memory");
    }

    const rsrc_t sk = mkrsrc(kc + (size_t)(b * NH + h1) * SK * DH * 2 - BIAS, 2 * SK * DH * 2 + BIAS);
    const rsrc_t sv = mkrsrc(vc + (size_t)(b * NH + h1) * SK * DH * 2 - BIAS, 2 * SK * DH * 2 + BIAS);
    const rsrc_t sof = mkrsrc(ws + WS_OF + (size_t)((br * NB + b) * NH + 8 * xq) * 32768, 8 * 32768);

    const unsigned dsub = lane >> 5, dr = (lane >> 2) & 7, dsl = lane & 3;
    const unsigned xr0 = 16u * (dsl ^ ((dr >> 2) & 3)), xr1 = 16u * (dsl ^ ((2 + (dr >> 2)) & 3));
    const unsigned vk0 = BIAS + 4096 * lw + dr * 256 + dsub * 64 + xr0, vk2 = BIAS + 4096 * lw + dr * 256 + dsub * 64 + xr1;
    const unsigned vv0 = BIAS + 4096 * lw + dr * 256 + dsub * 64 + 16 * dsl;
    const unsigned ldw = lds0 + 4096 * lw;

    auto issueK = [&](int t, unsigned pair) {
        if ((POL & 1) == ((POL >> 2) & 1) ? (POL & 1) : ((POL >> (2 * br)) & 1)) dma4<1>(ldw + pair, vk0, vk0 - 896, vk2, vk2 - 896, sk, (unsigned)t * 16384u);
        else dma4<0>(ldw + pair, vk0, vk0 - 896, vk2, vk2 - 896, sk, (unsigned)t * 16384u); };
    auto issueV = [&](int t, unsigned pair) {
        if (((POL >> 1) & 1) == ((POL >> 3) & 1) ? ((POL >> 1) & 1) : ((POL >> (2 * br + 1)) & 1)) dma4<1>(ldw + pair + SLOT, vv0, vv0 - 896, vv0, vv0 - 896, sv, (unsigned)t * 16384u);
        else dma4<0>(ldw + pair + SLOT, vv0, vv0 - 896, vv0, vv0 - 896, sv, (unsigned)t * 16384u); };
    auto nextpair = [](unsigned p) { return (p == 4u * SLOT) ? 0u : p + 2u * SLOT; };

    const int G1 = (lane >> 4) & 1, tq = (lane & 15) >> 2, tp = lane & 3;
    const int sig = ((tp & 1) << 1) | (tp >> 1);
    const unsigned wqb = SLOT + 2048 * h + 64 * tq + 32 * G1 + 8 * sig;
    const unsigned rowq = 32 * qb + l31;
    const unsigned xsw = (rowq >> 2) & 3;
    const unsigned xb0 = 1024 * (rowq >> 3) + 64 * (rowq & 7) + 16 * ((0 + h) ^ xsw);
    const unsigned xb1 = 1024 * (rowq >> 3) + 64 * (rowq & 7) + 16 * ((2 + h) ^ xsw);
    const unsigned ksw = (l31 >> 2) & 3;
    const unsigned kb0 = 2048 * (l31 >> 3) + 64 * (l31 & 7) + 16 * ((0 + h) ^ ksw);
    const unsigned kb1 = 2048 * (l31 >> 3) + 64 * (l31 & 7) + 16 * ((2 + h) ^ ksw);
    const unsigned trb = 64 * (4 * h + tq) + 32 * G1 + 8 * tp;
    const unsigned vo16 = lane * 16;

    auto flagp = [&](int bb, int stage, int w) { return (GAS u64*)(ws + WS_FLAG + (size_t)((bb * 3 + stage) * 8 + w) * 64); };
    auto partner = [&](int dj) { return (bid & ~24) | (((jj + dj) & 3) << 3); };
    auto hl1 = [&](int dj) { return 2 * ((jj + dj) & 3); };

    f32x16 acc[5];
    bf16x8 qf[8], pf[4], fr[8];
    f32x16 s0, s1;
#pragma unroll
    for (int d = 0; d < 4; ++d) zero16(acc[d]);

    auto nextstage = [](unsigned st) { return st == 8u * SLOT ? 4u * SLOT : (st == 4u * SLOT ? 6u * SLOT : (st == 6u * SLOT ? 2u * SLOT : 8u * SLOT)); };
    auto qbase = [&](unsigned st) { return QBase{opaque(smem + st + xb0), opaque(smem + st + xb1), opaque(smem + st + wqb)}; };
    if (grp == 0) {
        unsigned st = 8 * SLOT;
        unsigned dummy = 0;
        {
            const unsigned line = lw * 64 + lane;
            const char* p = codebase + (line < CODE_LINES ? line : 0) * 128;
            asm volatile("global_load_dword %0, %1, off" : "+v"(dummy) : "v"(p) : "memory");
        }
        BARX();
        QBase qc = qbase(st);
        qpre(fr, pf, qc);
#pragma unroll 1
        for (int i = 0; i < 10; ++i) {
            st = nextstage(st);
            const QBase qn = qbase(st);
            qsteps<true>(acc, fr, pf, qc, qn);
            qc = qn;
        }
        WAITV(0);
        PIN(dummy);
    } else {
        const rsrc_t sx = mkrsrc(x + (size_t)b * SQ * DM * 2, SQ * DM * 2);
        const rsrc_t sw = mkrsrc(wq + (size_t)h1 * 256 - BIAS, DM * 8192 - h1 * 256 + BIAS);
        const unsigned vx0 = (32 * lw + dr) * 1280 + dsub * 64 + xr0, vx1 = (32 * lw + 8 + dr) * 1280 + dsub * 64 + xr1 - 1024;
        const unsigned vw0 = BIAS + (16 * lw + dr) * 8192 + dsub * 64 + 16 * dsl;
        const int rot = ROT ? b + 3 * jj : 0;
        constexpr unsigned T0 = 6 * SLOT, T1 = 8 * SLOT, P1 = 2 * SLOT, P2 = 4 * SLOT;
        int ci = rot >= 10 ? rot - 10 : rot;
        auto issueQn = [&](int hd, unsigned st) {
            dma4<NT_W>(ldw + st + SLOT, vw0, vw0 - 896, vw0 + 65536 - 2048, vw0 + 65536 - 2944, sw, (unsigned)ci * 524288u + 256 * hd);
            dma4<NT_X>(ldw + st, vx0, vx1, vx0 + 20480 - 2048, vx1 + 20480 - 2048, sx, 128 * ci);
            ci = ci == 9 ? 0 : ci + 1;
        };
        issueQn(0, T1);
        WAITV(0); BARX();
        issueQn(0, P2); issueQn(0, T0);
        WAITV(8);  BARX(); issueQn(0, P1);
        WAITV(8);  BARX(); issueQn(0, T1);
        WAITV(8);  BARX(); issueQn(0, P2);
        WAITV(8);  BARX(); issueQn(0, T0); issueK(0, 0); issueV(0, 0);
        WAITV(16); BARX(); issueQn(0, P1);
        WAITV(16); BARX(); issueQn(0, T1);
        WAITV(8);  BARX(); issueQn(0, P2);
        WAITV(8);  BARX(); issueQn(1, T0);
        WAITV(8);  BARX(); issueK(1, P1); issueV(1, P1);
        BARX(); issueQn(1, T1);
        const QBase q0 = qbase(T0), q1 = qbase(T1);
        WAITV(16); BARX();
        qpre(fr, pf, q0);
        issueK(2, P2);
        qsteps<false>(acc, fr, pf, q0, q0);
        WAITV(4);
        unsigned pv = P2;
#pragma unroll 1
        for (int c = 1; c < 9; c += 2) {
            unsigned pk = nextpair(pv);
            BARX();
            qpre(fr, pf, q1);
            issueQn(1, T0);
            qsteps<false>(acc, fr, pf, q1, q1);
            issueV(c + 1, pv); issueK(c + 2, pk);
            pv = nextpair(pk);
            WAITV(8);
            BARX();
            qpre(fr, pf, q0);
            issueQn(1, T1);
            qsteps<false>(acc, fr, pf, q0, q0);
            issueV(c + 2, pk); issueK(c + 3, pv);
            WAITV(8);
        }
        BARX();
        qpre(fr, pf, q1);
        qsteps<false>(acc, fr, pf, q1, q1);
    }
    STAMP(1);
    asm volatile("" : "+v"(gen));
    const u64 tag = gen + 1;

    __builtin_amdgcn_s_setprio(PPRIO);
    MPAD4(acc);
    {
        float cexp = CEXP; PIN(cexp);
#pragma unroll
        for (int d = 0; d < 4; ++d) {
#pragma unroll
            for (int r = 0; r < 16; ++r) acc[d][r] = fmul_s(acc[d][r], cexp);
            qf[2 * d] = pack8(acc[d], 0); qf[2 * d + 1] = pack8(acc[d], 1); zero16(acc[d]);
        }
    }
    if (grp == 0) BARX();
    float la = 0.f, lb = 0.f, lc = 0.f, ld = 0.f;
    {
        f32x16 negm, p0; zero16(negm); zero16(s1); zero16(p0);
        unsigned pk = grp ? 4 * SLOT : 0;
        lptr kp0 = opaque(smem + pk + kb0), kp1 = opaque(smem + pk + kb1);
#pragma unroll
        for (int i = 16; i < 16 + LA; ++i) aload(i, fr, kp0, kp0, kp1);
        const GAS u64* fp = flagp(partner(1 + (lane % 3)), 0, qb);
        u64 fv = 0;
        const int tw = grp ? 3 : -1;
        float thr = -__builtin_inff(), dmin = -__builtin_inff();
        u32x4 pw[4];
#pragma unroll 1
        for (int t = 0; t < 8; ++t) {
            if (__builtin_expect(t == tw, 0)) asm volatile("global_load_dwordx2 %0, %1, off sc1" : "+v"(fv) : "v"(fp) : "memory");
            float c0, c1;
#pragma unroll
            for (int j = 0; j < 16; ++j) {
                const int i = 16 + j;
                SB();
                if (j < 8) s0 = MFMA32(fr[i & 7], qf[j], j == 0 ? negm : s0); else s1 = MFMA32(fr[i & 7], qf[j - 8], j == 8 ? negm : s1);
                SB();
                if (i + LA < 32) aload(i + LA, fr, kp0, kp0, kp1);
                if (j < 8) {
                    la = fadd_s(la, s1[2 * j]); lb = fadd_s(lb, s1[2 * j + 1]); lc = fadd_s(lc, p0[2 * j]); ld = fadd_s(ld, p0[2 * j + 1]);
                    PIN(la); PIN(lb); PIN(lc); PIN(ld);
                }
                if (j >= 10) {
                    const int e = 2 * (j - 10);
                    p0[e] = exp_s(s0[e]); p0[e + 1] = exp_s(s0[e + 1]); PIN(p0);
                    if (j == 10) { c0 = max3f(s0[0], s0[1], s0[2]); c1 = max3f(s0[3], s0[4], s0[5]); PIN(c0); PIN(c1); }
                    if (j == 11) { c0 = max3f(c0, s0[6], s0[7]); c1 = max3f(c1, s0[8], s0[9]); PIN(c0); PIN(c1); }
                    if (j == 12) { c0 = max3f(c0, s0[10], s0[11]); PIN(c0); }
                    if (j == 13) { c1 = max3f(c1, s0[12], s0[13]); PIN(c1); }
                    if (j == 14) { c0 = max3f(c0, s0[14], s0[15]); PIN(c0); }
                    if (j == 15) { c0 = max3f(c0, c1, c1); PIN(c0); }
                    pw[e >> 3][(e >> 1) & 3] = cvtpk(p0[e], p0[e + 1]); PIN(pw[e >> 3][(e >> 1) & 3]);
                }
            }
            WAITV(0);
            if (__builtin_expect(t == tw, 0)) { PIN(fv); if (!__all(fv == tag)) wait_flags(fp, tag); }
            STAMP(2 + 2 * t);
            BARX();
            const lptr vp = opaque(smem + pk + SLOT + trb);
            pk = nextpair(pk);
            kp0 = opaque(smem + pk + kb0); kp1 = opaque(smem + pk + kb1);
#pragma unroll
            for (int i = 0; i < LA; ++i) aload(i, fr, vp, vp, vp);
            PIN(s1);
            SB();
            {
                float c2 = max3f(s1[0], s1[1], s1[2]), c3 = max3f(s1[3], s1[4], s1[5]);
                c1 = max3f(s1[6], s1[7], s1[8]); c0 = max3f(c0, s1[9], s1[10]);
                c2 = max3f(c2, s1[11], s1[12]); c3 = max3f(c3, s1[13], s1[14]); c1 = max3f(c1, s1[15], c0);
                c0 = max3f(c1, c2, c3);
                auto r_ = __builtin_amdgcn_permlane32_swap(__float_as_uint(c0), __float_as_uint(c0), false, false);
                const float mx = max3f(__uint_as_float(r_[0]), __uint_as_float(r_[1]), __uint_as_float(r_[1]));
                if (__builtin_expect(__any(!(mx <= thr)), 0)) {
                    const float d = fmaxf(mx, dmin);
                    float f = __builtin_amdgcn_exp2f(fminf(-d, 0.f));
                    asm volatile("s_nop 1" : "+v"(f));
#pragma unroll
                    for (int r = 0; r < 16; ++r) { s0[r] = fsub_s(s0[r], d); s1[r] = fsub_s(s1[r], d); negm[r] = fsub_s(negm[r], d); }
                    la = fmul_s(la, f); lb = fmul_s(lb, f); lc = fmul_s(lc, f); ld = fmul_s(ld, f);
#pragma unroll
                    for (int dd = 0; dd < 4; ++dd)
#pragma unroll
                        for (int r = 0; r < 16; ++r) acc[dd][r] = fmul_s(acc[dd][r], f);
#pragma unroll
                    for (int e = 0; e < 12; ++e) p0[e] = __builtin_amdgcn_exp2f(s0[e]);
#pragma unroll
                    for (int e = 0; e < 12; e += 2) pw[e >> 3][(e >> 1) & 3] = cvtpk(p0[e], p0[e + 1]);
                }
                thr = SM_THR; dmin = 0.f;
            }
#pragma unroll
            for (int i = 0; i < 16; ++i) {
                SB();
                acc[i & 3] = MFMA32(fr[i & 7], __builtin_bit_cast(bf16x8, pw[i >> 2]), acc[i & 3]);
                SB();
                constexpr int pairs[16] = {6, 7, 8, 9, 10, 11, -1, 12, 13, 14, 15, -1, -1, -1, -1, -1};
                const int q = pairs[i];
                if (q >= 0) {
                    if (q < 8) { p0[2 * q] = __builtin_amdgcn_exp2f(s0[2 * q]); p0[2 * q + 1] = __builtin_amdgcn_exp2f(s0[2 * q + 1]); PIN(p0); }
                    else { s1[2 * q - 16] = __builtin_amdgcn_exp2f(s1[2 * q - 16]); s1[2 * q - 15] = __builtin_amdgcn_exp2f(s1[2 * q - 15]); PIN(s1); }
                }
                aload(i + LA, fr, vp, kp0, kp1);
                if (q >= 0) {
                    pw[q >> 2][q & 3] = q < 8 ? cvtpk(p0[2 * q], p0[2 * q + 1]) : cvtpk(s1[2 * q - 16], s1[2 * q - 15]);
                    PIN(pw[q >> 2][q & 3]);
                }
            }
            SB();
            STAMP(3 + 2 * t);
        }
#pragma unroll
        for (int j = 0; j < 8; ++j) { la = fadd_s(la, s1[2 * j]); lb = fadd_s(lb, s1[2 * j + 1]); lc = fadd_s(lc, p0[2 * j]); ld = fadd_s(ld, p0[2 * j + 1]); }
    }
    if (grp == 0) { __builtin_amdgcn_s_setprio(0); BARX(); }
    STAMP(18);

    {
        float inv = __builtin_amdgcn_rcpf(swapsum((la + lb) + (lc + ld)));
        asm volatile("s_nop 1" : "+v"(inv));
        MPAD4(acc);
#pragma unroll
        for (int d = 0; d < 4; ++d) {
#pragma unroll
            for (int r = 0; r < 16; ++r) acc[d][r] = fmul_s(acc[d][r], inv);
            qf[2 * d] = pack8(acc[d], 0); qf[2 * d + 1] = pack8(acc[d], 1);
            __builtin_amdgcn_raw_buffer_store_b128(__builtin_bit_cast(u32x4, qf[2 * d]), sof, vo16, (2 * jj + grp) * 32768 + qb * 8192 + d * 2048, 16);
            __builtin_amdgcn_raw_buffer_store_b128(__builtin_bit_cast(u32x4, qf[2 * d + 1]), sof, vo16, (2 * jj + grp) * 32768 + qb * 8192 + d * 2048 + 1024, 16);
            SB();
        }
    }
    const rsrc_t swo = mkrsrc(wo + (size_t)(8 * xq * 128) * 1280 + 320 * jj - BIAS, 1024 * 1280 - 320 * jj + BIAS);
    const unsigned vwA = BIAS + (16 * lw + dr) * 1280 + dsub * 64 + 16 * dsl, vwB = BIAS + (16 * lw + dr) * 1280 + 16 * dsl + (dsub ? 8 * 1280 : 256) - 2048;
    auto issueW = [&](int hl, int half, int buf) {
        const unsigned so = (unsigned)(hl * 128 + 64 * half) * 1280u, ld = lds0 + buf * WT + 5 * lw * 1024;
        dma4<NT_WO>(ld, vwA, vwA - 896, vwB, vwA + 10240 + 64 - 3072, swo, so);
        dma1<NT_WO>(ld + 4096, vwA, swo, so + 10240 + 192);
    };
    const lptr x1 = smem + (lw < 2 ? lw * 8192 : 7 * WT + (lw - 2) * 8192) + vo16;
    if (grp) {
#pragma unroll
        for (int f = 0; f < 8; ++f) *(LAS bf16x8*)(x1 + 1024 * f) = qf[f];
        __builtin_amdgcn_s_setprio(0);
        BAR();
        BARX();
        WAITV(0);
        if (lane == 0) __hip_atomic_store(flagp(bid, 1, qb), tag, __ATOMIC_RELAXED, __HIP_MEMORY_SCOPE_AGENT);
        STAMP(19);
        issueW(hl1(3) + 1, 0, 0); issueW(hl1(3) + 1, 1, 7);
        wait_flags(flagp(partner(1 + (lane % 3)), 1, qb), tag);
        WAITV(0);
        STAMP(21);
        BARX();
    } else {
        bf16x8 cur[4], nxt[4];
#pragma unroll
        for (int k = 0; k < 4; ++k) { cur[k] = qf[k]; nxt[k] = qf[4 + k]; }
        auto issueO = [&](bf16x8* f, int blk) { ld4_sc1(f, vo16, sof, hl1(blk >> 1) * 32768 + qb * 8192 + 4096 * (blk & 1)); };
        issueW(hl1(0), 0, 5);
        BARX();
        issueW(hl1(0), 1, 6); issueW(hl1(1), 0, 7);
        issueV(10, 2 * SLOT); issueK(11, 4 * SLOT);
        BARX();
        issueV(11, 4 * SLOT); issueK(12, 0);
        WAITV(8);
        if (lane == 0) __hip_atomic_store(flagp(bid, 0, qb), tag, __ATOMIC_RELAXED, __HIP_MEMORY_SCOPE_AGENT);
        BARX();
        oblock<true>(acc, cur, smem + 5 * WT + trb);
        issueV(12, 0); issueK(13, 2 * SLOT);
        WAITV(8);
        BARX();
        issueO(cur, 2);
        issueW(hl1(1), 1, 5);
        oblock(acc, nxt, smem + 6 * WT + trb);
        issueO(nxt, 3);
        issueV(13, 2 * SLOT); issueK(14, 4 * SLOT);
        WAITV(17);
        PIN4(cur);
        oblock(acc, cur, smem + 7 * WT + trb);
        issueO(cur, 4);
        WAITV(12);
        BARX();
        unsigned wdummy = 0;
        {
            const int r = 64 + 40 * b + (lane < 40 ? lane : 0);
            const char* p = wo + (size_t)((8 * xq + hl1(1 + (r >> 7)) + 1) * 128 + (r & 127)) * 1280 + 320 * jj + (lw == 3 ? 316 : 128 * lw);
            asm volatile("global_load_dword %0, %1, off" : "+v"(wdummy) : "v"(p) : "memory");
        }
        issueW(hl1(2), 0, 6); issueW(hl1(2), 1, 7);
        PIN4(nxt);
        oblock(acc, nxt, smem + 5 * WT + trb);
        issueO(nxt, 5);
        issueV(14, 4 * SLOT); issueK(15, 0);
        WAITV(17);
        PIN(wdummy);
        BARX();
        issueW(hl1(3), 0, 5);
        PIN4(cur);
        oblock(acc, cur, smem + 6 * WT + trb);
        issueO(cur, 6);
        issueV(15, 0);
        WAITV(13);
        BARX();
        issueW(hl1(3), 1, 6); issueW(hl1(0) + 1, 0, 2); issueW(hl1(0) + 1, 1, 3);
        PIN4(nxt);
        oblock(acc, nxt, smem + 7 * WT + trb);
        issueO(nxt, 7);
        WAITV(4);
        BARX();
        issueW(hl1(1) + 1, 0, 4);
        PIN4(cur);
        oblock(acc, cur, smem + 5 * WT + trb);
        WAITV(5);
        PIN4(nxt);
        oblock(acc, nxt, smem + 6 * WT + trb);
        WAITV(0);
        BARX();
        bf16x8 of2[24];
#pragma unroll
        for (int f = 0; f < 8; ++f) of2[f] = lds128(x1 + 1024 * f);
        BAR();
        issueW(hl1(1) + 1, 1, 5); issueW(hl1(2) + 1, 0, 6); issueW(hl1(2) + 1, 1, 1);
        oblock(acc, of2, smem + 2 * WT + trb);
        oblock(acc, of2 + 4, smem + 3 * WT + trb);
        WAITV(0);
        STAMP(20);
        BARX();
        const unsigned ob = qb * 8192 + 32768;
        auto ldo = [&](int k0) {
#pragma unroll
            for (int k = k0; k < k0 + 4; ++k)
                of2[k] = __builtin_bit_cast(bf16x8, __builtin_amdgcn_raw_buffer_load_b128(sof, vo16, (unsigned)hl1(1 + (k >> 3)) * 32768 + ob + (k & 7) * 1024, 16));
        };
        ldo(0); ldo(4); SB();
        oblock(acc, of2, smem + 4 * WT + trb);       ldo(8);  ldo(12); SB();
        oblock(acc, of2 + 4, smem + 5 * WT + trb);   ldo(16); SB();
        oblock(acc, of2 + 8, smem + 6 * WT + trb);   ldo(20); SB();
        oblock(acc, of2 + 12, smem + 1 * WT + trb);
        oblock(acc, of2 + 16, smem + 0 * WT + trb);
        oblock(acc, of2 + 20, smem + 7 * WT + trb);
        STAMP(22);
        const rsrc_t spb = mkrsrc(ws + WS_PB, 256 * PB_WG);
        f32x4 mine[5];
#define UNIT(u, e) acc[(u) >> 2][4 * ((u) & 3) + (e)]
#define UPK(u) cvtpk(UNIT(u, 0), UNIT(u, 1)), cvtpk(UNIT(u, 2), UNIT(u, 3))
#pragma unroll
        for (int g = 0; g < 4; ++g) {
            if (g == xq) {
#pragma unroll
                for (int n = 0; n < 5; ++n) mine[n] = (f32x4){UNIT(5 * g + n, 0), UNIT(5 * g + n, 1), UNIT(5 * g + n, 2), UNIT(5 * g + n, 3)};
            } else {
                const unsigned so = (unsigned)((bid & ~3) | g) * PB_WG + (xq * 4 + qb) * 5120;
                const u32x4 w01 = {UPK(5 * g), UPK(5 * g + 1)}, w23 = {UPK(5 * g + 2), UPK(5 * g + 3)};
                const u32x2 w4 = {UPK(5 * g + 4)};
                __builtin_amdgcn_raw_buffer_store_b128(w01, spb, vo16, so, 16);
                __builtin_amdgcn_raw_buffer_store_b128(w23, spb, vo16, so + 1024, 16);
                __builtin_amdgcn_raw_buffer_store_b64(w4, spb, lane * 8, so + 2048, 16);
            }
        }
        WAITV(0);
        if (lane == 0) __hip_atomic_store(flagp(bid, 2, qb), tag, __ATOMIC_RELAXED, __HIP_MEMORY_SCOPE_AGENT);
        STAMP(23);
        wait_flags(flagp((bid & ~3) | ((xq + 1 + (lane % 3)) & 3), 2, qb), tag);
        STAMP(24);
        {
            const rsrc_t sout = mkrsrc(out + ((size_t)b * SQ * (2 * DM) + br * DM + 160 * jj + 40 * xq) * 2, SQ * 2 * DM * 2 - (br * DM + 160 * jj + 40 * xq) * 2);
            const unsigned vop = rowq * (4 * DM) + 8 * h;
            u32x4 a01[3], a23[3]; u32x2 a4[3];
#pragma unroll
            for (int s = 0; s < 3; ++s) {
                const unsigned so = (unsigned)bid * PB_WG + (((xq + 1 + s) & 3) * 4 + qb) * 5120;
                a01[s] = __builtin_amdgcn_raw_buffer_load_b128(spb, vo16, so, 16);
                a23[s] = __builtin_amdgcn_raw_buffer_load_b128(spb, vo16, so + 1024, 16);
                a4[s] = __builtin_amdgcn_raw_buffer_load_b64(spb, lane * 8, so + 2048, 16);
            }
            auto up = [](unsigned lo, unsigned hi) { return (f32x4){__uint_as_float(lo << 16), __uint_as_float(lo & 0xffff0000u), __uint_as_float(hi << 16), __uint_as_float(hi & 0xffff0000u)}; };
#pragma unroll
            for (int s = 0; s < 3; ++s) {
                mine[0] += up(a01[s][0], a01[s][1]); mine[1] += up(a01[s][2], a01[s][3]);
                mine[2] += up(a23[s][0], a23[s][1]); mine[3] += up(a23[s][2], a23[s][3]); mine[4] += up(a4[s][0], a4[s][1]);
            }
#pragma unroll
            for (int n = 0; n < 5; ++n) __builtin_amdgcn_raw_buffer_store_b64((u32x2){cvtpk(mine[n][0], mine[n][1]), cvtpk(mine[n][2], mine[n][3])}, sout, vop, 16 * n, OUT_AUX);
        }
    }
    {
        if (xq == 0 && jj == 0 && wid == 0 && lane == 0)
            __hip_atomic_store((GAS u64*)(ws + WS_GEN + (br * NB + b) * 256), tag, __ATOMIC_RELAXED, __HIP_MEMORY_SCOPE_AGENT);
        STAMP(25);
    }
}

extern "C" void kernel_launch(void* const* d_in, const int* in_sizes, int n_in, void* d_out, int out_size,
                              void* d_ws, size_t ws_size, hipStream_t stream) {
    (void)hipFuncSetAttribute((const void*)fused_kernel, hipFuncAttributeMaxDynamicSharedMemorySize, LDS_BYTES);
    hipLaunchKernelGGL(fused_kernel, dim3(256), dim3(512), LDS_BYTES, stream,
                       (const char*)d_in[0], (const char*)d_in[1], (const char*)d_in[2], (const char*)d_in[3], (const char*)d_in[4],
                       (const char*)d_in[5], (const char*)d_in[6], (const char*)d_in[7], (const char*)d_in[8], (const char*)d_in[9],
                       (char*)d_ws + WS_SHIFT, (char*)d_out);
}
```

```cpp
#define KVORD 2
#include <hip/hip_runtime.h>
#include <hip/hip_bf16.h>
#include <stdint.h>
#include <type_traits>

typedef short bf16x8 __attribute__((ext_vector_type(8)));
typedef short s16x4 __attribute__((ext_vector_type(4)));
typedef float f32x16 __attribute__((ext_vector_type(16)));
typedef float f32x4 __attribute__((ext_vector_type(4)));
typedef float f32x2 __attribute__((ext_vector_type(2)));
typedef __bf16 bf16x2_t __attribute__((ext_vector_type(2)));
typedef unsigned u32x4 __attribute__((ext_vector_type(4)));
typedef unsigned u32x2 __attribute__((ext_vector_type(2)));
typedef unsigned long long u64;
#define LAS __attribute__((address_space(3)))
#define GAS __attribute__((address_space(1)))
#define DI __device__ __forceinline__
typedef LAS char* lptr;
typedef __amdgpu_buffer_rsrc_t rsrc_t;

constexpr int NB = 8, SQ = 128, DM = 640, NH = 32, SK = 512, DH = 128;
constexpr float CEXP = 0.08838834764831845f * 1.4426950408889634f;
constexpr int SLOT = 16384;
constexpr int LDS_T = 6 * SLOT;
constexpr int WT = 20480;
constexpr int LDS_BYTES = 10 * SLOT;
constexpr size_t WS_GEN = 0, WS_FLAG = 8192, WS_OF = 1 << 20;
constexpr size_t OF_BYTES = (size_t)2 * NB * NH * 32768;
constexpr size_t WS_PB = WS_OF + OF_BYTES;
constexpr unsigned PB_WG = 4 * 8 * 3 * 1024;
#define NT_W 0
#define NT_X 0
#define NT_WO 0
#define PPRIO 2
#define PBF16 1
#define WS_SHIFT 0
#define POL 15
#define PRETOUCH 1
#define CL 180
constexpr unsigned CODE_LINES = CL;
#define WOWARM 1
#define ROT 1
#define OUT_AUX 0
#define PROBE_BID 77
#define QLATE 0
constexpr unsigned SPIN_CAP = 1u << 16;

#define MFMA32(a, b, c) __builtin_amdgcn_mfma_f32_32x32x16_bf16((a), (b), (c), 0, 0, 0)
#define WAITV(n) asm volatile("s_waitcnt vmcnt(" #n ")" ::: "memory")
#define SB() __builtin_amdgcn_sched_barrier(0)
#define BARX() do { SB(); asm volatile("s_barrier" ::: "memory"); SB(); } while (0)
#define BAR() do { SB(); asm volatile("s_waitcnt lgkmcnt(0)\n\ts_barrier" ::: "memory"); SB(); } while (0)

#define STAMP(idx)

DI unsigned cvtpk(float lo, float hi) {
    f32x2 v = {lo, hi};
    bf16x2_t b = __builtin_convertvector(v, bf16x2_t);
    return __builtin_bit_cast(unsigned, b);
}
DI bf16x8 pack8(const f32x16& x, int s) {
    u32x4 w = {cvtpk(x[8 * s + 0], x[8 * s + 1]), cvtpk(x[8 * s + 2], x[8 * s + 3]),
               cvtpk(x[8 * s + 4], x[8 * s + 5]), cvtpk(x[8 * s + 6], x[8 * s + 7])};
    return __builtin_bit_cast(bf16x8, w);
}
template <int NT> DI void dma4(unsigned lds, unsigned v0, unsigned v1, unsigned v2, unsigned v3, rsrc_t srd, unsigned soff) {
    if (NT) asm volatile("s_mov_b32 m0, %0\n\ts_nop 3\n\tbuffer_load_dwordx4 %1, %5, %6 offen nt lds\n\tbuffer_load_dwordx4 %2, %5, %6 offen offset:1024 nt lds\n\t"
                         "buffer_load_dwordx4 %3, %5, %6 offen offset:2048 nt lds\n\tbuffer_load_dwordx4 %4, %5, %6 offen offset:3072 nt lds"
                         :: "s"(lds), "v"(v0), "v"(v1), "v"(v2), "v"(v3), "s"(srd), "s"(soff) : "memory");
    else asm volatile("s_mov_b32 m0, %0\n\ts_nop 3\n\tbuffer_load_dwordx4 %1, %5, %6 offen lds\n\tbuffer_load_dwordx4 %2, %5, %6 offen offset:1024 lds\n\t"
                      "buffer_load_dwordx4 %3, %5, %6 offen offset:2048 lds\n\tbuffer_load_dwordx4 %4, %5, %6 offen offset:3072 lds"
                      :: "s"(lds), "v"(v0), "v"(v1), "v"(v2), "v"(v3), "s"(srd), "s"(soff) : "memory");
}
template <int NT> DI void dma1(unsigned lds, unsigned voff, rsrc_t srd, unsigned soff) {
    if (NT) asm volatile("s_mov_b32 m0, %0\n\ts_nop 3\n\tbuffer_load_dwordx4 %1, %2, %3 offen nt lds" :: "s"(lds), "v"(voff), "s"(srd), "s"(soff) : "memory");
    else asm volatile("s_mov_b32 m0, %0\n\ts_nop 3\n\tbuffer_load_dwordx4 %1, %2, %3 offen lds" :: "s"(lds), "v"(voff), "s"(srd), "s"(soff) : "memory");
}
DI void ld4_sc1(bf16x8* d, unsigned voff, rsrc_t srd, unsigned soff) {
    asm volatile("s_nop 4\n\tbuffer_load_dwordx4 %0, %4, %5, %6 offen sc1\n\tbuffer_load_dwordx4 %1, %4, %5, %6 offen offset:1024 sc1\n\t"
                 "buffer_load_dwordx4 %2, %4, %5, %6 offen offset:2048 sc1\n\tbuffer_load_dwordx4 %3, %4, %5, %6 offen offset:3072 sc1"
                 : "+v"(d[0]), "+v"(d[1]), "+v"(d[2]), "+v"(d[3]) : "v"(voff), "s"(srd), "s"(soff) : "memory");
}
constexpr unsigned BIAS = 4096;
#define WAITV_F(n, f) asm volatile("s_waitcnt vmcnt(" #n ")" : "+v"((f)[0]), "+v"((f)[1]), "+v"((f)[2]), "+v"((f)[3]) :: "memory")
DI bf16x8 lds128(lptr p) { return *(const LAS bf16x8*)p; }
DI s16x4 ldstr(lptr p) {
    typedef short v4i16_t __attribute__((ext_vector_type(4)));
    return __builtin_amdgcn_ds_read_tr16_b64_v4i16((LAS v4i16_t*)p);
}
DI bf16x8 cat(s16x4 a, s16x4 b) { return __builtin_shufflevector(a, b, 0, 1, 2, 3, 4, 5, 6, 7); }
DI float swapmax(float v) {
    auto r = __builtin_amdgcn_permlane32_swap(__float_as_uint(v), __float_as_uint(v), false, false);
    return fmaxf(__uint_as_float(r[0]), __uint_as_float(r[1]));
}
DI float swapsum(float v) {
    auto r = __builtin_amdgcn_permlane32_swap(__float_as_uint(v), __float_as_uint(v), false, false);
    return __uint_as_float(r[0]) + __uint_as_float(r[1]);
}
DI rsrc_t mkrsrc(const void* p, unsigned bytes) {
    return __builtin_amdgcn_make_buffer_rsrc((void*)p, (short)0, (int)bytes, 0x00020000);
}
DI void zero16(f32x16& v) {
#pragma unroll
    for (int r = 0; r < 16; ++r) v[r] = 0.f;
}
DI void wait_flags(const GAS u64* p, u64 tag) {
#pragma unroll 1
    for (unsigned it = 0; it < SPIN_CAP; ++it) {
        const u64 v = __hip_atomic_load(p, __ATOMIC_RELAXED, __HIP_MEMORY_SCOPE_AGENT);
        if (__all(v == tag)) break;
        __builtin_amdgcn_s_sleep(1);
    }
    asm volatile("" ::: "memory");
}

DI float max3f(float a, float b, float c) { float r; asm("v_max3_f32 %0, %1, %2, %3" : "=v"(r) : "v"(a), "v"(b), "v"(c)); return r; }
DI float fadd_s(float a, float b) { float r; asm("v_add_f32_e32 %0, %1, %2" : "=v"(r) : "v"(a), "v"(b)); return r; }
DI float fmul_s(float a, float b) { float r; asm("v_mul_f32_e32 %0, %1, %2" : "=v"(r) : "v"(a), "v"(b)); return r; }
DI float fsub_s(float a, float b) { float r; asm("v_sub_f32_e32 %0, %1, %2" : "=v"(r) : "v"(a), "v"(b)); return r; }
DI float exp_s(float a) { float r; asm("v_exp_f32_e32 %0, %1" : "=v"(r) : "v"(a)); return r; }
#define MPAD4(a) asm volatile("s_nop 7\n\ts_nop 7" : "+v"((a)[0]), "+v"((a)[1]), "+v"((a)[2]), "+v"((a)[3]))
#define PIN(x) asm volatile("" : "+v"(x))
#define PIN4(f) asm volatile("" : "+v"((f)[0]), "+v"((f)[1]), "+v"((f)[2]), "+v"((f)[3]) :: "memory")
DI lptr opaque(lptr p) { unsigned a = (unsigned)(uintptr_t)p; asm volatile("" : "+v"(a)); return (lptr)(uintptr_t)a; }
#define LA_ 6
constexpr int LA = LA_;
#define SM_THR 8.0f

struct QBase { lptr x0, x1, w; };
DI void qload(int i, bf16x8* wf, bf16x8* xf, const QBase& q) {
    const int s = i >> 2, d = i & 3;
    if (d == 0) xf[s] = lds128(((s & 1) ? q.x1 : q.x0) + 512 * (s >> 1));
    wf[i & 7] = cat(ldstr(q.w + 4096 * s + 512 * d), ldstr(q.w + 4096 * s + 512 * d + 256));
}
DI void qpre(bf16x8* wf, bf16x8* xf, const QBase& q) {
#pragma unroll
    for (int i = 0; i < LA; ++i) qload(i, wf, xf, q);
}
template <bool NEXT> DI void qsteps(f32x16* acc, bf16x8* wf, bf16x8* xf, const QBase& q, const QBase& qn) {
#pragma unroll
    for (int i = 0; i < 16; ++i) {
        SB();
        acc[i & 3] = MFMA32(wf[i & 7], xf[i >> 2], acc[i & 3]);
        PIN(acc[i & 3]);
        SB();
        if (NEXT && i + LA == 16) BARX();
        if (i + LA < 16) qload(i + LA, wf, xf, q); else if (NEXT) qload(i + LA - 16, wf, xf, qn);
    }
    SB();
}
template <bool FIRST = false> DI void oblock(f32x16* acc, const bf16x8* of, lptr wp_) {
    const lptr wp = opaque(wp_);
    bf16x8 wf[8];
    auto load = [&](int i) { wf[i & 7] = cat(ldstr(wp + 5120 * (i / 5) + 512 * (i % 5)), ldstr(wp + 5120 * (i / 5) + 2560 + 512 * (i % 5))); };
#pragma unroll
    for (int i = 0; i < LA; ++i) load(i);
#pragma unroll
    for (int i = 0; i < 20; ++i) {
        SB();
        if (FIRST && i < 5) { f32x16 z; zero16(z); acc[i] = MFMA32(wf[i & 7], of[0], z); }
        else acc[i % 5] = MFMA32(wf[i & 7], of[i / 5], acc[i % 5]);
        PIN(acc[i % 5]);
        SB();
        if (i + LA < 20) load(i + LA);
    }
    SB();
}
DI void aload(int i, bf16x8* fr, lptr vp, lptr kp0, lptr kp1) {
    if (i < 16) fr[i & 7] = cat(ldstr(vp + 4096 * (i >> 2) + 512 * (i & 3)), ldstr(vp + 4096 * (i >> 2) + 2048 + 512 * (i & 3)));
    else { const int s = i & 7; fr[i & 7] = lds128(((s & 1) ? kp1 : kp0) + 512 * (s >> 1) + 8192 * ((i - 16) >> 3)); }
}

__global__ __launch_bounds__(512, 2) void fused_kernel(
    const char* xA, const char* kA, const char* vA, const char* wqA, const char* woA,
    const char* xB, const char* kB, const char* vB, const char* wqB, const char* woB,
    char* ws, char* out)
{
    extern __shared__ __attribute__((aligned(16))) char smem_[];
    const lptr smem = (lptr)smem_;
    const unsigned lds0 = (unsigned)(uintptr_t)smem;
    const int tid = threadIdx.x, lane = tid & 63;
    const int wid = __builtin_amdgcn_readfirstlane(tid >> 6);
    const int bid = blockIdx.x;
    const int xg = bid & 7, ii = bid >> 3;
    const int br = xg >> 2, xq = xg & 3, b = ii >> 2, jj = ii & 3;
    const int h1 = 8 * xq + 2 * jj;
    const char* x = br ? xB : xA;
    const char* kc = br ? kB : kA;
    const char* vc = br ? vB : vA;
    const char* wq = br ? wqB : wqA;
    const char* wo = br ? woB : woA;
    const int qb = wid & 3, grp = wid >> 2, lw = wid & 3;
    const int l31 = lane & 31, h = lane >> 5;
    STAMP(0);
    const char* codebase;
    asm volatile("s_getpc_b64 %0" : "=s"(codebase));

    u64 gen;
    {
        const char* gp = ws + WS_GEN + (br * NB + b) * 256;
        asm volatile("global_load_dwordx2 %0, %1, off sc1" : "=v"(gen) : "v"(gp) : "memory");
    }

    const rsrc_t sk = mkrsrc(kc + (size_t)(b * NH + h1) * SK * DH * 2 - BIAS, 2 * SK * DH * 2 + BIAS);
    const rsrc_t sv = mkrsrc(vc + (size_t)(b * NH + h1) * SK * DH * 2 - BIAS, 2 * SK * DH * 2 + BIAS);
    const rsrc_t sof = mkrsrc(ws + WS_OF + (size_t)((br * NB + b) * NH + 8 * xq) * 32768, 8 * 32768);

    const unsigned dsub = lane >> 5, dr = (lane >> 2) & 7, dsl = lane & 3;
    const unsigned xr0 = 16u * (dsl ^ ((dr >> 2) & 3)), xr1 = 16u * (dsl ^ ((2 + (dr >> 2)) & 3));
    const unsigned vk0 = BIAS + 4096 * lw + dr * 256 + dsub * 64 + xr0, vk2 = BIAS + 4096 * lw + dr * 256 + dsub * 64 + xr1;
    const unsigned vv0 = BIAS + 4096 * lw + dr * 256 + dsub * 64 + 16 * dsl;
    const unsigned ldw = lds0 + 4096 * lw;

    auto issueK = [&](int t, unsigned pair) {
        if ((POL & 1) == ((POL >> 2) & 1) ? (POL & 1) : ((POL >> (2 * br)) & 1)) dma4<1>(ldw + pair, vk0, vk0 - 896, vk2, vk2 - 896, sk, (unsigned)t * 16384u);
        else dma4<0>(ldw + pair, vk0, vk0 - 896, vk2, vk2 - 896, sk, (unsigned)t * 16384u); };
    auto issueV = [&](int t, unsigned pair) {
        if (((POL >> 1) & 1) == ((POL >> 3) & 1) ? ((POL >> 1) & 1) : ((POL >> (2 * br + 1)) & 1)) dma4<1>(ldw + pair + SLOT, vv0, vv0 - 896, vv0, vv0 - 896, sv, (unsigned)t * 16384u);
        else dma4<0>(ldw + pair + SLOT, vv0, vv0 - 896, vv0, vv0 - 896, sv, (unsigned)t * 16384u); };
    auto nextpair = [](unsigned p) { return (p == 4u * SLOT) ? 0u : p + 2u * SLOT; };

    const int G1 = (lane >> 4) & 1, tq = (lane & 15) >> 2, tp = lane & 3;
    const int sig = ((tp & 1) << 1) | (tp >> 1);
    const unsigned wqb = SLOT + 2048 * h + 64 * tq + 32 * G1 + 8 * sig;
    const unsigned rowq = 32 * qb + l31;
    const unsigned xsw = (rowq >> 2) & 3;
    const unsigned xb0 = 1024 * (rowq >> 3) + 64 * (rowq & 7) + 16 * ((0 + h) ^ xsw);
    const unsigned xb1 = 1024 * (rowq >> 3) + 64 * (rowq & 7) + 16 * ((2 + h) ^ xsw);
    const unsigned ksw = (l31 >> 2) & 3;
    const unsigned kb0 = 2048 * (l31 >> 3) + 64 * (l31 & 7) + 16 * ((0 + h) ^ ksw);
    const unsigned kb1 = 2048 * (l31 >> 3) + 64 * (l31 & 7) + 16 * ((2 + h) ^ ksw);
    const unsigned trb = 64 * (4 * h + tq) + 32 * G1 + 8 * tp;
    const unsigned vo16 = lane * 16;

    auto flagp = [&](int bb, int stage, int w) { return (GAS u64*)(ws + WS_FLAG + (size_t)((bb * 3 + stage) * 8 + w) * 64); };
    auto partner = [&](int dj) { return (bid & ~24) | (((jj + dj) & 3) << 3); };
    auto hl1 = [&](int dj) { return 2 * ((jj + dj) & 3); };

    f32x16 acc[5];
    bf16x8 qf[8], pf[4], fr[8];
    f32x16 s0, s1;
#pragma unroll
    for (int d = 0; d < 4; ++d) zero16(acc[d]);

    auto nextstage = [](unsigned st) { return st == 8u * SLOT ? 4u * SLOT : (st == 4u * SLOT ? 6u * SLOT : (st == 6u * SLOT ? 2u * SLOT : 8u * SLOT)); };
    auto qbase = [&](unsigned st) { return QBase{opaque(smem + st + xb0), opaque(smem + st + xb1), opaque(smem + st + wqb)}; };
    if (grp == 0) {
        unsigned st = 8 * SLOT;
        unsigned dummy = 0;
        {
            const unsigned line = lw * 64 + lane;
            const char* p = codebase + (line < CODE_LINES ? line : 0) * 128;
            asm volatile("global_load_dword %0, %1, off" : "+v"(dummy) : "v"(p) : "memory");
        }
        BARX();
        QBase qc = qbase(st);
        qpre(fr, pf, qc);
#pragma unroll 1
        for (int i = 0; i < 10; ++i) {
            st = nextstage(st);
            const QBase qn = qbase(st);
            qsteps<true>(acc, fr, pf, qc, qn);
            qc = qn;
        }
        WAITV(0);
        PIN(dummy);
    } else {
        const rsrc_t sx = mkrsrc(x + (size_t)b * SQ * DM * 2, SQ * DM * 2);
        const rsrc_t sw = mkrsrc(wq + (size_t)h1 * 256 - BIAS, DM * 8192 - h1 * 256 + BIAS);
        const unsigned vx0 = (32 * lw + dr) * 1280 + dsub * 64 + xr0, vx1 = (32 * lw + 8 + dr) * 1280 + dsub * 64 + xr1 - 1024;
        const unsigned vw0 = BIAS + (16 * lw + dr) * 8192 + dsub * 64 + 16 * dsl;
        const int rot = ROT ? b + 3 * jj : 0;
        constexpr unsigned T0 = 6 * SLOT, T1 = 8 * SLOT, P1 = 2 * SLOT, P2 = 4 * SLOT;
        int ci = rot >= 10 ? rot - 10 : rot;
        auto issueQn = [&](int hd, unsigned st) {
            dma4<NT_W>(ldw + st + SLOT, vw0, vw0 - 896, vw0 + 65536 - 2048, vw0 + 65536 - 2944, sw, (unsigned)ci * 524288u + 256 * hd);
            dma4<NT_X>(ldw + st, vx0, vx1, vx0 + 20480 - 2048, vx1 + 20480 - 2048, sx, 128 * ci);
            ci = ci == 9 ? 0 : ci + 1;
        };
        issueQn(0, T1);
        WAITV(0); BARX();
        issueQn(0, P2); issueQn(0, T0);
        WAITV(8);  BARX(); issueQn(0, P1);
        WAITV(8);  BARX(); issueQn(0, T1);
        WAITV(8);  BARX(); issueQn(0, P2);
        WAITV(8);  BARX(); issueQn(0, T0); issueK(0, 0); issueV(0, 0);
        WAITV(16); BARX(); issueQn(0, P1);
        WAITV(16); BARX(); issueQn(0, T1);
        WAITV(8);  BARX(); issueQn(0, P2);
        WAITV(8);  BARX(); issueQn(1, T0);
        WAITV(8);  BARX(); issueK(1, P1); issueV(1, P1);
        BARX(); issueQn(1, T1);
        const QBase q0 = qbase(T0), q1 = qbase(T1);
        WAITV(16); BARX();
        qpre(fr, pf, q0);
        issueK(2, P2);
        qsteps<false>(acc, fr, pf, q0, q0);
        WAITV(4);
        unsigned pv = P2;
#pragma unroll 1
        for (int c = 1; c < 9; c += 2) {
            unsigned pk = nextpair(pv);
            BARX();
            qpre(fr, pf, q1);
            issueQn(1, T0);
            if (KVORD >= 1) issueV(c + 1, pv);
            if (KVORD >= 2) issueK(c + 2, pk);
            qsteps<false>(acc, fr, pf, q1, q1);
            if (KVORD < 1) issueV(c + 1, pv);
            if (KVORD < 2) issueK(c + 2, pk);
            pv = nextpair(pk);
            WAITV(8);
            BARX();
            qpre(fr, pf, q0);
            issueQn(1, T1);
            if (KVORD >= 1) issueV(c + 2, pk);
            if (KVORD >= 2) issueK(c + 3, pv);
            qsteps<false>(acc, fr, pf, q0, q0);
            if (KVORD < 1) issueV(c + 2, pk);
            if (KVORD < 2) issueK(c + 3, pv);
            WAITV(8);
        }
        BARX();
        qpre(fr, pf, q1);
        qsteps<false>(acc, fr, pf, q1, q1);
    }
    STAMP(1);
    asm volatile("" : "+v"(gen));
    const u64 tag = gen + 1;

    __builtin_amdgcn_s_setprio(PPRIO);
    MPAD4(acc);
    {
        float cexp = CEXP; PIN(cexp);
#pragma unroll
        for (int d = 0; d < 4; ++d) {
#pragma unroll
            for (int r = 0; r < 16; ++r) acc[d][r] = fmul_s(acc[d][r], cexp);
            qf[2 * d] = pack8(acc[d], 0); qf[2 * d + 1] = pack8(acc[d], 1); zero16(acc[d]);
        }
    }
    if (grp == 0) BARX();
    float la = 0.f, lb = 0.f, lc = 0.f, ld = 0.f;
    {
        f32x16 negm, p0; zero16(negm); zero16(s1); zero16(p0);
        unsigned pk = grp ? 4 * SLOT : 0;
        lptr kp0 = opaque(smem + pk + kb0), kp1 = opaque(smem + pk + kb1);
#pragma unroll
        for (int i = 16; i < 16 + LA; ++i) aload(i, fr, kp0, kp0, kp1);
        const GAS u64* fp = flagp(partner(1 + (lane % 3)), 0, qb);
        u64 fv = 0;
        const int tw = grp ? 3 : -1;
        float thr = -__builtin_inff(), dmin = -__builtin_inff();
        u32x4 pw[4];
#pragma unroll 1
        for (int t = 0; t < 8; ++t) {
            if (__builtin_expect(t == tw, 0)) asm volatile("global_load_dwordx2 %0, %1, off sc1" : "+v"(fv) : "v"(fp) : "memory");
            float c0, c1;
#pragma unroll
            for (int j = 0; j < 16; ++j) {
                const int i = 16 + j;
                SB();
                if (j < 8) s0 = MFMA32(fr[i & 7], qf[j], j == 0 ? negm : s0); else s1 = MFMA32(fr[i & 7], qf[j - 8], j == 8 ? negm : s1);
                SB();
                if (i + LA < 32) aload(i + LA, fr, kp0, kp0, kp1);
                if (j < 8)
                    asm volatile("v_add_f32_e32 %0, %0, %4\n\tv_add_f32_e32 %1, %1, %5\n\tv_add_f32_e32 %2, %2, %6\n\tv_add_f32_e32 %3, %3, %7"
                                 : "+v"(la), "+v"(lb), "+v"(lc), "+v"(ld) : "v"(s1[2 * j]), "v"(s1[2 * j + 1]), "v"(p0[2 * j]), "v"(p0[2 * j + 1]));
                if (j >= 10) {
                    const int e = 2 * (j - 10);
                    float e0, e1; unsigned pk_;
                    if (j == 10)
                        asm volatile("v_exp_f32_e32 %0, %5\n\tv_exp_f32_e32 %1, %6\n\tv_max3_f32 %3, %5, %6, %7\n\tv_max3_f32 %4, %8, %9, %10\n\tv_cvt_pk_bf16_f32 %2, %0, %1"
                                     : "=&v"(e0), "=&v"(e1), "=&v"(pk_), "=&v"(c0), "=&v"(c1) : "v"(s0[0]), "v"(s0[1]), "v"(s0[12]), "v"(s0[13]), "v"(s0[14]), "v"(s0[15]));
                    else if (j & 1)
                        asm volatile("v_exp_f32_e32 %0, %4\n\tv_exp_f32_e32 %1, %5\n\tv_max3_f32 %3, %3, %4, %5\n\tv_cvt_pk_bf16_f32 %2, %0, %1"
                                     : "=&v"(e0), "=&v"(e1), "=&v"(pk_), "+v"(c1) : "v"(s0[e]), "v"(s0[e + 1]));
                    else
                        asm volatile("v_exp_f32_e32 %0, %4\n\tv_exp_f32_e32 %1, %5\n\tv_max3_f32 %3, %3, %4, %5\n\tv_cvt_pk_bf16_f32 %2, %0, %1"
                                     : "=&v"(e0), "=&v"(e1), "=&v"(pk_), "+v"(c0) : "v"(s0[e]), "v"(s0[e + 1]));
                    p0[e] = e0; p0[e + 1] = e1;
                    pw[e >> 3][(e >> 1) & 3] = pk_;
                }
            }
            WAITV(0);
            if (__builtin_expect(t == tw, 0)) { PIN(fv); if (!__all(fv == tag)) wait_flags(fp, tag); }
            STAMP(2 + 2 * t);
            BARX();
            const lptr vp = opaque(smem + pk + SLOT + trb);
            pk = nextpair(pk);
            kp0 = opaque(smem + pk + kb0); kp1 = opaque(smem + pk + kb1);
#pragma unroll
            for (int i = 0; i < LA; ++i) aload(i, fr, vp, vp, vp);
            PIN(s1);
            SB();
            {
                float c2, c3;
                asm volatile("v_max3_f32 %2, %4, %5, %6\n\tv_max3_f32 %3, %7, %8, %9\n\tv_max3_f32 %0, %0, %10, %11\n\tv_max3_f32 %1, %1, %12, %13\n\t"
                             "v_max3_f32 %2, %2, %14, %15\n\tv_max3_f32 %3, %3, %16, %17\n\tv_max3_f32 %0, %0, %18, %19\n\tv_max3_f32 %1, %1, %2, %3\n\tv_max_f32_e32 %0, %0, %1"
                             : "+v"(c0), "+v"(c1), "=&v"(c2), "=&v"(c3)
                             : "v"(s1[0]), "v"(s1[1]), "v"(s1[2]), "v"(s1[3]), "v"(s1[4]), "v"(s1[5]), "v"(s1[6]), "v"(s1[7]),
                               "v"(s1[8]), "v"(s1[9]), "v"(s1[10]), "v"(s1[11]), "v"(s1[12]), "v"(s1[13]), "v"(s1[14]), "v"(s1[15]));
                auto r_ = __builtin_amdgcn_permlane32_swap(__float_as_uint(c0), __float_as_uint(c0), false, false);
                const float mx = max3f(__uint_as_float(r_[0]), __uint_as_float(r_[1]), __uint_as_float(r_[1]));
                if (__builtin_expect(__any(!(mx <= thr)), 0)) {
                    const float d = fmaxf(mx, dmin);
                    float f = __builtin_amdgcn_exp2f(fminf(-d, 0.f));
                    asm volatile("s_nop 1" : "+v"(f));
#pragma unroll
                    for (int r = 0; r < 16; ++r) { s0[r] = fsub_s(s0[r], d); s1[r] = fsub_s(s1[r], d); negm[r] = fsub_s(negm[r], d); }
                    la = fmul_s(la, f); lb = fmul_s(lb, f); lc = fmul_s(lc, f); ld = fmul_s(ld, f);
#pragma unroll
                    for (int dd = 0; dd < 4; ++dd)
#pragma unroll
                        for (int r = 0; r < 16; ++r) acc[dd][r] = fmul_s(acc[dd][r], f);
#pragma unroll
                    for (int e = 0; e < 12; ++e) p0[e] = __builtin_amdgcn_exp2f(s0[e]);
#pragma unroll
                    for (int e = 0; e < 12; e += 2) pw[e >> 3][(e >> 1) & 3] = cvtpk(p0[e], p0[e + 1]);
                }
                thr = SM_THR; dmin = 0.f;
            }
#pragma unroll
            for (int i = 0; i < 16; ++i) {
                SB();
                acc[i & 3] = MFMA32(fr[i & 7], __builtin_bit_cast(bf16x8, pw[i >> 2]), acc[i & 3]);
                SB();
                constexpr int pairs[16] = {6, 7, 8, 9, 10, 11, -1, 12, 13, 14, 15, -1, -1, -1, -1, -1};
                const int q = pairs[i];
                if (q >= 0) {
                    if (q < 8) { p0[2 * q] = __builtin_amdgcn_exp2f(s0[2 * q]); p0[2 * q + 1] = __builtin_amdgcn_exp2f(s0[2 * q + 1]); PIN(p0); }
                    else { s1[2 * q - 16] = __builtin_amdgcn_exp2f(s1[2 * q - 16]); s1[2 * q - 15] = __builtin_amdgcn_exp2f(s1[2 * q - 15]); PIN(s1); }
                }
                aload(i + LA, fr, vp, kp0, kp1);
                if (q >= 0) {
                    pw[q >> 2][q & 3] = q < 8 ? cvtpk(p0[2 * q], p0[2 * q + 1]) : cvtpk(s1[2 * q - 16], s1[2 * q - 15]);
                    PIN(pw[q >> 2][q & 3]);
                }
            }
            SB();
            STAMP(3 + 2 * t);
        }
#pragma unroll
        for (int j = 0; j < 8; ++j) { la = fadd_s(la, s1[2 * j]); lb = fadd_s(lb, s1[2 * j + 1]); lc = fadd_s(lc, p0[2 * j]); ld = fadd_s(ld, p0[2 * j + 1]); }
    }
    if (grp == 0) { __builtin_amdgcn_s_setprio(0); BARX(); }
    STAMP(18);

    {
        float inv = __builtin_amdgcn_rcpf(swapsum((la + lb) + (lc + ld)));
        asm volatile("s_nop 1" : "+v"(inv));
        MPAD4(acc);
#pragma unroll
        for (int d = 0; d < 4; ++d) {
#pragma unroll
            for (int r = 0; r < 16; ++r) acc[d][r] = fmul_s(acc[d][r], inv);
            qf[2 * d] = pack8(acc[d], 0); qf[2 * d + 1] = pack8(acc[d], 1);
            __builtin_amdgcn_raw_buffer_store_b128(__builtin_bit_cast(u32x4, qf[2 * d]), sof, vo16, (2 * jj + grp) * 32768 + qb * 8192 + d * 2048, 16);
            __builtin_amdgcn_raw_buffer_store_b128(__builtin_bit_cast(u32x4, qf[2 * d + 1]), sof, vo16, (2 * jj + grp) * 32768 + qb * 8192 + d * 2048 + 1024, 16);
            SB();
        }
    }
    const rsrc_t swo = mkrsrc(wo + (size_t)(8 * xq * 128) * 1280 + 320 * jj - BIAS, 1024 * 1280 - 320 * jj + BIAS);
    const unsigned vwA = BIAS + (16 * lw + dr) * 1280 + dsub * 64 + 16 * dsl, vwB = BIAS + (16 * lw + dr) * 1280 + 16 * dsl + (dsub ? 8 * 1280 : 256) - 2048;
    auto issueW = [&](int hl, int half, int buf) {
        const unsigned so = (unsigned)(hl * 128 + 64 * half) * 1280u, ld = lds0 + buf * WT + 5 * lw * 1024;
        dma4<NT_WO>(ld, vwA, vwA - 896, vwB, vwA + 10240 + 64 - 3072, swo, so);
        dma1<NT_WO>(ld + 4096, vwA, swo, so + 10240 + 192);
    };
    const lptr x1 = smem + (lw < 2 ? lw * 8192 : 7 * WT + (lw - 2) * 8192) + vo16;
    if (grp) {
#pragma unroll
        for (int f = 0; f < 8; ++f) *(LAS bf16x8*)(x1 + 1024 * f) = qf[f];
        __builtin_amdgcn_s_setprio(0);
        BAR();
        BARX();
        WAITV(0);
        if (lane == 0) __hip_atomic_store(flagp(bid, 1, qb), tag, __ATOMIC_RELAXED, __HIP_MEMORY_SCOPE_AGENT);
        STAMP(19);
        issueW(hl1(3) + 1, 0, 0); issueW(hl1(3) + 1, 1, 7);
        wait_flags(flagp(partner(1 + (lane % 3)), 1, qb), tag);
        WAITV(0);
        STAMP(21);
        BARX();
    } else {
        bf16x8 cur[4], nxt[4];
#pragma unroll
        for (int k = 0; k < 4; ++k) { cur[k] = qf[k]; nxt[k] = qf[4 + k]; }
        auto issueO = [&](bf16x8* f, int blk) { ld4_sc1(f, vo16, sof, hl1(blk >> 1) * 32768 + qb * 8192 + 4096 * (blk & 1)); };
        issueW(hl1(0), 0, 5);
        BARX();
        issueW(hl1(0), 1, 6); issueW(hl1(1), 0, 7);
        issueV(10, 2 * SLOT); issueK(11, 4 * SLOT);
        BARX();
        issueV(11, 4 * SLOT); issueK(12, 0);
        WAITV(8);
        if (lane == 0) __hip_atomic_store(flagp(bid, 0, qb), tag, __ATOMIC_RELAXED, __HIP_MEMORY_SCOPE_AGENT);
        BARX();
        oblock<true>(acc, cur, smem + 5 * WT + trb);
        issueV(12, 0); issueK(13, 2 * SLOT);
        WAITV(8);
        BARX();
        issueO(cur, 2);
        issueW(hl1(1), 1, 5);
        oblock(acc, nxt, smem + 6 * WT + trb);
        issueO(nxt, 3);
        issueV(13, 2 * SLOT); issueK(14, 4 * SLOT);
        WAITV(17);
        PIN4(cur);
        oblock(acc, cur, smem + 7 * WT + trb);
        issueO(cur, 4);
        WAITV(12);
        BARX();
        unsigned wdummy = 0;
        {
            const int r = 64 + 40 * b + (lane < 40 ? lane : 0);
            const char* p = wo + (size_t)((8 * xq + hl1(1 + (r >> 7)) + 1) * 128 + (r & 127)) * 1280 + 320 * jj + (lw == 3 ? 316 : 128 * lw);
            asm volatile("global_load_dword %0, %1, off" : "+v"(wdummy) : "v"(p) : "memory");
        }
        issueW(hl1(2), 0, 6); issueW(hl1(2), 1, 7);
        PIN4(nxt);
        oblock(acc, nxt, smem + 5 * WT + trb);
        issueO(nxt, 5);
        issueV(14, 4 * SLOT); issueK(15, 0);
        WAITV(17);
        PIN(wdummy);
        BARX();
        issueW(hl1(3), 0, 5);
        PIN4(cur);
        oblock(acc, cur, smem + 6 * WT + trb);
        issueO(cur, 6);
        issueV(15, 0);
        WAITV(13);
        BARX();
        issueW(hl1(3), 1, 6); issueW(hl1(0) + 1, 0, 2); issueW(hl1(0) + 1, 1, 3);
        PIN4(nxt);
        oblock(acc, nxt, smem + 7 * WT + trb);
        issueO(nxt, 7);
        WAITV(4);
        BARX();
        issueW(hl1(1) + 1, 0, 4);
        PIN4(cur);
        oblock(acc, cur, smem + 5 * WT + trb);
        WAITV(5);
        PIN4(nxt);
        oblock(acc, nxt, smem + 6 * WT + trb);
        WAITV(0);
        BARX();
        bf16x8 of2[24];
#pragma unroll
        for (int f = 0; f < 8; ++f) of2[f] = lds128(x1 + 1024 * f);
        BAR();
        issueW(hl1(1) + 1, 1, 5); issueW(hl1(2) + 1, 0, 6); issueW(hl1(2) + 1, 1, 1);
        oblock(acc, of2, smem + 2 * WT + trb);
        oblock(acc, of2 + 4, smem + 3 * WT + trb);
        WAITV(0);
        STAMP(20);
        BARX();
        const unsigned ob = qb * 8192 + 32768;
        auto ldo = [&](int k0) {
#pragma unroll
            for (int k = k0; k < k0 + 4; ++k)
                of2[k] = __builtin_bit_cast(bf16x8, __builtin_amdgcn_raw_buffer_load_b128(sof, vo16, (unsigned)hl1(1 + (k >> 3)) * 32768 + ob + (k & 7) * 1024, 16));
        };
        ldo(0); ldo(4); SB();
        oblock(acc, of2, smem + 4 * WT + trb);       ldo(8);  ldo(12); SB();
        oblock(acc, of2 + 4, smem + 5 * WT + trb);   ldo(16); SB();
        oblock(acc, of2 + 8, smem + 6 * WT + trb);   ldo(20); SB();
        oblock(acc, of2 + 12, smem + 1 * WT + trb);
        oblock(acc, of2 + 16, smem + 0 * WT + trb);
        oblock(acc, of2 + 20, smem + 7 * WT + trb);
        STAMP(22);
        const rsrc_t spb = mkrsrc(ws + WS_PB, 256 * PB_WG);
        f32x4 mine[5];
#define UNIT(u, e) acc[(u) >> 2][4 * ((u) & 3) + (e)]
#define UPK(u) cvtpk(UNIT(u, 0), UNIT(u, 1)), cvtpk(UNIT(u, 2), UNIT(u, 3))
#pragma unroll
        for (int g = 0; g < 4; ++g) {
            if (g == xq) {
#pragma unroll
                for (int n = 0; n < 5; ++n) mine[n] = (f32x4){UNIT(5 * g + n, 0), UNIT(5 * g + n, 1), UNIT(5 * g + n, 2), UNIT(5 * g + n, 3)};
            } else {
                const unsigned so = (unsigned)((bid & ~3) | g) * PB_WG + (xq * 4 + qb) * 5120;
                const u32x4 w01 = {UPK(5 * g), UPK(5 * g + 1)}, w23 = {UPK(5 * g + 2), UPK(5 * g + 3)};
                const u32x2 w4 = {UPK(5 * g + 4)};
                __builtin_amdgcn_raw_buffer_store_b128(w01, spb, vo16, so, 16);
                __builtin_amdgcn_raw_buffer_store_b128(w23, spb, vo16, so + 1024, 16);
                __builtin_amdgcn_raw_buffer_store_b64(w4, spb, lane * 8, so + 2048, 16);
            }
        }
        WAITV(0);
        if (lane == 0) __hip_atomic_store(flagp(bid, 2, qb), tag, __ATOMIC_RELAXED, __HIP_MEMORY_SCOPE_AGENT);
        STAMP(23);
        wait_flags(flagp((bid & ~3) | ((xq + 1 + (lane % 3)) & 3), 2, qb), tag);
        STAMP(24);
        {
            const rsrc_t sout = mkrsrc(out + ((size_t)b * SQ * (2 * DM) + br * DM + 160 * jj + 40 * xq) * 2, SQ * 2 * DM * 2 - (br * DM + 160 * jj + 40 * xq) * 2);
            const unsigned vop = rowq * (4 * DM) + 8 * h;
            u32x4 a01[3], a23[3]; u32x2 a4[3];
#pragma unroll
            for (int s = 0; s < 3; ++s) {
                const unsigned so = (unsigned)bid * PB_WG + (((xq + 1 + s) & 3) * 4 + qb) * 5120;
                a01[s] = __builtin_amdgcn_raw_buffer_load_b128(spb, vo16, so, 16);
                a23[s] = __builtin_amdgcn_raw_buffer_load_b128(spb, vo16, so + 1024, 16);
                a4[s] = __builtin_amdgcn_raw_buffer_load_b64(spb, lane * 8, so + 2048, 16);
            }
            auto up = [](unsigned lo, unsigned hi) { return (f32x4){__uint_as_float(lo << 16), __uint_as_float(lo & 0xffff0000u), __uint_as_float(hi << 16), __uint_as_float(hi & 0xffff0000u)}; };
#pragma unroll
            for (int s = 0; s < 3; ++s) {
                mine[0] += up(a01[s][0], a01[s][1]); mine[1] += up(a01[s][2], a01[s][3]);
                mine[2] += up(a23[s][0], a23[s][1]); mine[3] += up(a23[s][2], a23[s][3]); mine[4] += up(a4[s][0], a4[s][1]);
            }
#pragma unroll
            for (int n = 0; n < 5; ++n) __builtin_amdgcn_raw_buffer_store_b64((u32x2){cvtpk(mine[n][0], mine[n][1]), cvtpk(mine[n][2], mine[n][3])}, sout, vop, 16 * n, OUT_AUX);
        }
    }
    {
        if (xq == 0 && jj == 0 && wid == 0 && lane == 0)
            __hip_atomic_store((GAS u64*)(ws + WS_GEN + (br * NB + b) * 256), tag, __ATOMIC_RELAXED, __HIP_MEMORY_SCOPE_AGENT);
        STAMP(25);
    }
}

extern "C" void kernel_launch(void* const* d_in, const int* in_sizes, int n_in, void* d_out, int out_size,
                              void* d_ws, size_t ws_size, hipStream_t stream) {
    (void)hipFuncSetAttribute((const void*)fused_kernel, hipFuncAttributeMaxDynamicSharedMemorySize, LDS_BYTES);
    hipLaunchKernelGGL(fused_kernel, dim3(256), dim3(512), LDS_BYTES, stream,
                       (const char*)d_in[0], (const char*)d_in[1], (const char*)d_in[2], (const char*)d_in[3], (const char*)d_in[4],
                       (const char*)d_in[5], (const char*)d_in[6], (const char*)d_in[7], (const char*)d_in[8], (const char*)d_in[9],
                       (char*)d_ws + WS_SHIFT, (char*)d_out);
}
```

```cpp
#include <hip/hip_runtime.h>
#include <hip/hip_bf16.h>
#include <stdint.h>
#include <type_traits>

typedef short bf16x8 __attribute__((ext_vector_type(8)));
typedef short s16x4 __attribute__((ext_vector_type(4)));
typedef float f32x16 __attribute__((ext_vector_type(16)));
typedef float f32x4 __attribute__((ext_vector_type(4)));
typedef float f32x2 __attribute__((ext_vector_type(2)));
typedef __bf16 bf16x2_t __attribute__((ext_vector_type(2)));
typedef unsigned u32x4 __attribute__((ext_vector_type(4)));
typedef unsigned u32x2 __attribute__((ext_vector_type(2)));
typedef unsigned long long u64;
#define LAS __attribute__((address_space(3)))
#define GAS __attribute__((address_space(1)))
#define DI __device__ __forceinline__
typedef LAS char* lptr;
typedef __amdgpu_buffer_rsrc_t rsrc_t;

constexpr int NB = 8, SQ = 128, DM = 640, NH = 32, SK = 512, DH = 128;
constexpr float CEXP = 0.08838834764831845f * 1.4426950408889634f;
constexpr int SLOT = 16384;
constexpr int LDS_T = 6 * SLOT;
constexpr int WT = 20480;
constexpr int LDS_BYTES = 10 * SLOT;
constexpr size_t WS_GEN = 0, WS_FLAG = 8192, WS_OF = 1 << 20;
constexpr size_t OF_BYTES = (size_t)2 * NB * NH * 32768;
constexpr size_t WS_PB = WS_OF + OF_BYTES;
constexpr unsigned PB_WG = 4 * 8 * 3 * 1024;
#define NT_W 0
#define NT_X 0
#define NT_WO 0
#define PPRIO 2
#define PBF16 1
#define WS_SHIFT 0
#define POL 15
#define PRETOUCH 1
#define CL 180
constexpr unsigned CODE_LINES = CL;
#define WOWARM 1
#define ROT 1
#define OUT_AUX 0
#define PROBE_BID 77
#define QLATE 0
#define KVORD 2
#define WARMA 1
#define WARMA_AT 2
constexpr unsigned SPIN_CAP = 1u << 16;

#define MFMA32(a, b, c) __builtin_amdgcn_mfma_f32_32x32x16_bf16((a), (b), (c), 0, 0, 0)
#define WAITV(n) asm volatile("s_waitcnt vmcnt(" #n ")" ::: "memory")
#define SB() __builtin_amdgcn_sched_barrier(0)
#define BARX() do { SB(); asm volatile("s_barrier" ::: "memory"); SB(); } while (0)
#define BAR() do { SB(); asm volatile("s_waitcnt lgkmcnt(0)\n\ts_barrier" ::: "memory"); SB(); } while (0)

#define STAMP(idx)

DI unsigned cvtpk(float lo, float hi) {
    f32x2 v = {lo, hi};
    bf16x2_t b = __builtin_convertvector(v, bf16x2_t);
    return __builtin_bit_cast(unsigned, b);
}
DI bf16x8 pack8(const f32x16& x, int s) {
    u32x4 w = {cvtpk(x[8 * s + 0], x[8 * s + 1]), cvtpk(x[8 * s + 2], x[8 * s + 3]),
               cvtpk(x[8 * s + 4], x[8 * s + 5]), cvtpk(x[8 * s + 6], x[8 * s + 7])};
    return __builtin_bit_cast(bf16x8, w);
}
template <int NT> DI void dma4(unsigned lds, unsigned v0, unsigned v1, unsigned v2, unsigned v3, rsrc_t srd, unsigned soff) {
    if (NT) asm volatile("s_mov_b32 m0, %0\n\ts_nop 3\n\tbuffer_load_dwordx4 %1, %5, %6 offen nt lds\n\tbuffer_load_dwordx4 %2, %5, %6 offen offset:1024 nt lds\n\t"
                         "buffer_load_dwordx4 %3, %5, %6 offen offset:2048 nt lds\n\tbuffer_load_dwordx4 %4, %5, %6 offen offset:3072 nt lds"
                         :: "s"(lds), "v"(v0), "v"(v1), "v"(v2), "v"(v3), "s"(srd), "s"(soff) : "memory");
    else asm volatile("s_mov_b32 m0, %0\n\ts_nop 3\n\tbuffer_load_dwordx4 %1, %5, %6 offen lds\n\tbuffer_load_dwordx4 %2, %5, %6 offen offset:1024 lds\n\t"
                      "buffer_load_dwordx4 %3, %5, %6 offen offset:2048 lds\n\tbuffer_load_dwordx4 %4, %5, %6 offen offset:3072 lds"
                      :: "s"(lds), "v"(v0), "v"(v1), "v"(v2), "v"(v3), "s"(srd), "s"(soff) : "memory");
}
template <int NT> DI void dma1(unsigned lds, unsigned voff, rsrc_t srd, unsigned soff) {
    if (NT) asm volatile("s_mov_b32 m0, %0\n\ts_nop 3\n\tbuffer_load_dwordx4 %1, %2, %3 offen nt lds" :: "s"(lds), "v"(voff), "s"(srd), "s"(soff) : "memory");
    else asm volatile("s_mov_b32 m0, %0\n\ts_nop 3\n\tbuffer_load_dwordx4 %1, %2, %3 offen lds" :: "s"(lds), "v"(voff), "s"(srd), "s"(soff) : "memory");
}
DI void ld4_sc1(bf16x8* d, unsigned voff, rsrc_t srd, unsigned soff) {
    asm volatile("s_nop 4\n\tbuffer_load_dwordx4 %0, %4, %5, %6 offen sc1\n\tbuffer_load_dwordx4 %1, %4, %5, %6 offen offset:1024 sc1\n\t"
                 "buffer_load_dwordx4 %2, %4, %5, %6 offen offset:2048 sc1\n\tbuffer_load_dwordx4 %3, %4, %5, %6 offen offset:3072 sc1"
                 : "+v"(d[0]), "+v"(d[1]), "+v"(d[2]), "+v"(d[3]) : "v"(voff), "s"(srd), "s"(soff) : "memory");
}
constexpr unsigned BIAS = 4096;
#define WAITV_F(n, f) asm volatile("s_waitcnt vmcnt(" #n ")" : "+v"((f)[0]), "+v"((f)[1]), "+v"((f)[2]), "+v"((f)[3]) :: "memory")
DI bf16x8 lds128(lptr p) { return *(const LAS bf16x8*)p; }
DI s16x4 ldstr(lptr p) {
    typedef short v4i16_t __attribute__((ext_vector_type(4)));
    return __builtin_amdgcn_ds_read_tr16_b64_v4i16((LAS v4i16_t*)p);
}
DI bf16x8 cat(s16x4 a, s16x4 b) { return __builtin_shufflevector(a, b, 0, 1, 2, 3, 4, 5, 6, 7); }
DI float swapmax(float v) {
    auto r = __builtin_amdgcn_permlane32_swap(__float_as_uint(v), __float_as_uint(v), false, false);
    return fmaxf(__uint_as_float(r[0]), __uint_as_float(r[1]));
}
DI float swapsum(float v) {
    auto r = __builtin_amdgcn_permlane32_swap(__float_as_uint(v), __float_as_uint(v), false, false);
    return __uint_as_float(r[0]) + __uint_as_float(r[1]);
}
DI rsrc_t mkrsrc(const void* p, unsigned bytes) {
    return __builtin_amdgcn_make_buffer_rsrc((void*)p, (short)0, (int)bytes, 0x00020000);
}
DI void zero16(f32x16& v) {
#pragma unroll
    for (int r = 0; r < 16; ++r) v[r] = 0.f;
}
DI void wait_flags(const GAS u64* p, u64 tag) {
#pragma unroll 1
    for (unsigned it = 0; it < SPIN_CAP; ++it) {
        const u64 v = __hip_atomic_load(p, __ATOMIC_RELAXED, __HIP_MEMORY_SCOPE_AGENT);
        if (__all(v == tag)) break;
        __builtin_amdgcn_s_sleep(1);
    }
    asm volatile("" ::: "memory");
}

DI float max3f(float a, float b, float c) { float r; asm("v_max3_f32 %0, %1, %2, %3" : "=v"(r) : "v"(a), "v"(b), "v"(c)); return r; }
DI float fadd_s(float a, float b) { float r; asm("v_add_f32_e32 %0, %1, %2" : "=v"(r) : "v"(a), "v"(b)); return r; }
DI float fmul_s(float a, float b) { float r; asm("v_mul_f32_e32 %0, %1, %2" : "=v"(r) : "v"(a), "v"(b)); return r; }
DI float fsub_s(float a, float b) { float r; asm("v_sub_f32_e32 %0, %1, %2" : "=v"(r) : "v"(a), "v"(b)); return r; }
DI float exp_s(float a) { float r; asm("v_exp_f32_e32 %0, %1" : "=v"(r) : "v"(a)); return r; }
#define MPAD4(a) asm volatile("s_nop 7\n\ts_nop 7" : "+v"((a)[0]), "+v"((a)[1]), "+v"((a)[2]), "+v"((a)[3]))
#define PIN(x) asm volatile("" : "+v"(x))
#define PIN4(f) asm volatile("" : "+v"((f)[0]), "+v"((f)[1]), "+v"((f)[2]), "+v"((f)[3]) :: "memory")
DI lptr opaque(lptr p) { unsigned a = (unsigned)(uintptr_t)p; asm volatile("" : "+v"(a)); return (lptr)(uintptr_t)a; }
#define LA_ 6
constexpr int LA = LA_;
#define SM_THR 8.0f

struct QBase { lptr x0, x1, w; };
DI void qload(int i, bf16x8* wf, bf16x8* xf, const QBase& q) {
    const int s = i >> 2, d = i & 3;
    if (d == 0) xf[s] = lds128(((s & 1) ? q.x1 : q.x0) + 512 * (s >> 1));
    wf[i & 7] = cat(ldstr(q.w + 4096 * s + 512 * d), ldstr(q.w + 4096 * s + 512 * d + 256));
}
DI void qpre(bf16x8* wf, bf16x8* xf, const QBase& q) {
#pragma unroll
    for (int i = 0; i < LA; ++i) qload(i, wf, xf, q);
}
template <bool NEXT> DI void qsteps(f32x16* acc, bf16x8* wf, bf16x8* xf, const QBase& q, const QBase& qn) {
#pragma unroll
    for (int i = 0; i < 16; ++i) {
        SB();
        acc[i & 3] = MFMA32(wf[i & 7], xf[i >> 2], acc[i & 3]);
        PIN(acc[i & 3]);
        SB();
        if (NEXT && i + LA == 16) BARX();
        if (i + LA < 16) qload(i + LA, wf, xf, q); else if (NEXT) qload(i + LA - 16, wf, xf, qn);
    }
    SB();
}
template <bool FIRST = false> DI void oblock(f32x16* acc, const bf16x8* of, lptr wp_) {
    const lptr wp = opaque(wp_);
    bf16x8 wf[8];
    auto load = [&](int i) { wf[i & 7] = cat(ldstr(wp + 5120 * (i / 5) + 512 * (i % 5)), ldstr(wp + 5120 * (i / 5) + 2560 + 512 * (i % 5))); };
#pragma unroll
    for (int i = 0; i < LA; ++i) load(i);
#pragma unroll
    for (int i = 0; i < 20; ++i) {
        SB();
        if (FIRST && i < 5) { f32x16 z; zero16(z); acc[i] = MFMA32(wf[i & 7], of[0], z); }
        else acc[i % 5] = MFMA32(wf[i & 7], of[i / 5], acc[i % 5]);
        PIN(acc[i % 5]);
        SB();
        if (i + LA < 20) load(i + LA);
    }
    SB();
}
DI void aload(int i, bf16x8* fr, lptr vp, lptr kp0, lptr kp1) {
    if (i < 16) fr[i & 7] = cat(ldstr(vp + 4096 * (i >> 2) + 512 * (i & 3)), ldstr(vp + 4096 * (i >> 2) + 2048 + 512 * (i & 3)));
    else { const int s = i & 7; fr[i & 7] = lds128(((s & 1) ? kp1 : kp0) + 512 * (s >> 1) + 8192 * ((i - 16) >> 3)); }
}

__global__ __launch_bounds__(512, 2) void fused_kernel(
    const char* xA, const char* kA, const char* vA, const char* wqA, const char* woA,
    const char* xB, const char* kB, const char* vB, const char* wqB, const char* woB,
    char* ws, char* out)
{
    extern __shared__ __attribute__((aligned(16))) char smem_[];
    const lptr smem = (lptr)smem_;
    const unsigned lds0 = (unsigned)(uintptr_t)smem;
    const int tid = threadIdx.x, lane = tid & 63;
    const int wid = __builtin_amdgcn_readfirstlane(tid >> 6);
    const int bid = blockIdx.x;
    const int xg = bid & 7, ii = bid >> 3;
    const int br = xg >> 2, xq = xg & 3, b = ii >> 2, jj = ii & 3;
    const int h1 = 8 * xq + 2 * jj;
    const char* x = br ? xB : xA;
    const char* kc = br ? kB : kA;
    const char* vc = br ? vB : vA;
    const char* wq = br ? wqB : wqA;
    const char* wo = br ? woB : woA;
    const int qb = wid & 3, grp = wid >> 2, lw = wid & 3;
    const int l31 = lane & 31, h = lane >> 5;
    STAMP(0);
    const char* codebase;
    asm volatile("s_getpc_b64 %0" : "=s"(codebase));

    u64 gen;
    {
        const char* gp = ws + WS_GEN + (br * NB + b) * 256;
        asm volatile("global_load_dwordx2 %0, %1, off sc1" : "=v"(gen) : "v"(gp) : "memory");
    }

    const rsrc_t sk = mkrsrc(kc + (size_t)(b * NH + h1) * SK * DH * 2 - BIAS, 2 * SK * DH * 2 + BIAS);
    const rsrc_t sv = mkrsrc(vc + (size_t)(b * NH + h1) * SK * DH * 2 - BIAS, 2 * SK * DH * 2 + BIAS);
    const rsrc_t sof = mkrsrc(ws + WS_OF + (size_t)((br * NB + b) * NH + 8 * xq) * 32768, 8 * 32768);

    const unsigned dsub = lane >> 5, dr = (lane >> 2) & 7, dsl = lane & 3;
    const unsigned xr0 = 16u * (dsl ^ ((dr >> 2) & 3)), xr1 = 16u * (dsl ^ ((2 + (dr >> 2)) & 3));
    const unsigned vk0 = BIAS + 4096 * lw + dr * 256 + dsub * 64 + xr0, vk2 = BIAS + 4096 * lw + dr * 256 + dsub * 64 + xr1;
    const unsigned vv0 = BIAS + 4096 * lw + dr * 256 + dsub * 64 + 16 * dsl;
    const unsigned ldw = lds0 + 4096 * lw;

    auto issueK = [&](int t, unsigned pair) {
        if ((POL & 1) == ((POL >> 2) & 1) ? (POL & 1) : ((POL >> (2 * br)) & 1)) dma4<1>(ldw + pair, vk0, vk0 - 896, vk2, vk2 - 896, sk, (unsigned)t * 16384u);
        else dma4<0>(ldw + pair, vk0, vk0 - 896, vk2, vk2 - 896, sk, (unsigned)t * 16384u); };
    auto issueV = [&](int t, unsigned pair) {
        if (((POL >> 1) & 1) == ((POL >> 3) & 1) ? ((POL >> 1) & 1) : ((POL >> (2 * br + 1)) & 1)) dma4<1>(ldw + pair + SLOT, vv0, vv0 - 896, vv0, vv0 - 896, sv, (unsigned)t * 16384u);
        else dma4<0>(ldw + pair + SLOT, vv0, vv0 - 896, vv0, vv0 - 896, sv, (unsigned)t * 16384u); };
    auto nextpair = [](unsigned p) { return (p == 4u * SLOT) ? 0u : p + 2u * SLOT; };

    const int G1 = (lane >> 4) & 1, tq = (lane & 15) >> 2, tp = lane & 3;
    const int sig = ((tp & 1) << 1) | (tp >> 1);
    const unsigned wqb = SLOT + 2048 * h + 64 * tq + 32 * G1 + 8 * sig;
    const unsigned rowq = 32 * qb + l31;
    const unsigned xsw = (rowq >> 2) & 3;
    const unsigned xb0 = 1024 * (rowq >> 3) + 64 * (rowq & 7) + 16 * ((0 + h) ^ xsw);
    const unsigned xb1 = 1024 * (rowq >> 3) + 64 * (rowq & 7) + 16 * ((2 + h) ^ xsw);
    const unsigned ksw = (l31 >> 2) & 3;
    const unsigned kb0 = 2048 * (l31 >> 3) + 64 * (l31 & 7) + 16 * ((0 + h) ^ ksw);
    const unsigned kb1 = 2048 * (l31 >> 3) + 64 * (l31 & 7) + 16 * ((2 + h) ^ ksw);
    const unsigned trb = 64 * (4 * h + tq) + 32 * G1 + 8 * tp;
    const unsigned vo16 = lane * 16;

    auto flagp = [&](int bb, int stage, int w) { return (GAS u64*)(ws + WS_FLAG + (size_t)((bb * 3 + stage) * 8 + w) * 64); };
    auto partner = [&](int dj) { return (bid & ~24) | (((jj + dj) & 3) << 3); };
    auto hl1 = [&](int dj) { return 2 * ((jj + dj) & 3); };

    f32x16 acc[5];
    bf16x8 qf[8], pf[4], fr[8];
    f32x16 s0, s1;
#pragma unroll
    for (int d = 0; d < 4; ++d) zero16(acc[d]);

    auto nextstage = [](unsigned st) { return st == 8u * SLOT ? 4u * SLOT : (st == 4u * SLOT ? 6u * SLOT : (st == 6u * SLOT ? 2u * SLOT : 8u * SLOT)); };
    auto qbase = [&](unsigned st) { return QBase{opaque(smem + st + xb0), opaque(smem + st + xb1), opaque(smem + st + wqb)}; };
    if (grp == 0) {
        unsigned st = 8 * SLOT;
        unsigned dummy = 0;
        {
            const unsigned line = lw * 64 + lane;
            const char* p = codebase + (line < CODE_LINES ? line : 0) * 128;
            asm volatile("global_load_dword %0, %1, off" : "+v"(dummy) : "v"(p) : "memory");
        }
        BARX();
        QBase qc = qbase(st);
        qpre(fr, pf, qc);
#pragma unroll 1
        for (int i = 0; i < 10; ++i) {
            st = nextstage(st);
            const QBase qn = qbase(st);
            qsteps<true>(acc, fr, pf, qc, qn);
            qc = qn;
            if (__builtin_expect(i == WARMA_AT, 0)) {
                const int idx = lw * 64 + lane;
                const char* p1 = wq + (size_t)(80 * b + (idx < 160 ? idx >> 1 : 0)) * 8192 + (h1 + 1) * 256 + (idx & 1) * 128;
                asm volatile("global_load_dword %0, %1, off" : "+v"(dummy) : "v"(p1) : "memory");
                if (WARMA >= 2) {
                    const char* p2 = wo + (size_t)(8 * xq * 128) * 1280 + (size_t)(ii * 320 + idx) * 128;
                    const char* p3 = wo + (size_t)(8 * xq * 128) * 1280 + (size_t)(ii * 320 + 256 + (idx & 63)) * 128;
                    asm volatile("global_load_dword %0, %1, off" : "+v"(dummy) : "v"(p2) : "memory");
                    if (lw == 0) asm volatile("global_load_dword %0, %1, off" : "+v"(dummy) : "v"(p3) : "memory");
                }
            }
        }
        WAITV(0);
        PIN(dummy);
    } else {
        const rsrc_t sx = mkrsrc(x + (size_t)b * SQ * DM * 2, SQ * DM * 2);
        const rsrc_t sw = mkrsrc(wq + (size_t)h1 * 256 - BIAS, DM * 8192 - h1 * 256 + BIAS);
        const unsigned vx0 = (32 * lw + dr) * 1280 + dsub * 64 + xr0, vx1 = (32 * lw + 8 + dr) * 1280 + dsub * 64 + xr1 - 1024;
        const unsigned vw0 = BIAS + (16 * lw + dr) * 8192 + dsub * 64 + 16 * dsl;
        const int rot = ROT ? b + 3 * jj : 0;
        constexpr unsigned T0 = 6 * SLOT, T1 = 8 * SLOT, P1 = 2 * SLOT, P2 = 4 * SLOT;
        int ci = rot >= 10 ? rot - 10 : rot;
        auto issueQn = [&](int hd, unsigned st) {
            dma4<NT_W>(ldw + st + SLOT, vw0, vw0 - 896, vw0 + 65536 - 2048, vw0 + 65536 - 2944, sw, (unsigned)ci * 524288u + 256 * hd);
            dma4<NT_X>(ldw + st, vx0, vx1, vx0 + 20480 - 2048, vx1 + 20480 - 2048, sx, 128 * ci);
            ci = ci == 9 ? 0 : ci + 1;
        };
        issueQn(0, T1);
        WAITV(0); BARX();
        issueQn(0, P2); issueQn(0, T0);
        WAITV(8);  BARX(); issueQn(0, P1);
        WAITV(8);  BARX(); issueQn(0, T1);
        WAITV(8);  BARX(); issueQn(0, P2);
        WAITV(8);  BARX(); issueQn(0, T0); issueK(0, 0); issueV(0, 0);
        WAITV(16); BARX(); issueQn(0, P1);
        WAITV(16); BARX(); issueQn(0, T1);
        WAITV(8);  BARX(); issueQn(0, P2);
        WAITV(8);  BARX(); issueQn(1, T0);
        WAITV(8);  BARX(); issueK(1, P1); issueV(1, P1);
        BARX(); issueQn(1, T1);
        const QBase q0 = qbase(T0), q1 = qbase(T1);
        WAITV(16); BARX();
        qpre(fr, pf, q0);
        issueK(2, P2);
        qsteps<false>(acc, fr, pf, q0, q0);
        WAITV(4);
        unsigned pv = P2;
#pragma unroll 1
        for (int c = 1; c < 9; c += 2) {
            unsigned pk = nextpair(pv);
            BARX();
            qpre(fr, pf, q1);
            issueQn(1, T0);
            if (KVORD >= 1) issueV(c + 1, pv);
            if (KVORD >= 2) issueK(c + 2, pk);
            qsteps<false>(acc, fr, pf, q1, q1);
            if (KVORD < 1) issueV(c + 1, pv);
            if (KVORD < 2) issueK(c + 2, pk);
            pv = nextpair(pk);
            WAITV(8);
            BARX();
            qpre(fr, pf, q0);
            issueQn(1, T1);
            if (KVORD >= 1) issueV(c + 2, pk);
            if (KVORD >= 2) issueK(c + 3, pv);
            qsteps<false>(acc, fr, pf, q0, q0);
            if (KVORD < 1) issueV(c + 2, pk);
            if (KVORD < 2) issueK(c + 3, pv);
            WAITV(8);
        }
        BARX();
        qpre(fr, pf, q1);
        qsteps<false>(acc, fr, pf, q1, q1);
    }
    STAMP(1);
    asm volatile("" : "+v"(gen));
    const u64 tag = gen + 1;

    __builtin_amdgcn_s_setprio(PPRIO);
    MPAD4(acc);
    {
        float cexp = CEXP; PIN(cexp);
#pragma unroll
        for (int d = 0; d < 4; ++d) {
#pragma unroll
            for (int r = 0; r < 16; ++r) acc[d][r] = fmul_s(acc[d][r], cexp);
            qf[2 * d] = pack8(acc[d], 0); qf[2 * d + 1] = pack8(acc[d], 1); zero16(acc[d]);
        }
    }
    if (grp == 0) BARX();
    float la = 0.f, lb = 0.f, lc = 0.f, ld = 0.f;
    {
        f32x16 negm, p0; zero16(negm); zero16(s1); zero16(p0);
        unsigned pk = grp ? 4 * SLOT : 0;
        lptr kp0 = opaque(smem + pk + kb0), kp1 = opaque(smem + pk + kb1);
#pragma unroll
        for (int i = 16; i < 16 + LA; ++i) aload(i, fr, kp0, kp0, kp1);
        const GAS u64* fp = flagp(partner(1 + (lane % 3)), 0, qb);
        u64 fv = 0;
        const int tw = grp ? 3 : -1;
        float thr = -__builtin_inff(), dmin = -__builtin_inff();
        u32x4 pw[4];
#pragma unroll 1
        for (int t = 0; t < 8; ++t) {
            if (__builtin_expect(t == tw, 0)) asm volatile("global_load_dwordx2 %0, %1, off sc1" : "+v"(fv) : "v"(fp) : "memory");
            float c0, c1;
#pragma unroll
            for (int j = 0; j < 16; ++j) {
                const int i = 16 + j;
                SB();
                if (j < 8) s0 = MFMA32(fr[i & 7], qf[j], j == 0 ? negm : s0); else s1 = MFMA32(fr[i & 7], qf[j - 8], j == 8 ? negm : s1);
                SB();
                if (i + LA < 32) aload(i + LA, fr, kp0, kp0, kp1);
                if (j < 8)
                    asm volatile("v_add_f32_e32 %0, %0, %4\n\tv_add_f32_e32 %1, %1, %5\n\tv_add_f32_e32 %2, %2, %6\n\tv_add_f32_e32 %3, %3, %7"
                                 : "+v"(la), "+v"(lb), "+v"(lc), "+v"(ld) : "v"(s1[2 * j]), "v"(s1[2 * j + 1]), "v"(p0[2 * j]), "v"(p0[2 * j + 1]));
                if (j >= 10) {
                    const int e = 2 * (j - 10);
                    float e0, e1; unsigned pk_;
                    if (j == 10)
                        asm volatile("v_exp_f32_e32 %0, %5\n\tv_exp_f32_e32 %1, %6\n\tv_max3_f32 %3, %5, %6, %7\n\tv_max3_f32 %4, %8, %9, %10\n\tv_cvt_pk_bf16_f32 %2, %0, %1"
                                     : "=&v"(e0), "=&v"(e1), "=&v"(pk_), "=&v"(c0), "=&v"(c1) : "v"(s0[0]), "v"(s0[1]), "v"(s0[12]), "v"(s0[13]), "v"(s0[14]), "v"(s0[15]));
                    else if (j & 1)
                        asm volatile("v_exp_f32_e32 %0, %4\n\tv_exp_f32_e32 %1, %5\n\tv_max3_f32 %3, %3, %4, %5\n\tv_cvt_pk_bf16_f32 %2, %0, %1"
                                     : "=&v"(e0), "=&v"(e1), "=&v"(pk_), "+v"(c1) : "v"(s0[e]), "v"(s0[e + 1]));
                    else
                        asm volatile("v_exp_f32_e32 %0, %4\n\tv_exp_f32_e32 %1, %5\n\tv_max3_f32 %3, %3, %4, %5\n\tv_cvt_pk_bf16_f32 %2, %0, %1"
                                     : "=&v"(e0), "=&v"(e1), "=&v"(pk_), "+v"(c0) : "v"(s0[e]), "v"(s0[e + 1]));
                    p0[e] = e0; p0[e + 1] = e1;
                    pw[e >> 3][(e >> 1) & 3] = pk_;
                }
            }
            WAITV(0);
            if (__builtin_expect(t == tw, 0)) { PIN(fv); if (!__all(fv == tag)) wait_flags(fp, tag); }
            STAMP(2 + 2 * t);
            BARX();
            const lptr vp = opaque(smem + pk + SLOT + trb);
            pk = nextpair(pk);
            kp0 = opaque(smem + pk + kb0); kp1 = opaque(smem + pk + kb1);
#pragma unroll
            for (int i = 0; i < LA; ++i) aload(i, fr, vp, vp, vp);
            PIN(s1);
            SB();
            {
                float c2, c3;
                asm volatile("v_max3_f32 %2, %4, %5, %6\n\tv_max3_f32 %3, %7, %8, %9\n\tv_max3_f32 %0, %0, %10, %11\n\tv_max3_f32 %1, %1, %12, %13\n\t"
                             "v_max3_f32 %2, %2, %14, %15\n\tv_max3_f32 %3, %3, %16, %17\n\tv_max3_f32 %0, %0, %18, %19\n\tv_max3_f32 %1, %1, %2, %3\n\tv_max_f32_e32 %0, %0, %1"
                             : "+v"(c0), "+v"(c1), "=&v"(c2), "=&v"(c3)
                             : "v"(s1[0]), "v"(s1[1]), "v"(s1[2]), "v"(s1[3]), "v"(s1[4]), "v"(s1[5]), "v"(s1[6]), "v"(s1[7]),
                               "v"(s1[8]), "v"(s1[9]), "v"(s1[10]), "v"(s1[11]), "v"(s1[12]), "v"(s1[13]), "v"(s1[14]), "v"(s1[15]));
                auto r_ = __builtin_amdgcn_permlane32_swap(__float_as_uint(c0), __float_as_uint(c0), false, false);
                const float mx = max3f(__uint_as_float(r_[0]), __uint_as_float(r_[1]), __uint_as_float(r_[1]));
                if (__builtin_expect(__any(!(mx <= thr)), 0)) {
                    const float d = fmaxf(mx, dmin);
                    float f = __builtin_amdgcn_exp2f(fminf(-d, 0.f));
                    asm volatile("s_nop 1" : "+v"(f));
#pragma unroll
                    for (int r = 0; r < 16; ++r) { s0[r] = fsub_s(s0[r], d); s1[r] = fsub_s(s1[r], d); negm[r] = fsub_s(negm[r], d); }
                    la = fmul_s(la, f); lb = fmul_s(lb, f); lc = fmul_s(lc, f); ld = fmul_s(ld, f);
#pragma unroll
                    for (int dd = 0; dd < 4; ++dd)
#pragma unroll
                        for (int r = 0; r < 16; ++r) acc[dd][r] = fmul_s(acc[dd][r], f);
#pragma unroll
                    for (int e = 0; e < 12; ++e) p0[e] = __builtin_amdgcn_exp2f(s0[e]);
#pragma unroll
                    for (int e = 0; e < 12; e += 2) pw[e >> 3][(e >> 1) & 3] = cvtpk(p0[e], p0[e + 1]);
                }
                thr = SM_THR; dmin = 0.f;
            }
#pragma unroll
            for (int i = 0; i < 16; ++i) {
                SB();
                acc[i & 3] = MFMA32(fr[i & 7], __builtin_bit_cast(bf16x8, pw[i >> 2]), acc[i & 3]);
                SB();
                constexpr int pairs[16] = {6, 7, 8, 9, 10, 11, -1, 12, 13, 14, 15, -1, -1, -1, -1, -1};
                const int q = pairs[i];
                if (q >= 0) {
                    if (q < 8) { p0[2 * q] = __builtin_amdgcn_exp2f(s0[2 * q]); p0[2 * q + 1] = __builtin_amdgcn_exp2f(s0[2 * q + 1]); PIN(p0); }
                    else { s1[2 * q - 16] = __builtin_amdgcn_exp2f(s1[2 * q - 16]); s1[2 * q - 15] = __builtin_amdgcn_exp2f(s1[2 * q - 15]); PIN(s1); }
                }
                aload(i + LA, fr, vp, kp0, kp1);
                if (q >= 0) {
                    pw[q >> 2][q & 3] = q < 8 ? cvtpk(p0[2 * q], p0[2 * q + 1]) : cvtpk(s1[2 * q - 16], s1[2 * q - 15]);
                    PIN(pw[q >> 2][q & 3]);
                }
            }
            SB();
            STAMP(3 + 2 * t);
        }
#pragma unroll
        for (int j = 0; j < 8; ++j) { la = fadd_s(la, s1[2 * j]); lb = fadd_s(lb, s1[2 * j + 1]); lc = fadd_s(lc, p0[2 * j]); ld = fadd_s(ld, p0[2 * j + 1]); }
    }
    if (grp == 0) { __builtin_amdgcn_s_setprio(0); BARX(); }
    STAMP(18);

    {
        float inv = __builtin_amdgcn_rcpf(swapsum((la + lb) + (lc + ld)));
        asm volatile("s_nop 1" : "+v"(inv));
        MPAD4(acc);
#pragma unroll
        for (int d = 0; d < 4; ++d) {
#pragma unroll
            for (int r = 0; r < 16; ++r) acc[d][r] = fmul_s(acc[d][r], inv);
            qf[2 * d] = pack8(acc[d], 0); qf[2 * d + 1] = pack8(acc[d], 1);
            __builtin_amdgcn_raw_buffer_store_b128(__builtin_bit_cast(u32x4, qf[2 * d]), sof, vo16, (2 * jj + grp) * 32768 + qb * 8192 + d * 2048, 16);
            __builtin_amdgcn_raw_buffer_store_b128(__builtin_bit_cast(u32x4, qf[2 * d + 1]), sof, vo16, (2 * jj + grp) * 32768 + qb * 8192 + d * 2048 + 1024, 16);
            SB();
        }
    }
    const rsrc_t swo = mkrsrc(wo + (size_t)(8 * xq * 128) * 1280 + 320 * jj - BIAS, 1024 * 1280 - 320 * jj + BIAS);
    const unsigned vwA = BIAS + (16 * lw + dr) * 1280 + dsub * 64 + 16 * dsl, vwB = BIAS + (16 * lw + dr) * 1280 + 16 * dsl + (dsub ? 8 * 1280 : 256) - 2048;
    auto issueW = [&](int hl, int half, int buf) {
        const unsigned so = (unsigned)(hl * 128 + 64 * half) * 1280u, ld = lds0 + buf * WT + 5 * lw * 1024;
        dma4<NT_WO>(ld, vwA, vwA - 896, vwB, vwA + 10240 + 64 - 3072, swo, so);
        dma1<NT_WO>(ld + 4096, vwA, swo, so + 10240 + 192);
    };
    const lptr x1 = smem + (lw < 2 ? lw * 8192 : 7 * WT + (lw - 2) * 8192) + vo16;
    if (grp) {
#pragma unroll
        for (int f = 0; f < 8; ++f) *(LAS bf16x8*)(x1 + 1024 * f) = qf[f];
        __builtin_amdgcn_s_setprio(0);
        BAR();
        BARX();
        WAITV(0);
        if (lane == 0) __hip_atomic_store(flagp(bid, 1, qb), tag, __ATOMIC_RELAXED, __HIP_MEMORY_SCOPE_AGENT);
        STAMP(19);
        issueW(hl1(3) + 1, 0, 0); issueW(hl1(3) + 1, 1, 7);
        wait_flags(flagp(partner(1 + (lane % 3)), 1, qb), tag);
        WAITV(0);
        STAMP(21);
        BARX();
    } else {
        bf16x8 cur[4], nxt[4];
#pragma unroll
        for (int k = 0; k < 4; ++k) { cur[k] = qf[k]; nxt[k] = qf[4 + k]; }
        auto issueO = [&](bf16x8* f, int blk) { ld4_sc1(f, vo16, sof, hl1(blk >> 1) * 32768 + qb * 8192 + 4096 * (blk & 1)); };
        issueW(hl1(0), 0, 5);
        BARX();
        issueW(hl1(0), 1, 6); issueW(hl1(1), 0, 7);
        issueV(10, 2 * SLOT); issueK(11, 4 * SLOT);
        BARX();
        issueV(11, 4 * SLOT); issueK(12, 0);
        WAITV(8);
        if (lane == 0) __hip_atomic_store(flagp(bid, 0, qb), tag, __ATOMIC_RELAXED, __HIP_MEMORY_SCOPE_AGENT);
        BARX();
        oblock<true>(acc, cur, smem + 5 * WT + trb);
        issueV(12, 0); issueK(13, 2 * SLOT);
        WAITV(8);
        BARX();
        issueO(cur, 2);
        issueW(hl1(1), 1, 5);
        oblock(acc, nxt, smem + 6 * WT + trb);
        issueO(nxt, 3);
        issueV(13, 2 * SLOT); issueK(14, 4 * SLOT);
        WAITV(17);
        PIN4(cur);
        oblock(acc, cur, smem + 7 * WT + trb);
        issueO(cur, 4);
        WAITV(12);
        BARX();
        unsigned wdummy = 0;
        {
            const int r = 64 + 40 * b + (lane < 40 ? lane : 0);
            const char* p = wo + (size_t)((8 * xq + hl1(1 + (r >> 7)) + 1) * 128 + (r & 127)) * 1280 + 320 * jj + (lw == 3 ? 316 : 128 * lw);
            asm volatile("global_load_dword %0, %1, off" : "+v"(wdummy) : "v"(p) : "memory");
        }
        issueW(hl1(2), 0, 6); issueW(hl1(2), 1, 7);
        PIN4(nxt);
        oblock(acc, nxt, smem + 5 * WT + trb);
        issueO(nxt, 5);
        issueV(14, 4 * SLOT); issueK(15, 0);
        WAITV(17);
        PIN(wdummy);
        BARX();
        issueW(hl1(3), 0, 5);
        PIN4(cur);
        oblock(acc, cur, smem + 6 * WT + trb);
        issueO(cur, 6);
        issueV(15, 0);
        WAITV(13);
        BARX();
        issueW(hl1(3), 1, 6); issueW(hl1(0) + 1, 0, 2); issueW(hl1(0) + 1, 1, 3);
        PIN4(nxt);
        oblock(acc, nxt, smem + 7 * WT + trb);
        issueO(nxt, 7);
        WAITV(4);
        BARX();
        issueW(hl1(1) + 1, 0, 4);
        PIN4(cur);
        oblock(acc, cur, smem + 5 * WT + trb);
        WAITV(5);
        PIN4(nxt);
        oblock(acc, nxt, smem + 6 * WT + trb);
        WAITV(0);
        BARX();
        bf16x8 of2[24];
#pragma unroll
        for (int f = 0; f < 8; ++f) of2[f] = lds128(x1 + 1024 * f);
        BAR();
        issueW(hl1(1) + 1, 1, 5); issueW(hl1(2) + 1, 0, 6); issueW(hl1(2) + 1, 1, 1);
        oblock(acc, of2, smem + 2 * WT + trb);
        oblock(acc, of2 + 4, smem + 3 * WT + trb);
        WAITV(0);
        STAMP(20);
        BARX();
        const unsigned ob = qb * 8192 + 32768;
        auto ldo = [&](int k0) {
#pragma unroll
            for (int k = k0; k < k0 + 4; ++k)
                of2[k] = __builtin_bit_cast(bf16x8, __builtin_amdgcn_raw_buffer_load_b128(sof, vo16, (unsigned)hl1(1 + (k >> 3)) * 32768 + ob + (k & 7) * 1024, 16));
        };
        ldo(0); ldo(4); SB();
        oblock(acc, of2, smem + 4 * WT + trb);       ldo(8);  ldo(12); SB();
        oblock(acc, of2 + 4, smem + 5 * WT + trb);   ldo(16); SB();
        oblock(acc, of2 + 8, smem + 6 * WT + trb);   ldo(20); SB();
        oblock(acc, of2 + 12, smem + 1 * WT + trb);
        oblock(acc, of2 + 16, smem + 0 * WT + trb);
        oblock(acc, of2 + 20, smem + 7 * WT + trb);
        STAMP(22);
        const rsrc_t spb = mkrsrc(ws + WS_PB, 256 * PB_WG);
        f32x4 mine[5];
#define UNIT(u, e) acc[(u) >> 2][4 * ((u) & 3) + (e)]
#define UPK(u) cvtpk(UNIT(u, 0), UNIT(u, 1)), cvtpk(UNIT(u, 2), UNIT(u, 3))
#pragma unroll
        for (int g = 0; g < 4; ++g) {
            if (g == xq) {
#pragma unroll
                for (int n = 0; n < 5; ++n) mine[n] = (f32x4){UNIT(5 * g + n, 0), UNIT(5 * g + n, 1), UNIT(5 * g + n, 2), UNIT(5 * g + n, 3)};
            } else {
                const unsigned so = (unsigned)((bid & ~3) | g) * PB_WG + (xq * 4 + qb) * 5120;
                const u32x4 w01 = {UPK(5 * g), UPK(5 * g + 1)}, w23 = {UPK(5 * g + 2), UPK(5 * g + 3)};
                const u32x2 w4 = {UPK(5 * g + 4)};
                __builtin_amdgcn_raw_buffer_store_b128(w01, spb, vo16, so, 16);
                __builtin_amdgcn_raw_buffer_store_b128(w23, spb, vo16, so + 1024, 16);
                __builtin_amdgcn_raw_buffer_store_b64(w4, spb, lane * 8, so + 2048, 16);
            }
        }
        WAITV(0);
        if (lane == 0) __hip_atomic_store(flagp(bid, 2, qb), tag, __ATOMIC_RELAXED, __HIP_MEMORY_SCOPE_AGENT);
        STAMP(23);
        wait_flags(flagp((bid & ~3) | ((xq + 1 + (lane % 3)) & 3), 2, qb), tag);
        STAMP(24);
        {
            const rsrc_t sout = mkrsrc(out + ((size_t)b * SQ * (2 * DM) + br * DM + 160 * jj + 40 * xq) * 2, SQ * 2 * DM * 2 - (br * DM + 160 * jj + 40 * xq) * 2);
            const unsigned vop = rowq * (4 * DM) + 8 * h;
            u32x4 a01[3], a23[3]; u32x2 a4[3];
#pragma unroll
            for (int s = 0; s < 3; ++s) {
                const unsigned so = (unsigned)bid * PB_WG + (((xq + 1 + s) & 3) * 4 + qb) * 5120;
                a01[s] = __builtin_amdgcn_raw_buffer_load_b128(spb, vo16, so, 16);
                a23[s] = __builtin_amdgcn_raw_buffer_load_b128(spb, vo16, so + 1024, 16);
                a4[s] = __builtin_amdgcn_raw_buffer_load_b64(spb, lane * 8, so + 2048, 16);
            }
            auto up = [](unsigned lo, unsigned hi) { return (f32x4){__uint_as_float(lo << 16), __uint_as_float(lo & 0xffff0000u), __uint_as_float(hi << 16), __uint_as_float(hi & 0xffff0000u)}; };
#pragma unroll
            for (int s = 0; s < 3; ++s) {
                mine[0] += up(a01[s][0], a01[s][1]); mine[1] += up(a01[s][2], a01[s][3]);
                mine[2] += up(a23[s][0], a23[s][1]); mine[3] += up(a23[s][2], a23[s][3]); mine[4] += up(a4[s][0], a4[s][1]);
            }
#pragma unroll
            for (int n = 0; n < 5; ++n) __builtin_amdgcn_raw_buffer_store_b64((u32x2){cvtpk(mine[n][0], mine[n][1]), cvtpk(mine[n][2], mine[n][3])}, sout, vop, 16 * n, OUT_AUX);
        }
    }
    {
        if (xq == 0 && jj == 0 && wid == 0 && lane == 0)
            __hip_atomic_store((GAS u64*)(ws + WS_GEN + (br * NB + b) * 256), tag, __ATOMIC_RELAXED, __HIP_MEMORY_SCOPE_AGENT);
        STAMP(25);
    }
}

extern "C" void kernel_launch(void* const* d_in, const int* in_sizes, int n_in, void* d_out, int out_size,
                              void* d_ws, size_t ws_size, hipStream_t stream) {
    (void)hipFuncSetAttribute((const void*)fused_kernel, hipFuncAttributeMaxDynamicSharedMemorySize, LDS_BYTES);
    hipLaunchKernelGGL(fused_kernel, dim3(256), dim3(512), LDS_BYTES, stream,
                       (const char*)d_in[0], (const char*)d_in[1], (const char*)d_in[2], (const char*)d_in[3], (const char*)d_in[4],
                       (const char*)d_in[5], (const char*)d_in[6], (const char*)d_in[7], (const char*)d_in[8], (const char*)d_in[9],
                       (char*)d_ws + WS_SHIFT, (char*)d_out);
}
```

```cpp
#include <hip/hip_runtime.h>
#include <hip/hip_bf16.h>
#include <stdint.h>
#include <type_traits>

typedef short bf16x8 __attribute__((ext_vector_type(8)));
typedef short s16x4 __attribute__((ext_vector_type(4)));
typedef float f32x16 __attribute__((ext_vector_type(16)));
typedef float f32x4 __attribute__((ext_vector_type(4)));
typedef float f32x2 __attribute__((ext_vector_type(2)));
typedef __bf16 bf16x2_t __attribute__((ext_vector_type(2)));
typedef unsigned u32x4 __attribute__((ext_vector_type(4)));
typedef unsigned u32x2 __attribute__((ext_vector_type(2)));
typedef unsigned long long u64;
#define LAS __attribute__((address_space(3)))
#define GAS __attribute__((address_space(1)))
#define DI __device__ __forceinline__
typedef LAS char* lptr;
typedef __amdgpu_buffer_rsrc_t rsrc_t;

constexpr int NB = 8, SQ = 128, DM = 640, NH = 32, SK = 512, DH = 128;
constexpr float CEXP = 0.08838834764831845f * 1.4426950408889634f;
constexpr int SLOT = 16384;
constexpr int LDS_T = 6 * SLOT;
constexpr int WT = 20480;
constexpr int LDS_BYTES = 10 * SLOT;
constexpr size_t WS_GEN = 0, WS_FLAG = 8192, WS_OF = 1 << 20;
constexpr size_t OF_BYTES = (size_t)2 * NB * NH * 32768;
constexpr size_t WS_PB = WS_OF + OF_BYTES;
constexpr unsigned PB_WG = 4 * 8 * 3 * 1024;
#define NT_W 0
#define NT_X 0
#define NT_WO 0
#define PPRIO 2
#define PBF16 1
#define WS_SHIFT 0
#define POL 15
#define PRETOUCH 1
#define CL 180
constexpr unsigned CODE_LINES = CL;
#define WOWARM 1
#define ROT 1
#define OUT_AUX 0
#define PROBE_BID 77
#define QLATE 0
#define KVORD 2
#define WARMA 1
#define WARMA_AT 2
constexpr unsigned SPIN_CAP = 1u << 16;

#define MFMA32(a, b, c) __builtin_amdgcn_mfma_f32_32x32x16_bf16((a), (b), (c), 0, 0, 0)
#define WAITV(n) asm volatile("s_waitcnt vmcnt(" #n ")" ::: "memory")
#define SB() __builtin_amdgcn_sched_barrier(0)
#define BARX() do { SB(); asm volatile("s_barrier" ::: "memory"); SB(); } while (0)
#define BAR() do { SB(); asm volatile("s_waitcnt lgkmcnt(0)\n\ts_barrier" ::: "memory"); SB(); } while (0)

#define STAMP(idx)

DI unsigned cvtpk(float lo, float hi) {
    f32x2 v = {lo, hi};
    bf16x2_t b = __builtin_convertvector(v, bf16x2_t);
    return __builtin_bit_cast(unsigned, b);
}
DI bf16x8 pack8(const f32x16& x, int s) {
    u32x4 w = {cvtpk(x[8 * s + 0], x[8 * s + 1]), cvtpk(x[8 * s + 2], x[8 * s + 3]),
               cvtpk(x[8 * s + 4], x[8 * s + 5]), cvtpk(x[8 * s + 6], x[8 * s + 7])};
    return __builtin_bit_cast(bf16x8, w);
}
template <int NT> DI void dma4(unsigned lds, unsigned v0, unsigned v1, unsigned v2, unsigned v3, rsrc_t srd, unsigned soff) {
    if (NT) asm volatile("s_mov_b32 m0, %0\n\ts_nop 3\n\tbuffer_load_dwordx4 %1, %5, %6 offen nt lds\n\tbuffer_load_dwordx4 %2, %5, %6 offen offset:1024 nt lds\n\t"
                         "buffer_load_dwordx4 %3, %5, %6 offen offset:2048 nt lds\n\tbuffer_load_dwordx4 %4, %5, %6 offen offset:3072 nt lds"
                         :: "s"(lds), "v"(v0), "v"(v1), "v"(v2), "v"(v3), "s"(srd), "s"(soff) : "memory");
    else asm volatile("s_mov_b32 m0, %0\n\ts_nop 3\n\tbuffer_load_dwordx4 %1, %5, %6 offen lds\n\tbuffer_load_dwordx4 %2, %5, %6 offen offset:1024 lds\n\t"
                      "buffer_load_dwordx4 %3, %5, %6 offen offset:2048 lds\n\tbuffer_load_dwordx4 %4, %5, %6 offen offset:3072 lds"
                      :: "s"(lds), "v"(v0), "v"(v1), "v"(v2), "v"(v3), "s"(srd), "s"(soff) : "memory");
}
template <int NT> DI void dma1(unsigned lds, unsigned voff, rsrc_t srd, unsigned soff) {
    if (NT) asm volatile("s_mov_b32 m0, %0\n\ts_nop 3\n\tbuffer_load_dwordx4 %1, %2, %3 offen nt lds" :: "s"(lds), "v"(voff), "s"(srd), "s"(soff) : "memory");
    else asm volatile("s_mov_b32 m0, %0\n\ts_nop 3\n\tbuffer_load_dwordx4 %1, %2, %3 offen lds" :: "s"(lds), "v"(voff), "s"(srd), "s"(soff) : "memory");
}
DI void ld4_sc1(bf16x8* d, unsigned voff, rsrc_t srd, unsigned soff) {
    asm volatile("s_nop 4\n\tbuffer_load_dwordx4 %0, %4, %5, %6 offen sc1\n\tbuffer_load_dwordx4 %1, %4, %5, %6 offen offset:1024 sc1\n\t"
                 "buffer_load_dwordx4 %2, %4, %5, %6 offen offset:2048 sc1\n\tbuffer_load_dwordx4 %3, %4, %5, %6 offen offset:3072 sc1"
                 : "+v"(d[0]), "+v"(d[1]), "+v"(d[2]), "+v"(d[3]) : "v"(voff), "s"(srd), "s"(soff) : "memory");
}
DI void ld4_nt(bf16x8* d, unsigned v0, unsigned v1, unsigned v2, unsigned v3, rsrc_t srd, unsigned soff) {
    asm volatile("s_nop 4\n\tbuffer_load_dwordx4 %0, %4, %8, %9 offen nt\n\tbuffer_load_dwordx4 %1, %5, %8, %9 offen offset:1024 nt\n\t"
                 "buffer_load_dwordx4 %2, %6, %8, %9 offen offset:2048 nt\n\tbuffer_load_dwordx4 %3, %7, %8, %9 offen offset:3072 nt"
                 : "+v"(d[0]), "+v"(d[1]), "+v"(d[2]), "+v"(d[3]) : "v"(v0), "v"(v1), "v"(v2), "v"(v3), "s"(srd), "s"(soff) : "memory");
}
constexpr unsigned BIAS = 4096;
#define WAITV_F(n, f) asm volatile("s_waitcnt vmcnt(" #n ")" : "+v"((f)[0]), "+v"((f)[1]), "+v"((f)[2]), "+v"((f)[3]) :: "memory")
DI bf16x8 lds128(lptr p) { return *(const LAS bf16x8*)p; }
DI s16x4 ldstr(lptr p) {
    typedef short v4i16_t __attribute__((ext_vector_type(4)));
    return __builtin_amdgcn_ds_read_tr16_b64_v4i16((LAS v4i16_t*)p);
}
DI bf16x8 cat(s16x4 a, s16x4 b) { return __builtin_shufflevector(a, b, 0, 1, 2, 3, 4, 5, 6, 7); }
DI float swapmax(float v) {
    auto r = __builtin_amdgcn_permlane32_swap(__float_as_uint(v), __float_as_uint(v), false, false);
    return fmaxf(__uint_as_float(r[0]), __uint_as_float(r[1]));
}
DI float swapsum(float v) {
    auto r = __builtin_amdgcn_permlane32_swap(__float_as_uint(v), __float_as_uint(v), false, false);
    return __uint_as_float(r[0]) + __uint_as_float(r[1]);
}
DI rsrc_t mkrsrc(const void* p, unsigned bytes) {
    return __builtin_amdgcn_make_buffer_rsrc((void*)p, (short)0, (int)bytes, 0x00020000);
}
DI void zero16(f32x16& v) {
#pragma unroll
    for (int r = 0; r < 16; ++r) v[r] = 0.f;
}
DI void wait_flags(const GAS u64* p, u64 tag) {
#pragma unroll 1
    for (unsigned it = 0; it < SPIN_CAP; ++it) {
        const u64 v = __hip_atomic_load(p, __ATOMIC_RELAXED, __HIP_MEMORY_SCOPE_AGENT);
        if (__all(v == tag)) break;
        __builtin_amdgcn_s_sleep(1);
    }
    asm volatile("" ::: "memory");
}

DI float max3f(float a, float b, float c) { float r; asm("v_max3_f32 %0, %1, %2, %3" : "=v"(r) : "v"(a), "v"(b), "v"(c)); return r; }
DI float fadd_s(float a, float b) { float r; asm("v_add_f32_e32 %0, %1, %2" : "=v"(r) : "v"(a), "v"(b)); return r; }
DI float fmul_s(float a, float b) { float r; asm("v_mul_f32_e32 %0, %1, %2" : "=v"(r) : "v"(a), "v"(b)); return r; }
DI float fsub_s(float a, float b) { float r; asm("v_sub_f32_e32 %0, %1, %2" : "=v"(r) : "v"(a), "v"(b)); return r; }
DI float exp_s(float a) { float r; asm("v_exp_f32_e32 %0, %1" : "=v"(r) : "v"(a)); return r; }
#define MPAD4(a) asm volatile("s_nop 7\n\ts_nop 7" : "+v"((a)[0]), "+v"((a)[1]), "+v"((a)[2]), "+v"((a)[3]))
#define PIN(x) asm volatile("" : "+v"(x))
#define PIN4(f) asm volatile("" : "+v"((f)[0]), "+v"((f)[1]), "+v"((f)[2]), "+v"((f)[3]) :: "memory")
DI lptr opaque(lptr p) { unsigned a = (unsigned)(uintptr_t)p; asm volatile("" : "+v"(a)); return (lptr)(uintptr_t)a; }
#define LA_ 6
constexpr int LA = LA_;
#define SM_THR 8.0f

struct QBase { lptr x0, x1, w; };
DI void qload(int i, bf16x8* wf, bf16x8* xf, const QBase& q) {
    const int s = i >> 2, d = i & 3;
    if (d == 0) xf[s] = lds128(((s & 1) ? q.x1 : q.x0) + 512 * (s >> 1));
    wf[i & 7] = cat(ldstr(q.w + 4096 * s + 512 * d), ldstr(q.w + 4096 * s + 512 * d + 256));
}
DI void qpre(bf16x8* wf, bf16x8* xf, const QBase& q) {
#pragma unroll
    for (int i = 0; i < LA; ++i) qload(i, wf, xf, q);
}
template <bool NEXT> DI void qsteps(f32x16* acc, bf16x8* wf, bf16x8* xf, const QBase& q, const QBase& qn) {
#pragma unroll
    for (int i = 0; i < 16; ++i) {
        SB();
        acc[i & 3] = MFMA32(wf[i & 7], xf[i >> 2], acc[i & 3]);
        PIN(acc[i & 3]);
        SB();
        if (NEXT && i + LA == 16) BARX();
        if (i + LA < 16) qload(i + LA, wf, xf, q); else if (NEXT) qload(i + LA - 16, wf, xf, qn);
    }
    SB();
}
template <bool FIRST = false> DI void oblock(f32x16* acc, const bf16x8* of, lptr wp_) {
    const lptr wp = opaque(wp_);
    bf16x8 wf[8];
    auto load = [&](int i) { wf[i & 7] = cat(ldstr(wp + 5120 * (i / 5) + 512 * (i % 5)), ldstr(wp + 5120 * (i / 5) + 2560 + 512 * (i % 5))); };
#pragma unroll
    for (int i = 0; i < LA; ++i) load(i);
#pragma unroll
    for (int i = 0; i < 20; ++i) {
        SB();
        if (FIRST && i < 5) { f32x16 z; zero16(z); acc[i] = MFMA32(wf[i & 7], of[0], z); }
        else acc[i % 5] = MFMA32(wf[i & 7], of[i / 5], acc[i % 5]);
        PIN(acc[i % 5]);
        SB();
        if (i + LA < 20) load(i + LA);
    }
    SB();
}
DI void aload(int i, bf16x8* fr, lptr vp, lptr kp0, lptr kp1) {
    if (i < 16) fr[i & 7] = cat(ldstr(vp + 4096 * (i >> 2) + 512 * (i & 3)), ldstr(vp + 4096 * (i >> 2) + 2048 + 512 * (i & 3)));
    else { const int s = i & 7; fr[i & 7] = lds128(((s & 1) ? kp1 : kp0) + 512 * (s >> 1) + 8192 * ((i - 16) >> 3)); }
}

__global__ __launch_bounds__(512, 2) void fused_kernel(
    const char* xA, const char* kA, const char* vA, const char* wqA, const char* woA,
    const char* xB, const char* kB, const char* vB, const char* wqB, const char* woB,
    char* ws, char* out)
{
    extern __shared__ __attribute__((aligned(16))) char smem_[];
    const lptr smem = (lptr)smem_;
    const unsigned lds0 = (unsigned)(uintptr_t)smem;
    const int tid = threadIdx.x, lane = tid & 63;
    const int wid = __builtin_amdgcn_readfirstlane(tid >> 6);
    const int bid = blockIdx.x;
    const int xg = bid & 7, ii = bid >> 3;
    const int br = xg >> 2, xq = xg & 3, b = ii >> 2, jj = ii & 3;
    const int h1 = 8 * xq + 2 * jj;
    const char* x = br ? xB : xA;
    const char* kc = br ? kB : kA;
    const char* vc = br ? vB : vA;
    const char* wq = br ? wqB : wqA;
    const char* wo = br ? woB : woA;
    const int qb = wid & 3, grp = wid >> 2, lw = wid & 3;
    const int l31 = lane & 31, h = lane >> 5;
    STAMP(0);
    const char* codebase;
    asm volatile("s_getpc_b64 %0" : "=s"(codebase));

    u64 gen;
    {
        const char* gp = ws + WS_GEN + (br * NB + b) * 256;
        asm volatile("global_load_dwordx2 %0, %1, off sc1" : "=v"(gen) : "v"(gp) : "memory");
    }

    const rsrc_t sk = mkrsrc(kc + (size_t)(b * NH + h1) * SK * DH * 2 - BIAS, 2 * SK * DH * 2 + BIAS);
    const rsrc_t sv = mkrsrc(vc + (size_t)(b * NH + h1) * SK * DH * 2 - BIAS, 2 * SK * DH * 2 + BIAS);
    const rsrc_t sof = mkrsrc(ws + WS_OF + (size_t)((br * NB + b) * NH + 8 * xq) * 32768, 8 * 32768);

    const unsigned dsub = lane >> 5, dr = (lane >> 2) & 7, dsl = lane & 3;
    const unsigned xr0 = 16u * (dsl ^ ((dr >> 2) & 3)), xr1 = 16u * (dsl ^ ((2 + (dr >> 2)) & 3));
    const unsigned vk0 = BIAS + 4096 * lw + dr * 256 + dsub * 64 + xr0, vk2 = BIAS + 4096 * lw + dr * 256 + dsub * 64 + xr1;
    const unsigned vv0 = BIAS + 4096 * lw + dr * 256 + dsub * 64 + 16 * dsl;
    const unsigned ldw = lds0 + 4096 * lw;

    auto issueK = [&](int t, unsigned pair) {
        if ((POL & 1) == ((POL >> 2) & 1) ? (POL & 1) : ((POL >> (2 * br)) & 1)) dma4<1>(ldw + pair, vk0, vk0 - 896, vk2, vk2 - 896, sk, (unsigned)t * 16384u);
        else dma4<0>(ldw + pair, vk0, vk0 - 896, vk2, vk2 - 896, sk, (unsigned)t * 16384u); };
    auto issueV = [&](int t, unsigned pair) {
        if (((POL >> 1) & 1) == ((POL >> 3) & 1) ? ((POL >> 1) & 1) : ((POL >> (2 * br + 1)) & 1)) dma4<1>(ldw + pair + SLOT, vv0, vv0 - 896, vv0, vv0 - 896, sv, (unsigned)t * 16384u);
        else dma4<0>(ldw + pair + SLOT, vv0, vv0 - 896, vv0, vv0 - 896, sv, (unsigned)t * 16384u); };
    auto nextpair = [](unsigned p) { return (p == 4u * SLOT) ? 0u : p + 2u * SLOT; };

    const int G1 = (lane >> 4) & 1, tq = (lane & 15) >> 2, tp = lane & 3;
    const int sig = ((tp & 1) << 1) | (tp >> 1);
    const unsigned wqb = SLOT + 2048 * h + 64 * tq + 32 * G1 + 8 * sig;
    const unsigned rowq = 32 * qb + l31;
    const unsigned xsw = (rowq >> 2) & 3;
    const unsigned xb0 = 1024 * (rowq >> 3) + 64 * (rowq & 7) + 16 * ((0 + h) ^ xsw);
    const unsigned xb1 = 1024 * (rowq >> 3) + 64 * (rowq & 7) + 16 * ((2 + h) ^ xsw);
    const unsigned ksw = (l31 >> 2) & 3;
    const unsigned kb0 = 2048 * (l31 >> 3) + 64 * (l31 & 7) + 16 * ((0 + h) ^ ksw);
    const unsigned kb1 = 2048 * (l31 >> 3) + 64 * (l31 & 7) + 16 * ((2 + h) ^ ksw);
    const unsigned trb = 64 * (4 * h + tq) + 32 * G1 + 8 * tp;
    const unsigned vo16 = lane * 16;

    auto flagp = [&](int bb, int stage, int w) { return (GAS u64*)(ws + WS_FLAG + (size_t)((bb * 3 + stage) * 8 + w) * 64); };
    auto partner = [&](int dj) { return (bid & ~24) | (((jj + dj) & 3) << 3); };
    auto hl1 = [&](int dj) { return 2 * ((jj + dj) & 3); };

    f32x16 acc[5];
    bf16x8 qf[8], pf[4], fr[8];
    f32x16 s0, s1;
#pragma unroll
    for (int d = 0; d < 4; ++d) zero16(acc[d]);

    auto nextstage = [](unsigned st) { return st == 8u * SLOT ? 4u * SLOT : (st == 4u * SLOT ? 6u * SLOT : (st == 6u * SLOT ? 2u * SLOT : 8u * SLOT)); };
    auto qbase = [&](unsigned st) { return QBase{opaque(smem + st + xb0), opaque(smem + st + xb1), opaque(smem + st + wqb)}; };
    if (grp == 0) {
        unsigned st = 8 * SLOT;
        unsigned dummy = 0;
        {
            const unsigned line = lw * 64 + lane;
            const char* p = codebase + (line < CODE_LINES ? line : 0) * 128;
            asm volatile("global_load_dword %0, %1, off" : "+v"(dummy) : "v"(p) : "memory");
        }
        BARX();
        QBase qc = qbase(st);
        qpre(fr, pf, qc);
#pragma unroll 1
        for (int i = 0; i < 10; ++i) {
            st = nextstage(st);
            const QBase qn = qbase(st);
            qsteps<true>(acc, fr, pf, qc, qn);
            qc = qn;
            if (__builtin_expect(i == WARMA_AT, 0)) {
                const int idx = lw * 64 + lane;
                const char* p1 = wq + (size_t)(80 * b + (idx < 160 ? idx >> 1 : 0)) * 8192 + (h1 + 1) * 256 + (idx & 1) * 128;
                asm volatile("global_load_dword %0, %1, off" : "+v"(dummy) : "v"(p1) : "memory");
                if (WARMA >= 2) {
                    const char* p2 = wo + (size_t)(8 * xq * 128) * 1280 + (size_t)(ii * 320 + idx) * 128;
                    const char* p3 = wo + (size_t)(8 * xq * 128) * 1280 + (size_t)(ii * 320 + 256 + (idx & 63)) * 128;
                    asm volatile("global_load_dword %0, %1, off" : "+v"(dummy) : "v"(p2) : "memory");
                    if (lw == 0) asm volatile("global_load_dword %0, %1, off" : "+v"(dummy) : "v"(p3) : "memory");
                }
            }
        }
        WAITV(0);
        PIN(dummy);
    } else {
        const rsrc_t sx = mkrsrc(x + (size_t)b * SQ * DM * 2, SQ * DM * 2);
        const rsrc_t sw = mkrsrc(wq + (size_t)h1 * 256 - BIAS, DM * 8192 - h1 * 256 + BIAS);
        const unsigned vx0 = (32 * lw + dr) * 1280 + dsub * 64 + xr0, vx1 = (32 * lw + 8 + dr) * 1280 + dsub * 64 + xr1 - 1024;
        const unsigned vw0 = BIAS + (16 * lw + dr) * 8192 + dsub * 64 + 16 * dsl;
        const int rot = ROT ? b + 3 * jj : 0;
        constexpr unsigned T0 = 6 * SLOT, T1 = 8 * SLOT, P1 = 2 * SLOT, P2 = 4 * SLOT;
        int ci = rot >= 10 ? rot - 10 : rot;
        auto issueQn = [&](int hd, unsigned st) {
            dma4<NT_W>(ldw + st + SLOT, vw0, vw0 - 896, vw0 + 65536 - 2048, vw0 + 65536 - 2944, sw, (unsigned)ci * 524288u + 256 * hd);
            dma4<NT_X>(ldw + st, vx0, vx1, vx0 + 20480 - 2048, vx1 + 20480 - 2048, sx, 128 * ci);
            ci = ci == 9 ? 0 : ci + 1;
        };
        issueQn(0, T1);
        WAITV(0); BARX();
        issueQn(0, P2); issueQn(0, T0);
        WAITV(8);  BARX(); issueQn(0, P1);
        WAITV(8);  BARX(); issueQn(0, T1);
        WAITV(8);  BARX(); issueQn(0, P2);
        WAITV(8);  BARX(); issueQn(0, T0); issueK(0, 0); issueV(0, 0);
        WAITV(16); BARX(); issueQn(0, P1);
        bf16x8 r0[8], r1[8];
#pragma unroll
        for (int i = 0; i < 8; ++i) { r0[i] = fr[0]; r1[i] = fr[0]; }
        auto ldkv = [&](bf16x8* r, int tv) {
            ld4_nt(r, vv0, vv0 - 896, vv0, vv0 - 896, sv, (unsigned)tv * 16384u);
            ld4_nt(r + 4, vk0, vk0 - 896, vk2, vk2 - 896, sk, (unsigned)(tv + 1) * 16384u);
        };
        auto putkv = [&](bf16x8* r, unsigned pairv, unsigned pairk) {
            PIN4(r); PIN4(r + 4);
            const lptr dv = smem + pairv + SLOT + 4096 * lw + vo16, dk = smem + pairk + 4096 * lw + vo16;
#pragma unroll
            for (int j = 0; j < 4; ++j) { *(LAS bf16x8*)(dv + 1024 * j) = r[j]; *(LAS bf16x8*)(dk + 1024 * j) = r[4 + j]; }
            asm volatile("s_waitcnt lgkmcnt(0)" ::: "memory");
        };
        WAITV(16); BARX(); issueQn(0, T1); ldkv(r0, 2);
        WAITV(16); BARX(); issueQn(0, P2); ldkv(r1, 3);
        WAITV(24); BARX(); issueQn(1, T0);
        WAITV(16); BARX(); issueK(1, P1); issueV(1, P1);
        BARX(); issueQn(1, T1);
        const QBase q0 = qbase(T0), q1 = qbase(T1);
        WAITV(16); BARX();
        qpre(fr, pf, q0);
        issueK(2, P2);
        qsteps<false>(acc, fr, pf, q0, q0);
        WAITV(4);
        unsigned pv = P2;
#pragma unroll 1
        for (int c = 1; c < 9; c += 2) {
            unsigned pk = nextpair(pv);
            const int tn = c < 7 ? c + 3 : 4000;
            BARX();
            issueQn(1, T0);
            putkv(r0, pv, pk);
            ldkv(r0, tn);
            qpre(fr, pf, q1);
            qsteps<false>(acc, fr, pf, q1, q1);
            pv = nextpair(pk);
            WAITV(8);
            BARX();
            issueQn(1, T1);
            putkv(r1, pk, pv);
            ldkv(r1, tn + 1);
            qpre(fr, pf, q0);
            qsteps<false>(acc, fr, pf, q0, q0);
            WAITV(8);
        }
        WAITV(0);
        PIN4(r0); PIN4(r0 + 4); PIN4(r1); PIN4(r1 + 4);
        BARX();
        qpre(fr, pf, q1);
        qsteps<false>(acc, fr, pf, q1, q1);
    }
    STAMP(1);
    asm volatile("" : "+v"(gen));
    const u64 tag = gen + 1;

    __builtin_amdgcn_s_setprio(PPRIO);
    MPAD4(acc);
    {
        float cexp = CEXP; PIN(cexp);
#pragma unroll
        for (int d = 0; d < 4; ++d) {
#pragma unroll
            for (int r = 0; r < 16; ++r) acc[d][r] = fmul_s(acc[d][r], cexp);
            qf[2 * d] = pack8(acc[d], 0); qf[2 * d + 1] = pack8(acc[d], 1); zero16(acc[d]);
        }
    }
    if (grp == 0) BARX();
    float la = 0.f, lb = 0.f, lc = 0.f, ld = 0.f;
    {
        f32x16 negm, p0; zero16(negm); zero16(s1); zero16(p0);
        unsigned pk = grp ? 4 * SLOT : 0;
        lptr kp0 = opaque(smem + pk + kb0), kp1 = opaque(smem + pk + kb1);
#pragma unroll
        for (int i = 16; i < 16 + LA; ++i) aload(i, fr, kp0, kp0, kp1);
        const GAS u64* fp = flagp(partner(1 + (lane % 3)), 0, qb);
        u64 fv = 0;
        const int tw = grp ? 3 : -1;
        float thr = -__builtin_inff(), dmin = -__builtin_inff();
        u32x4 pw[4];
#pragma unroll 1
        for (int t = 0; t < 8; ++t) {
            if (__builtin_expect(t == tw, 0)) asm volatile("global_load_dwordx2 %0, %1, off sc1" : "+v"(fv) : "v"(fp) : "memory");
            float c0, c1;
#pragma unroll
            for (int j = 0; j < 16; ++j) {
                const int i = 16 + j;
                SB();
                if (j < 8) s0 = MFMA32(fr[i & 7], qf[j], j == 0 ? negm : s0); else s1 = MFMA32(fr[i & 7], qf[j - 8], j == 8 ? negm : s1);
                SB();
                if (i + LA < 32) aload(i + LA, fr, kp0, kp0, kp1);
                if (j < 8)
                    asm volatile("v_add_f32_e32 %0, %0, %4\n\tv_add_f32_e32 %1, %1, %5\n\tv_add_f32_e32 %2, %2, %6\n\tv_add_f32_e32 %3, %3, %7"
                                 : "+v"(la), "+v"(lb), "+v"(lc), "+v"(ld) : "v"(s1[2 * j]), "v"(s1[2 * j + 1]), "v"(p0[2 * j]), "v"(p0[2 * j + 1]));
                if (j >= 10) {
                    const int e = 2 * (j - 10);
                    float e0, e1; unsigned pk_;
                    if (j == 10)
                        asm volatile("v_exp_f32_e32 %0, %5\n\tv_exp_f32_e32 %1, %6\n\tv_max3_f32 %3, %5, %6, %7\n\tv_max3_f32 %4, %8, %9, %10\n\tv_cvt_pk_bf16_f32 %2, %0, %1"
                                     : "=&v"(e0), "=&v"(e1), "=&v"(pk_), "=&v"(c0), "=&v"(c1) : "v"(s0[0]), "v"(s0[1]), "v"(s0[12]), "v"(s0[13]), "v"(s0[14]), "v"(s0[15]));
                    else if (j & 1)
                        asm volatile("v_exp_f32_e32 %0, %4\n\tv_exp_f32_e32 %1, %5\n\tv_max3_f32 %3, %3, %4, %5\n\tv_cvt_pk_bf16_f32 %2, %0, %1"
                                     : "=&v"(e0), "=&v"(e1), "=&v"(pk_), "+v"(c1) : "v"(s0[e]), "v"(s0[e + 1]));
                    else
                        asm volatile("v_exp_f32_e32 %0, %4\n\tv_exp_f32_e32 %1, %5\n\tv_max3_f32 %3, %3, %4, %5\n\tv_cvt_pk_bf16_f32 %2, %0, %1"
                                     : "=&v"(e0), "=&v"(e1), "=&v"(pk_), "+v"(c0) : "v"(s0[e]), "v"(s0[e + 1]));
                    p0[e] = e0; p0[e + 1] = e1;
                    pw[e >> 3][(e >> 1) & 3] = pk_;
                }
            }
            WAITV(0);
            if (__builtin_expect(t == tw, 0)) { PIN(fv); if (!__all(fv == tag)) wait_flags(fp, tag); }
            STAMP(2 + 2 * t);
            BARX();
            const lptr vp = opaque(smem + pk + SLOT + trb);
            pk = nextpair(pk);
            kp0 = opaque(smem + pk + kb0); kp1 = opaque(smem + pk + kb1);
#pragma unroll
            for (int i = 0; i < LA; ++i) aload(i, fr, vp, vp, vp);
            PIN(s1);
            SB();
            {
                float c2, c3;
                asm volatile("v_max3_f32 %2, %4, %5, %6\n\tv_max3_f32 %3, %7, %8, %9\n\tv_max3_f32 %0, %0, %10, %11\n\tv_max3_f32 %1, %1, %12, %13\n\t"
                             "v_max3_f32 %2, %2, %14, %15\n\tv_max3_f32 %3, %3, %16, %17\n\tv_max3_f32 %0, %0, %18, %19\n\tv_max3_f32 %1, %1, %2, %3\n\tv_max_f32_e32 %0, %0, %1"
                             : "+v"(c0), "+v"(c1), "=&v"(c2), "=&v"(c3)
                             : "v"(s1[0]), "v"(s1[1]), "v"(s1[2]), "v"(s1[3]), "v"(s1[4]), "v"(s1[5]), "v"(s1[6]), "v"(s1[7]),
                               "v"(s1[8]), "v"(s1[9]), "v"(s1[10]), "v"(s1[11]), "v"(s1[12]), "v"(s1[13]), "v"(s1[14]), "v"(s1[15]));
                auto r_ = __builtin_amdgcn_permlane32_swap(__float_as_uint(c0), __float_as_uint(c0), false, false);
                const float mx = max3f(__uint_as_float(r_[0]), __uint_as_float(r_[1]), __uint_as_float(r_[1]));
                if (__builtin_expect(__any(!(mx <= thr)), 0)) {
                    const float d = fmaxf(mx, dmin);
                    float f = __builtin_amdgcn_exp2f(fminf(-d, 0.f));
                    asm volatile("s_nop 1" : "+v"(f));
#pragma unroll
                    for (int r = 0; r < 16; ++r) { s0[r] = fsub_s(s0[r], d); s1[r] = fsub_s(s1[r], d); negm[r] = fsub_s(negm[r], d); }
                    la = fmul_s(la, f); lb = fmul_s(lb, f); lc = fmul_s(lc, f); ld = fmul_s(ld, f);
#pragma unroll
                    for (int dd = 0; dd < 4; ++dd)
#pragma unroll
                        for (int r = 0; r < 16; ++r) acc[dd][r] = fmul_s(acc[dd][r], f);
#pragma unroll
                    for (int e = 0; e < 12; ++e) p0[e] = __builtin_amdgcn_exp2f(s0[e]);
#pragma unroll
                    for (int e = 0; e < 12; e += 2) pw[e >> 3][(e >> 1) & 3] = cvtpk(p0[e], p0[e + 1]);
                }
                thr = SM_THR; dmin = 0.f;
            }
#pragma unroll
            for (int i = 0; i < 16; ++i) {
                SB();
                acc[i & 3] = MFMA32(fr[i & 7], __builtin_bit_cast(bf16x8, pw[i >> 2]), acc[i & 3]);
                SB();
                constexpr int pairs[16] = {6, 7, 8, 9, 10, 11, -1, 12, 13, 14, 15, -1, -1, -1, -1, -1};
                const int q = pairs[i];
                if (q >= 0) {
                    if (q < 8) { p0[2 * q] = __builtin_amdgcn_exp2f(s0[2 * q]); p0[2 * q + 1] = __builtin_amdgcn_exp2f(s0[2 * q + 1]); PIN(p0); }
                    else { s1[2 * q - 16] = __builtin_amdgcn_exp2f(s1[2 * q - 16]); s1[2 * q - 15] = __builtin_amdgcn_exp2f(s1[2 * q - 15]); PIN(s1); }
                }
                aload(i + LA, fr, vp, kp0, kp1);
                if (q >= 0) {
                    pw[q >> 2][q & 3] = q < 8 ? cvtpk(p0[2 * q], p0[2 * q + 1]) : cvtpk(s1[2 * q - 16], s1[2 * q - 15]);
                    PIN(pw[q >> 2][q & 3]);
                }
            }
            SB();
            STAMP(3 + 2 * t);
        }
#pragma unroll
        for (int j = 0; j < 8; ++j) { la = fadd_s(la, s1[2 * j]); lb = fadd_s(lb, s1[2 * j + 1]); lc = fadd_s(lc, p0[2 * j]); ld = fadd_s(ld, p0[2 * j + 1]); }
    }
    if (grp == 0) { __builtin_amdgcn_s_setprio(0); BARX(); }
    STAMP(18);

    {
        float inv = __builtin_amdgcn_rcpf(swapsum((la + lb) + (lc + ld)));
        asm volatile("s_nop 1" : "+v"(inv));
        MPAD4(acc);
#pragma unroll
        for (int d = 0; d < 4; ++d) {
#pragma unroll
            for (int r = 0; r < 16; ++r) acc[d][r] = fmul_s(acc[d][r], inv);
            qf[2 * d] = pack8(acc[d], 0); qf[2 * d + 1] = pack8(acc[d], 1);
            __builtin_amdgcn_raw_buffer_store_b128(__builtin_bit_cast(u32x4, qf[2 * d]), sof, vo16, (2 * jj + grp) * 32768 + qb * 8192 + d * 2048, 16);
            __builtin_amdgcn_raw_buffer_store_b128(__builtin_bit_cast(u32x4, qf[2 * d + 1]), sof, vo16, (2 * jj + grp) * 32768 + qb * 8192 + d * 2048 + 1024, 16);
            SB();
        }
    }
    const rsrc_t swo = mkrsrc(wo + (size_t)(8 * xq * 128) * 1280 + 320 * jj - BIAS, 1024 * 1280 - 320 * jj + BIAS);
    const unsigned vwA = BIAS + (16 * lw + dr) * 1280 + dsub * 64 + 16 * dsl, vwB = BIAS + (16 * lw + dr) * 1280 + 16 * dsl + (dsub ? 8 * 1280 : 256) - 2048;
    auto issueW = [&](int hl, int half, int buf) {
        const unsigned so = (unsigned)(hl * 128 + 64 * half) * 1280u, ld = lds0 + buf * WT + 5 * lw * 1024;
        dma4<NT_WO>(ld, vwA, vwA - 896, vwB, vwA + 10240 + 64 - 3072, swo, so);
        dma1<NT_WO>(ld + 4096, vwA, swo, so + 10240 + 192);
    };
    const lptr x1 = smem + (lw < 2 ? lw * 8192 : 7 * WT + (lw - 2) * 8192) + vo16;
    if (grp) {
#pragma unroll
        for (int f = 0; f < 8; ++f) *(LAS bf16x8*)(x1 + 1024 * f) = qf[f];
        __builtin_amdgcn_s_setprio(0);
        BAR();
        BARX();
        WAITV(0);
        if (lane == 0) __hip_atomic_store(flagp(bid, 1, qb), tag, __ATOMIC_RELAXED, __HIP_MEMORY_SCOPE_AGENT);
        STAMP(19);
        issueW(hl1(3) + 1, 0, 0); issueW(hl1(3) + 1, 1, 7);
        wait_flags(flagp(partner(1 + (lane % 3)), 1, qb), tag);
        WAITV(0);
        STAMP(21);
        BARX();
    } else {
        bf16x8 cur[4], nxt[4];
#pragma unroll
        for (int k = 0; k < 4; ++k) { cur[k] = qf[k]; nxt[k] = qf[4 + k]; }
        auto issueO = [&](bf16x8* f, int blk) { ld4_sc1(f, vo16, sof, hl1(blk >> 1) * 32768 + qb * 8192 + 4096 * (blk & 1)); };
        issueW(hl1(0), 0, 5);
        BARX();
        issueW(hl1(0), 1, 6); issueW(hl1(1), 0, 7);
        issueV(10, 2 * SLOT); issueK(11, 4 * SLOT);
        BARX();
        issueV(11, 4 * SLOT); issueK(12, 0);
        WAITV(8);
        if (lane == 0) __hip_atomic_store(flagp(bid, 0, qb), tag, __ATOMIC_RELAXED, __HIP_MEMORY_SCOPE_AGENT);
        BARX();
        oblock<true>(acc, cur, smem + 5 * WT + trb);
        issueV(12, 0); issueK(13, 2 * SLOT);
        WAITV(8);
        BARX();
        issueO(cur, 2);
        issueW(hl1(1), 1, 5);
        oblock(acc, nxt, smem + 6 * WT + trb);
        issueO(nxt, 3);
        issueV(13, 2 * SLOT); issueK(14, 4 * SLOT);
        WAITV(17);
        PIN4(cur);
        oblock(acc, cur, smem + 7 * WT + trb);
        issueO(cur, 4);
        WAITV(12);
        BARX();
        unsigned wdummy = 0;
        {
            const int r = 64 + 40 * b + (lane < 40 ? lane : 0);
            const char* p = wo + (size_t)((8 * xq + hl1(1 + (r >> 7)) + 1) * 128 + (r & 127)) * 1280 + 320 * jj + (lw == 3 ? 316 : 128 * lw);
            asm volatile("global_load_dword %0, %1, off" : "+v"(wdummy) : "v"(p) : "memory");
        }
        issueW(hl1(2), 0, 6); issueW(hl1(2), 1, 7);
        PIN4(nxt);
        oblock(acc, nxt, smem + 5 * WT + trb);
        issueO(nxt, 5);
        issueV(14, 4 * SLOT); issueK(15, 0);
        WAITV(17);
        PIN(wdummy);
        BARX();
        issueW(hl1(3), 0, 5);
        PIN4(cur);
        oblock(acc, cur, smem + 6 * WT + trb);
        issueO(cur, 6);
        issueV(15, 0);
        WAITV(13);
        BARX();
        issueW(hl1(3), 1, 6); issueW(hl1(0) + 1, 0, 2); issueW(hl1(0) + 1, 1, 3);
        PIN4(nxt);
        oblock(acc, nxt, smem + 7 * WT + trb);
        issueO(nxt, 7);
        WAITV(4);
        BARX();
        issueW(hl1(1) + 1, 0, 4);
        PIN4(cur);
        oblock(acc, cur, smem + 5 * WT + trb);
        WAITV(5);
        PIN4(nxt);
        oblock(acc, nxt, smem + 6 * WT + trb);
        WAITV(0);
        BARX();
        bf16x8 of2[24];
#pragma unroll
        for (int f = 0; f < 8; ++f) of2[f] = lds128(x1 + 1024 * f);
        BAR();
        issueW(hl1(1) + 1, 1, 5); issueW(hl1(2) + 1, 0, 6); issueW(hl1(2) + 1, 1, 1);
        oblock(acc, of2, smem + 2 * WT + trb);
        oblock(acc, of2 + 4, smem + 3 * WT + trb);
        WAITV(0);
        STAMP(20);
        BARX();
        const unsigned ob = qb * 8192 + 32768;
        auto ldo = [&](int k0) {
#pragma unroll
            for (int k = k0; k < k0 + 4; ++k)
                of2[k] = __builtin_bit_cast(bf16x8, __builtin_amdgcn_raw_buffer_load_b128(sof, vo16, (unsigned)hl1(1 + (k >> 3)) * 32768 + ob + (k & 7) * 1024, 16));
        };
        ldo(0); ldo(4); SB();
        oblock(acc, of2, smem + 4 * WT + trb);       ldo(8);  ldo(12); SB();
        oblock(acc, of2 + 4, smem + 5 * WT + trb);   ldo(16); SB();
        oblock(acc, of2 + 8, smem + 6 * WT + trb);   ldo(20); SB();
        oblock(acc, of2 + 12, smem + 1 * WT + trb);
        oblock(acc, of2 + 16, smem + 0 * WT + trb);
        oblock(acc, of2 + 20, smem + 7 * WT + trb);
        STAMP(22);
        const rsrc_t spb = mkrsrc(ws + WS_PB, 256 * PB_WG);
        f32x4 mine[5];
#define UNIT(u, e) acc[(u) >> 2][4 * ((u) & 3) + (e)]
#define UPK(u) cvtpk(UNIT(u, 0), UNIT(u, 1)), cvtpk(UNIT(u, 2), UNIT(u, 3))
#pragma unroll
        for (int g = 0; g < 4; ++g) {
            if (g == xq) {
#pragma unroll
                for (int n = 0; n < 5; ++n) mine[n] = (f32x4){UNIT(5 * g + n, 0), UNIT(5 * g + n, 1), UNIT(5 * g + n, 2), UNIT(5 * g + n, 3)};
            } else {
                const unsigned so = (unsigned)((bid & ~3) | g) * PB_WG + (xq * 4 + qb) * 5120;
                const u32x4 w01 = {UPK(5 * g), UPK(5 * g + 1)}, w23 = {UPK(5 * g + 2), UPK(5 * g + 3)};
                const u32x2 w4 = {UPK(5 * g + 4)};
                __builtin_amdgcn_raw_buffer_store_b128(w01, spb, vo16, so, 16);
                __builtin_amdgcn_raw_buffer_store_b128(w23, spb, vo16, so + 1024, 16);
                __builtin_amdgcn_raw_buffer_store_b64(w4, spb, lane * 8, so + 2048, 16);
            }
        }
        WAITV(0);
        if (lane == 0) __hip_atomic_store(flagp(bid, 2, qb), tag, __ATOMIC_RELAXED, __HIP_MEMORY_SCOPE_AGENT);
        STAMP(23);
        wait_flags(flagp((bid & ~3) | ((xq + 1 + (lane % 3)) & 3), 2, qb), tag);
        STAMP(24);
        {
            const rsrc_t sout = mkrsrc(out + ((size_t)b * SQ * (2 * DM) + br * DM + 160 * jj + 40 * xq) * 2, SQ * 2 * DM * 2 - (br * DM + 160 * jj + 40 * xq) * 2);
            const unsigned vop = rowq * (4 * DM) + 8 * h;
            u32x4 a01[3], a23[3]; u32x2 a4[3];
#pragma unroll
            for (int s = 0; s < 3; ++s) {
                const unsigned so = (unsigned)bid * PB_WG + (((xq + 1 + s) & 3) * 4 + qb) * 5120;
                a01[s] = __builtin_amdgcn_raw_buffer_load_b128(spb, vo16, so, 16);
                a23[s] = __builtin_amdgcn_raw_buffer_load_b128(spb, vo16, so + 1024, 16);
                a4[s] = __builtin_amdgcn_raw_buffer_load_b64(spb, lane * 8, so + 2048, 16);
            }
            auto up = [](unsigned lo, unsigned hi) { return (f32x4){__uint_as_float(lo << 16), __uint_as_float(lo & 0xffff0000u), __uint_as_float(hi << 16), __uint_as_float(hi & 0xffff0000u)}; };
#pragma unroll
            for (int s = 0; s < 3; ++s) {
                mine[0] += up(a01[s][0], a01[s][1]); mine[1] += up(a01[s][2], a01[s][3]);
                mine[2] += up(a23[s][0], a23[s][1]); mine[3] += up(a23[s][2], a23[s][3]); mine[4] += up(a4[s][0], a4[s][1]);
            }
#pragma unroll
            for (int n = 0; n < 5; ++n) __builtin_amdgcn_raw_buffer_store_b64((u32x2){cvtpk(mine[n][0], mine[n][1]), cvtpk(mine[n][2], mine[n][3])}, sout, vop, 16 * n, OUT_AUX);
        }
    }
    {
        if (xq == 0 && jj == 0 && wid == 0 && lane == 0)
            __hip_atomic_store((GAS u64*)(ws + WS_GEN + (br * NB + b) * 256), tag, __ATOMIC_RELAXED, __HIP_MEMORY_SCOPE_AGENT);
        STAMP(25);
    }
}

extern "C" void kernel_launch(void* const* d_in, const int* in_sizes, int n_in, void* d_out, int out_size,
                              void* d_ws, size_t ws_size, hipStream_t stream) {
    (void)hipFuncSetAttribute((const void*)fused_kernel, hipFuncAttributeMaxDynamicSharedMemorySize, LDS_BYTES);
    hipLaunchKernelGGL(fused_kernel, dim3(256), dim3(512), LDS_BYTES, stream,
                       (const char*)d_in[0], (const char*)d_in[1], (const char*)d_in[2], (const char*)d_in[3], (const char*)d_in[4],
                       (const char*)d_in[5], (const char*)d_in[6], (const char*)d_in[7], (const char*)d_in[8], (const char*)d_in[9],
                       (char*)d_ws + WS_SHIFT, (char*)d_out);
}
```

```cpp
#include <hip/hip_runtime.h>
#include <hip/hip_bf16.h>
#include <stdint.h>

typedef short bf16x8 __attribute__((ext_vector_type(8)));
typedef short s16x4 __attribute__((ext_vector_type(4)));
typedef float f32x16 __attribute__((ext_vector_type(16)));
typedef float f32x4 __attribute__((ext_vector_type(4)));
typedef float f32x2 __attribute__((ext_vector_type(2)));
typedef __bf16 bf16x2_t __attribute__((ext_vector_type(2)));
typedef unsigned u32x4 __attribute__((ext_vector_type(4)));
typedef unsigned u32x2 __attribute__((ext_vector_type(2)));
typedef unsigned long long u64;
#define LAS __attribute__((address_space(3)))
#define GAS __attribute__((address_space(1)))
#define DI __device__ __forceinline__
typedef LAS char* lptr;
typedef __amdgpu_buffer_rsrc_t rsrc_t;

constexpr int NB = 8, SQ = 128, DM = 640, NH = 32, SK = 512, DH = 128;
constexpr float CEXP = 0.08838834764831845f * 1.4426950408889634f;
constexpr int SLOT = 16384;
constexpr int WT = 20480;
constexpr int LDS_BYTES = 10 * SLOT;
constexpr size_t WS_GEN = 0, WS_FLAG = 8192, WS_OF = 1 << 20;
constexpr size_t OF_BYTES = (size_t)2 * NB * NH * 32768;
constexpr size_t WS_PB = WS_OF + OF_BYTES;
constexpr unsigned PB_WG = 4 * 8 * 3 * 1024;
constexpr unsigned CODE_LINES = 180;
constexpr unsigned SPIN_CAP = 1u << 16;

#define MFMA32(a, b, c) __builtin_amdgcn_mfma_f32_32x32x16_bf16((a), (b), (c), 0, 0, 0)
#define WAITV(n) asm volatile("s_waitcnt vmcnt(" #n ")" ::: "memory")
#define SB() __builtin_amdgcn_sched_barrier(0)
#define BARX() do { SB(); asm volatile("s_barrier" ::: "memory"); SB(); } while (0)
#define BAR() do { SB(); asm volatile("s_waitcnt lgkmcnt(0)\n\ts_barrier" ::: "memory"); SB(); } while (0)


DI unsigned cvtpk(float lo, float hi) {
    f32x2 v = {lo, hi};
    bf16x2_t b = __builtin_convertvector(v, bf16x2_t);
    return __builtin_bit_cast(unsigned, b);
}
DI bf16x8 pack8(const f32x16& x, int s) {
    u32x4 w = {cvtpk(x[8 * s + 0], x[8 * s + 1]), cvtpk(x[8 * s + 2], x[8 * s + 3]),
               cvtpk(x[8 * s + 4], x[8 * s + 5]), cvtpk(x[8 * s + 6], x[8 * s + 7])};
    return __builtin_bit_cast(bf16x8, w);
}
template <int NT> DI void dma4(unsigned lds, unsigned v0, unsigned v1, unsigned v2, unsigned v3, rsrc_t srd, unsigned soff) {
    if (NT) asm volatile("s_mov_b32 m0, %0\n\ts_nop 3\n\tbuffer_load_dwordx4 %1, %5, %6 offen nt lds\n\tbuffer_load_dwordx4 %2, %5, %6 offen offset:1024 nt lds\n\t"
                         "buffer_load_dwordx4 %3, %5, %6 offen offset:2048 nt lds\n\tbuffer_load_dwordx4 %4, %5, %6 offen offset:3072 nt lds"
                         :: "s"(lds), "v"(v0), "v"(v1), "v"(v2), "v"(v3), "s"(srd), "s"(soff) : "memory");
    else asm volatile("s_mov_b32 m0, %0\n\ts_nop 3\n\tbuffer_load_dwordx4 %1, %5, %6 offen lds\n\tbuffer_load_dwordx4 %2, %5, %6 offen offset:1024 lds\n\t"
                      "buffer_load_dwordx4 %3, %5, %6 offen offset:2048 lds\n\tbuffer_load_dwordx4 %4, %5, %6 offen offset:3072 lds"
                      :: "s"(lds), "v"(v0), "v"(v1), "v"(v2), "v"(v3), "s"(srd), "s"(soff) : "memory");
}
template <int NT> DI void dma1(unsigned lds, unsigned voff, rsrc_t srd, unsigned soff) {
    if (NT) asm volatile("s_mov_b32 m0, %0\n\ts_nop 3\n\tbuffer_load_dwordx4 %1, %2, %3 offen nt lds" :: "s"(lds), "v"(voff), "s"(srd), "s"(soff) : "memory");
    else asm volatile("s_mov_b32 m0, %0\n\ts_nop 3\n\tbuffer_load_dwordx4 %1, %2, %3 offen lds" :: "s"(lds), "v"(voff), "s"(srd), "s"(soff) : "memory");
}
DI void ld4_sc1(bf16x8* d, unsigned voff, rsrc_t srd, unsigned soff) {
    asm volatile("s_nop 4\n\tbuffer_load_dwordx4 %0, %4, %5, %6 offen sc1\n\tbuffer_load_dwordx4 %1, %4, %5, %6 offen offset:1024 sc1\n\t"
                 "buffer_load_dwordx4 %2, %4, %5, %6 offen offset:2048 sc1\n\tbuffer_load_dwordx4 %3, %4, %5, %6 offen offset:3072 sc1"
                 : "+v"(d[0]), "+v"(d[1]), "+v"(d[2]), "+v"(d[3]) : "v"(voff), "s"(srd), "s"(soff) : "memory");
}
constexpr unsigned BIAS = 4096;
DI bf16x8 lds128(lptr p) { return *(const LAS bf16x8*)p; }
DI s16x4 ldstr(lptr p) {
    typedef short v4i16_t __attribute__((ext_vector_type(4)));
    return __builtin_amdgcn_ds_read_tr16_b64_v4i16((LAS v4i16_t*)p);
}
DI bf16x8 cat(s16x4 a, s16x4 b) { return __builtin_shufflevector(a, b, 0, 1, 2, 3, 4, 5, 6, 7); }
DI float swapsum(float v) {
    auto r = __builtin_amdgcn_permlane32_swap(__float_as_uint(v), __float_as_uint(v), false, false);
    return __uint_as_float(r[0]) + __uint_as_float(r[1]);
}
DI rsrc_t mkrsrc(const void* p, unsigned bytes) {
    return __builtin_amdgcn_make_buffer_rsrc((void*)p, (short)0, (int)bytes, 0x00020000);
}
DI void zero16(f32x16& v) {
#pragma unroll
    for (int r = 0; r < 16; ++r) v[r] = 0.f;
}
DI void wait_flags(const GAS u64* p, u64 tag) {
#pragma unroll 1
    for (unsigned it = 0; it < SPIN_CAP; ++it) {
        const u64 v = __hip_atomic_load(p, __ATOMIC_RELAXED, __HIP_MEMORY_SCOPE_AGENT);
        if (__all(v == tag)) break;
        __builtin_amdgcn_s_sleep(1);
    }
    asm volatile("" ::: "memory");
}


DI float max3f(float a, float b, float c) { float r; asm("v_max3_f32 %0, %1, %2, %3" : "=v"(r) : "v"(a), "v"(b), "v"(c)); return r; }
DI float fadd_s(float a, float b) { float r; asm("v_add_f32_e32 %0, %1, %2" : "=v"(r) : "v"(a), "v"(b)); return r; }
DI float fmul_s(float a, float b) { float r; asm("v_mul_f32_e32 %0, %1, %2" : "=v"(r) : "v"(a), "v"(b)); return r; }
DI float fsub_s(float a, float b) { float r; asm("v_sub_f32_e32 %0, %1, %2" : "=v"(r) : "v"(a), "v"(b)); return r; }
#define MPAD4(a) asm volatile("s_nop 7\n\ts_nop 7" : "+v"((a)[0]), "+v"((a)[1]), "+v"((a)[2]), "+v"((a)[3]))
#define PIN(x) asm volatile("" : "+v"(x))
#define PIN4(f) asm volatile("" : "+v"((f)[0]), "+v"((f)[1]), "+v"((f)[2]), "+v"((f)[3]) :: "memory")
DI lptr opaque(lptr p) { unsigned a = (unsigned)(uintptr_t)p; asm volatile("" : "+v"(a)); return (lptr)(uintptr_t)a; }
constexpr int LA = 6;
constexpr float SM_THR = 8.0f;

struct QBase { lptr x0, x1, w; };
DI void qload(int i, bf16x8* wf, bf16x8* xf, const QBase& q) {
    const int s = i >> 2, d = i & 3;
    if (d == 0) xf[s] = lds128(((s & 1) ? q.x1 : q.x0) + 512 * (s >> 1));
    wf[i & 7] = cat(ldstr(q.w + 4096 * s + 512 * d), ldstr(q.w + 4096 * s + 512 * d + 256));
}
DI void qpre(bf16x8* wf, bf16x8* xf, const QBase& q) {
#pragma unroll
    for (int i = 0; i < LA; ++i) qload(i, wf, xf, q);
}
template <bool NEXT> DI void qsteps(f32x16* acc, bf16x8* wf, bf16x8* xf, const QBase& q, const QBase& qn) {
#pragma unroll
    for (int i = 0; i < 16; ++i) {
        SB();
        acc[i & 3] = MFMA32(wf[i & 7], xf[i >> 2], acc[i & 3]);
        PIN(acc[i & 3]);
        SB();
        if (NEXT && i + LA == 16) BARX();
        if (i + LA < 16) qload(i + LA, wf, xf, q); else if (NEXT) qload(i + LA - 16, wf, xf, qn);
    }
    SB();
}
template <bool FIRST = false> DI void oblock(f32x16* acc, const bf16x8* of, lptr wp_) {
    const lptr wp = opaque(wp_);
    bf16x8 wf[8];
    auto load = [&](int i) { wf[i & 7] = cat(ldstr(wp + 5120 * (i / 5) + 512 * (i % 5)), ldstr(wp + 5120 * (i / 5) + 2560 + 512 * (i % 5))); };
#pragma unroll
    for (int i = 0; i < LA; ++i) load(i);
#pragma unroll
    for (int i = 0; i < 20; ++i) {
        SB();
        if (FIRST && i < 5) { f32x16 z; zero16(z); acc[i] = MFMA32(wf[i & 7], of[0], z); }
        else acc[i % 5] = MFMA32(wf[i & 7], of[i / 5], acc[i % 5]);
        PIN(acc[i % 5]);
        SB();
        if (i + LA < 20) load(i + LA);
    }
    SB();
}
DI void aload(int i, bf16x8* fr, lptr vp, lptr kp0, lptr kp1) {
    if (i < 16) fr[i & 7] = cat(ldstr(vp + 4096 * (i >> 2) + 512 * (i & 3)), ldstr(vp + 4096 * (i >> 2) + 2048 + 512 * (i & 3)));
    else { const int s = i & 7; fr[i & 7] = lds128(((s & 1) ? kp1 : kp0) + 512 * (s >> 1) + 8192 * ((i - 16) >> 3)); }
}

__global__ __launch_bounds__(512, 2) void fused_kernel(
    const char* xA, const char* kA, const char* vA, const char* wqA, const char* woA,
    const char* xB, const char* kB, const char* vB, const char* wqB, const char* woB,
    char* ws, char* out)
{
    extern __shared__ __attribute__((aligned(16))) char smem_[];
    const lptr smem = (lptr)smem_;
    const unsigned lds0 = (unsigned)(uintptr_t)smem;
    const int tid = threadIdx.x, lane = tid & 63;
    const int wid = __builtin_amdgcn_readfirstlane(tid >> 6);
    const int bid = blockIdx.x;
    const int xg = bid & 7, ii = bid >> 3;
    const int br = xg >> 2, xq = xg & 3, b = ii >> 2, jj = ii & 3;
    const int h1 = 8 * xq + 2 * jj;
    const char* x = br ? xB : xA;
    const char* kc = br ? kB : kA;
    const char* vc = br ? vB : vA;
    const char* wq = br ? wqB : wqA;
    const char* wo = br ? woB : woA;
    const int qb = wid & 3, grp = wid >> 2, lw = wid & 3;
    const int l31 = lane & 31, h = lane >> 5;
    const char* codebase;
    asm volatile("s_getpc_b64 %0" : "=s"(codebase));

    u64 gen;
    {
        const char* gp = ws + WS_GEN + (br * NB + b) * 256;
        asm volatile("global_load_dwordx2 %0, %1, off sc1" : "=v"(gen) : "v"(gp) : "memory");
    }

    const rsrc_t sk = mkrsrc(kc + (size_t)(b * NH + h1) * SK * DH * 2 - BIAS, 2 * SK * DH * 2 + BIAS);
    const rsrc_t sv = mkrsrc(vc + (size_t)(b * NH + h1) * SK * DH * 2 - BIAS, 2 * SK * DH * 2 + BIAS);
    const rsrc_t sof = mkrsrc(ws + WS_OF + (size_t)((br * NB + b) * NH + 8 * xq) * 32768, 8 * 32768);

    const unsigned dsub = lane >> 5, dr = (lane >> 2) & 7, dsl = lane & 3;
    const unsigned xr0 = 16u * (dsl ^ ((dr >> 2) & 3)), xr1 = 16u * (dsl ^ ((2 + (dr >> 2)) & 3));
    const unsigned vk0 = BIAS + 4096 * lw + dr * 256 + dsub * 64 + xr0, vk2 = BIAS + 4096 * lw + dr * 256 + dsub * 64 + xr1;
    const unsigned vv0 = BIAS + 4096 * lw + dr * 256 + dsub * 64 + 16 * dsl;
    const unsigned ldw = lds0 + 4096 * lw;

    auto issueK = [&](int t, unsigned pair) { dma4<1>(ldw + pair, vk0, vk0 - 896, vk2, vk2 - 896, sk, (unsigned)t * 16384u); };
    auto issueV = [&](int t, unsigned pair) { dma4<1>(ldw + pair + SLOT, vv0, vv0 - 896, vv0, vv0 - 896, sv, (unsigned)t * 16384u); };
    auto nextpair = [](unsigned p) { return (p == 4u * SLOT) ? 0u : p + 2u * SLOT; };

    const int G1 = (lane >> 4) & 1, tq = (lane & 15) >> 2, tp = lane & 3;
    const int sig = ((tp & 1) << 1) | (tp >> 1);
    const unsigned wqb = SLOT + 2048 * h + 64 * tq + 32 * G1 + 8 * sig;
    const unsigned rowq = 32 * qb + l31;
    const unsigned xsw = (rowq >> 2) & 3;
    const unsigned xb0 = 1024 * (rowq >> 3) + 64 * (rowq & 7) + 16 * ((0 + h) ^ xsw);
    const unsigned xb1 = 1024 * (rowq >> 3) + 64 * (rowq & 7) + 16 * ((2 + h) ^ xsw);
    const unsigned ksw = (l31 >> 2) & 3;
    const unsigned kb0 = 2048 * (l31 >> 3) + 64 * (l31 & 7) + 16 * ((0 + h) ^ ksw);
    const unsigned kb1 = 2048 * (l31 >> 3) + 64 * (l31 & 7) + 16 * ((2 + h) ^ ksw);
    const unsigned trb = 64 * (4 * h + tq) + 32 * G1 + 8 * tp;
    const unsigned vo16 = lane * 16;

    auto flagp = [&](int bb, int stage, int w) { return (GAS u64*)(ws + WS_FLAG + (size_t)((bb * 3 + stage) * 8 + w) * 64); };
    auto partner = [&](int dj) { return (bid & ~24) | (((jj + dj) & 3) << 3); };
    auto hl1 = [&](int dj) { return 2 * ((jj + dj) & 3); };

    f32x16 acc[5];
    bf16x8 qf[8], pf[4], fr[8];
    f32x16 s0, s1;
#pragma unroll
    for (int d = 0; d < 4; ++d) zero16(acc[d]);

    auto nextstage = [](unsigned st) { return st == 8u * SLOT ? 4u * SLOT : (st == 4u * SLOT ? 6u * SLOT : (st == 6u * SLOT ? 2u * SLOT : 8u * SLOT)); };
    auto qbase = [&](unsigned st) { return QBase{opaque(smem + st + xb0), opaque(smem + st + xb1), opaque(smem + st + wqb)}; };
    if (grp == 0) {
        unsigned st = 8 * SLOT;
        unsigned dummy = 0;
        {
            const unsigned line = lw * 64 + lane;
            const char* p = codebase + (line < CODE_LINES ? line : 0) * 128;
            asm volatile("global_load_dword %0, %1, off" : "+v"(dummy) : "v"(p) : "memory");
        }
        BARX();
        QBase qc = qbase(st);
        qpre(fr, pf, qc);
#pragma unroll 1
        for (int i = 0; i < 10; ++i) {
            st = nextstage(st);
            const QBase qn = qbase(st);
            qsteps<true>(acc, fr, pf, qc, qn);
            qc = qn;
            if (__builtin_expect(i == 2, 0)) {
                const int idx = lw * 64 + lane;
                const char* p1 = wq + (size_t)(80 * b + (idx < 160 ? idx >> 1 : 0)) * 8192 + (h1 + 1) * 256 + (idx & 1) * 128;
                asm volatile("global_load_dword %0, %1, off" : "+v"(dummy) : "v"(p1) : "memory");
            }
        }
        WAITV(0);
        PIN(dummy);
    } else {
        const rsrc_t sx = mkrsrc(x + (size_t)b * SQ * DM * 2, SQ * DM * 2);
        const rsrc_t sw = mkrsrc(wq + (size_t)h1 * 256 - BIAS, DM * 8192 - h1 * 256 + BIAS);
        const unsigned vx0 = (32 * lw + dr) * 1280 + dsub * 64 + xr0, vx1 = (32 * lw + 8 + dr) * 1280 + dsub * 64 + xr1 - 1024;
        const unsigned vw0 = BIAS + (16 * lw + dr) * 8192 + dsub * 64 + 16 * dsl;
        const int rot = b + 3 * jj;
        constexpr unsigned T0 = 6 * SLOT, T1 = 8 * SLOT, P1 = 2 * SLOT, P2 = 4 * SLOT;
        int ci = rot >= 10 ? rot - 10 : rot;
        auto issueQn = [&](int hd, unsigned st) {
            dma4<0>(ldw + st + SLOT, vw0, vw0 - 896, vw0 + 65536 - 2048, vw0 + 65536 - 2944, sw, (unsigned)ci * 524288u + 256 * hd);
            dma4<0>(ldw + st, vx0, vx1, vx0 + 20480 - 2048, vx1 + 20480 - 2048, sx, 128 * ci);
            ci = ci == 9 ? 0 : ci + 1;
        };
        issueQn(0, T1);
        WAITV(0); BARX();
        issueQn(0, P2); issueQn(0, T0);
        WAITV(8);  BARX(); issueQn(0, P1);
        WAITV(8);  BARX(); issueQn(0, T1);
        WAITV(8);  BARX(); issueQn(0, P2);
        WAITV(8);  BARX(); issueQn(0, T0); issueK(0, 0); issueV(0, 0);
        WAITV(16); BARX(); issueQn(0, P1);
        WAITV(16); BARX(); issueQn(0, T1);
        WAITV(8);  BARX(); issueQn(0, P2);
        WAITV(8);  BARX(); issueQn(1, T0);
        WAITV(8);  BARX(); issueK(1, P1); issueV(1, P1);
        BARX(); issueQn(1, T1);
        const QBase q0 = qbase(T0), q1 = qbase(T1);
        WAITV(16); BARX();
        qpre(fr, pf, q0);
        issueK(2, P2);
        qsteps<false>(acc, fr, pf, q0, q0);
        WAITV(4);
        unsigned pv = P2;
#pragma unroll 1
        for (int c = 1; c < 9; c += 2) {
            unsigned pk = nextpair(pv);
            BARX();
            qpre(fr, pf, q1);
            issueQn(1, T0);
            issueV(c + 1, pv);
            issueK(c + 2, pk);
            qsteps<false>(acc, fr, pf, q1, q1);
            pv = nextpair(pk);
            WAITV(8);
            BARX();
            qpre(fr, pf, q0);
            issueQn(1, T1);
            issueV(c + 2, pk);
            issueK(c + 3, pv);
            qsteps<false>(acc, fr, pf, q0, q0);
            WAITV(8);
        }
        BARX();
        qpre(fr, pf, q1);
        qsteps<false>(acc, fr, pf, q1, q1);
    }
    asm volatile("" : "+v"(gen));
    const u64 tag = gen + 1;

    __builtin_amdgcn_s_setprio(2);
    MPAD4(acc);
    {
        float cexp = CEXP; PIN(cexp);
#pragma unroll
        for (int d = 0; d < 4; ++d) {
#pragma unroll
            for (int r = 0; r < 16; ++r) acc[d][r] = fmul_s(acc[d][r], cexp);
            qf[2 * d] = pack8(acc[d], 0); qf[2 * d + 1] = pack8(acc[d], 1); zero16(acc[d]);
        }
    }
    if (grp == 0) BARX();
    float la = 0.f, lb = 0.f, lc = 0.f, ld = 0.f;
    {
        f32x16 negm, p0; zero16(negm); zero16(s1); zero16(p0);
        unsigned pk = grp ? 4 * SLOT : 0;
        lptr kp0 = opaque(smem + pk + kb0), kp1 = opaque(smem + pk + kb1);
#pragma unroll
        for (int i = 16; i < 16 + LA; ++i) aload(i, fr, kp0, kp0, kp1);
        const GAS u64* fp = flagp(partner(1 + (lane % 3)), 0, qb);
        u64 fv = 0;
        const int tw = grp ? 3 : -1;
        float thr = -__builtin_inff(), dmin = -__builtin_inff();
        u32x4 pw[4];
#pragma unroll 1
        for (int t = 0; t < 8; ++t) {
            if (__builtin_expect(t == tw, 0)) asm volatile("global_load_dwordx2 %0, %1, off sc1" : "+v"(fv) : "v"(fp) : "memory");
            float c0, c1;
#pragma unroll
            for (int j = 0; j < 16; ++j) {
                const int i = 16 + j;
                SB();
                if (j < 8) s0 = MFMA32(fr[i & 7], qf[j], j == 0 ? negm : s0); else s1 = MFMA32(fr[i & 7], qf[j - 8], j == 8 ? negm : s1);
                SB();
                if (i + LA < 32) aload(i + LA, fr, kp0, kp0, kp1);
                if (j < 8)
                    asm volatile("v_add_f32_e32 %0, %0, %4\n\tv_add_f32_e32 %1, %1, %5\n\tv_add_f32_e32 %2, %2, %6\n\tv_add_f32_e32 %3, %3, %7"
                                 : "+v"(la), "+v"(lb), "+v"(lc), "+v"(ld) : "v"(s1[2 * j]), "v"(s1[2 * j + 1]), "v"(p0[2 * j]), "v"(p0[2 * j + 1]));
                if (j >= 10) {
                    const int e = 2 * (j - 10);
                    float e0, e1; unsigned pk_;
                    if (j == 10)
                        asm volatile("v_exp_f32_e32 %0, %5\n\tv_exp_f32_e32 %1, %6\n\tv_max3_f32 %3, %5, %6, %7\n\tv_max3_f32 %4, %8, %9, %10\n\tv_cvt_pk_bf16_f32 %2, %0, %1"
                                     : "=&v"(e0), "=&v"(e1), "=&v"(pk_), "=&v"(c0), "=&v"(c1) : "v"(s0[0]), "v"(s0[1]), "v"(s0[12]), "v"(s0[13]), "v"(s0[14]), "v"(s0[15]));
                    else if (j & 1)
                        asm volatile("v_exp_f32_e32 %0, %4\n\tv_exp_f32_e32 %1, %5\n\tv_max3_f32 %3, %3, %4, %5\n\tv_cvt_pk_bf16_f32 %2, %0, %1"
                                     : "=&v"(e0), "=&v"(e1), "=&v"(pk_), "+v"(c1) : "v"(s0[e]), "v"(s0[e + 1]));
                    else
                        asm volatile("v_exp_f32_e32 %0, %4\n\tv_exp_f32_e32 %1, %5\n\tv_max3_f32 %3, %3, %4, %5\n\tv_cvt_pk_bf16_f32 %2, %0, %1"
                                     : "=&v"(e0), "=&v"(e1), "=&v"(pk_), "+v"(c0) : "v"(s0[e]), "v"(s0[e + 1]));
                    p0[e] = e0; p0[e + 1] = e1;
                    pw[e >> 3][(e >> 1) & 3] = pk_;
                }
            }
            WAITV(0);
            if (__builtin_expect(t == tw, 0)) { PIN(fv); if (!__all(fv == tag)) wait_flags(fp, tag); }
            BARX();
            const lptr vp = opaque(smem + pk + SLOT + trb);
            pk = nextpair(pk);
            kp0 = opaque(smem + pk + kb0); kp1 = opaque(smem + pk + kb1);
#pragma unroll
            for (int i = 0; i < LA; ++i) aload(i, fr, vp, vp, vp);
            PIN(s1);
            SB();
            {
                float c2, c3;
                asm volatile("v_max3_f32 %2, %4, %5, %6\n\tv_max3_f32 %3, %7, %8, %9\n\tv_max3_f32 %0, %0, %10, %11\n\tv_max3_f32 %1, %1, %12, %13\n\t"
                             "v_max3_f32 %2, %2, %14, %15\n\tv_max3_f32 %3, %3, %16, %17\n\tv_max3_f32 %0, %0, %18, %19\n\tv_max3_f32 %1, %1, %2, %3\n\tv_max_f32_e32 %0, %0, %1"
                             : "+v"(c0), "+v"(c1), "=&v"(c2), "=&v"(c3)
                             : "v"(s1[0]), "v"(s1[1]), "v"(s1[2]), "v"(s1[3]), "v"(s1[4]), "v"(s1[5]), "v"(s1[6]), "v"(s1[7]),
                               "v"(s1[8]), "v"(s1[9]), "v"(s1[10]), "v"(s1[11]), "v"(s1[12]), "v"(s1[13]), "v"(s1[14]), "v"(s1[15]));
                auto r_ = __builtin_amdgcn_permlane32_swap(__float_as_uint(c0), __float_as_uint(c0), false, false);
                const float mx = max3f(__uint_as_float(r_[0]), __uint_as_float(r_[1]), __uint_as_float(r_[1]));
                if (__builtin_expect(__any(!(mx <= thr)), 0)) {
                    const float d = fmaxf(mx, dmin);
                    float f = __builtin_amdgcn_exp2f(fminf(-d, 0.f));
                    asm volatile("s_nop 1" : "+v"(f));
#pragma unroll
                    for (int r = 0; r < 16; ++r) { s0[r] = fsub_s(s0[r], d); s1[r] = fsub_s(s1[r], d); negm[r] = fsub_s(negm[r], d); }
                    la = fmul_s(la, f); lb = fmul_s(lb, f); lc = fmul_s(lc, f); ld = fmul_s(ld, f);
#pragma unroll
                    for (int dd = 0; dd < 4; ++dd)
#pragma unroll
                        for (int r = 0; r < 16; ++r) acc[dd][r] = fmul_s(acc[dd][r], f);
#pragma unroll
                    for (int e = 0; e < 12; ++e) p0[e] = __builtin_amdgcn_exp2f(s0[e]);
#pragma unroll
                    for (int e = 0; e < 12; e += 2) pw[e >> 3][(e >> 1) & 3] = cvtpk(p0[e], p0[e + 1]);
                }
                thr = SM_THR; dmin = 0.f;
            }
#pragma unroll
            for (int i = 0; i < 16; ++i) {
                SB();
                acc[i & 3] = MFMA32(fr[i & 7], __builtin_bit_cast(bf16x8, pw[i >> 2]), acc[i & 3]);
                SB();
                constexpr int pairs[16] = {6, 7, 8, 9, 10, 11, -1, 12, 13, 14, 15, -1, -1, -1, -1, -1};
                const int q = pairs[i];
                if (q >= 0) {
                    if (q < 8) { p0[2 * q] = __builtin_amdgcn_exp2f(s0[2 * q]); p0[2 * q + 1] = __builtin_amdgcn_exp2f(s0[2 * q + 1]); PIN(p0); }
                    else { s1[2 * q - 16] = __builtin_amdgcn_exp2f(s1[2 * q - 16]); s1[2 * q - 15] = __builtin_amdgcn_exp2f(s1[2 * q - 15]); PIN(s1); }
                }
                aload(i + LA, fr, vp, kp0, kp1);
                if (q >= 0) {
                    pw[q >> 2][q & 3] = q < 8 ? cvtpk(p0[2 * q], p0[2 * q + 1]) : cvtpk(s1[2 * q - 16], s1[2 * q - 15]);
                    PIN(pw[q >> 2][q & 3]);
                }
            }
            SB();
        }
#pragma unroll
        for (int j = 0; j < 8; ++j) { la = fadd_s(la, s1[2 * j]); lb = fadd_s(lb, s1[2 * j + 1]); lc = fadd_s(lc, p0[2 * j]); ld = fadd_s(ld, p0[2 * j + 1]); }
    }
    if (grp == 0) { __builtin_amdgcn_s_setprio(0); BARX(); }

    {
        float inv = __builtin_amdgcn_rcpf(swapsum((la + lb) + (lc + ld)));
        asm volatile("s_nop 1" : "+v"(inv));
        MPAD4(acc);
#pragma unroll
        for (int d = 0; d < 4; ++d) {
#pragma unroll
            for (int r = 0; r < 16; ++r) acc[d][r] = fmul_s(acc[d][r], inv);
            qf[2 * d] = pack8(acc[d], 0); qf[2 * d + 1] = pack8(acc[d], 1);
            __builtin_amdgcn_raw_buffer_store_b128(__builtin_bit_cast(u32x4, qf[2 * d]), sof, vo16, (2 * jj + grp) * 32768 + qb * 8192 + d * 2048, 16);
            __builtin_amdgcn_raw_buffer_store_b128(__builtin_bit_cast(u32x4, qf[2 * d + 1]), sof, vo16, (2 * jj + grp) * 32768 + qb * 8192 + d * 2048 + 1024, 16);
            SB();
        }
    }
    const rsrc_t swo = mkrsrc(wo + (size_t)(8 * xq * 128) * 1280 + 320 * jj - BIAS, 1024 * 1280 - 320 * jj + BIAS);
    const unsigned vwA = BIAS + (16 * lw + dr) * 1280 + dsub * 64 + 16 * dsl, vwB = BIAS + (16 * lw + dr) * 1280 + 16 * dsl + (dsub ? 8 * 1280 : 256) - 2048;
    auto issueW = [&](int hl, int half, int buf) {
        const unsigned so = (unsigned)(hl * 128 + 64 * half) * 1280u, ld = lds0 + buf * WT + 5 * lw * 1024;
        dma4<0>(ld, vwA, vwA - 896, vwB, vwA + 10240 + 64 - 3072, swo, so);
        dma1<0>(ld + 4096, vwA, swo, so + 10240 + 192);
    };
    const lptr x1 = smem + (lw < 2 ? lw * 8192 : 7 * WT + (lw - 2) * 8192) + vo16;
    if (grp) {
#pragma unroll
        for (int f = 0; f < 8; ++f) *(LAS bf16x8*)(x1 + 1024 * f) = qf[f];
        __builtin_amdgcn_s_setprio(0);
        BAR();
        BARX();
        WAITV(0);
        if (lane == 0) __hip_atomic_store(flagp(bid, 1, qb), tag, __ATOMIC_RELAXED, __HIP_MEMORY_SCOPE_AGENT);
        issueW(hl1(3) + 1, 0, 0); issueW(hl1(3) + 1, 1, 7);
        wait_flags(flagp(partner(1 + (lane % 3)), 1, qb), tag);
        WAITV(0);
        BARX();
    } else {
        bf16x8 cur[4], nxt[4];
#pragma unroll
        for (int k = 0; k < 4; ++k) { cur[k] = qf[k]; nxt[k] = qf[4 + k]; }
        auto issueO = [&](bf16x8* f, int blk) { ld4_sc1(f, vo16, sof, hl1(blk >> 1) * 32768 + qb * 8192 + 4096 * (blk & 1)); };
        issueW(hl1(0), 0, 5);
        BARX();
        issueW(hl1(0), 1, 6); issueW(hl1(1), 0, 7);
        issueV(10, 2 * SLOT); issueK(11, 4 * SLOT);
        BARX();
        issueV(11, 4 * SLOT); issueK(12, 0);
        WAITV(8);
        if (lane == 0) __hip_atomic_store(flagp(bid, 0, qb), tag, __ATOMIC_RELAXED, __HIP_MEMORY_SCOPE_AGENT);
        BARX();
        oblock<true>(acc, cur, smem + 5 * WT + trb);
        issueV(12, 0); issueK(13, 2 * SLOT);
        WAITV(8);
        BARX();
        issueO(cur, 2);
        issueW(hl1(1), 1, 5);
        oblock(acc, nxt, smem + 6 * WT + trb);
        issueO(nxt, 3);
        issueV(13, 2 * SLOT); issueK(14, 4 * SLOT);
        WAITV(17);
        PIN4(cur);
        oblock(acc, cur, smem + 7 * WT + trb);
        issueO(cur, 4);
        WAITV(12);
        BARX();
        unsigned wdummy = 0;
        {
            const int r = 64 + 40 * b + (lane < 40 ? lane : 0);
            const char* p = wo + (size_t)((8 * xq + hl1(1 + (r >> 7)) + 1) * 128 + (r & 127)) * 1280 + 320 * jj + (lw == 3 ? 316 : 128 * lw);
            asm volatile("global_load_dword %0, %1, off" : "+v"(wdummy) : "v"(p) : "memory");
        }
        issueW(hl1(2), 0, 6); issueW(hl1(2), 1, 7);
        PIN4(nxt);
        oblock(acc, nxt, smem + 5 * WT + trb);
        issueO(nxt, 5);
        issueV(14, 4 * SLOT); issueK(15, 0);
        WAITV(17);
        PIN(wdummy);
        BARX();
        issueW(hl1(3), 0, 5);
        PIN4(cur);
        oblock(acc, cur, smem + 6 * WT + trb);
        issueO(cur, 6);
        issueV(15, 0);
        WAITV(13);
        BARX();
        issueW(hl1(3), 1, 6); issueW(hl1(0) + 1, 0, 2); issueW(hl1(0) + 1, 1, 3);
        PIN4(nxt);
        oblock(acc, nxt, smem + 7 * WT + trb);
        issueO(nxt, 7);
        WAITV(4);
        BARX();
        issueW(hl1(1) + 1, 0, 4);
        PIN4(cur);
        oblock(acc, cur, smem + 5 * WT + trb);
        WAITV(5);
        PIN4(nxt);
        oblock(acc, nxt, smem + 6 * WT + trb);
        WAITV(0);
        BARX();
        bf16x8 of2[24];
#pragma unroll
        for (int f = 0; f < 8; ++f) of2[f] = lds128(x1 + 1024 * f);
        BAR();
        issueW(hl1(1) + 1, 1, 5); issueW(hl1(2) + 1, 0, 6); issueW(hl1(2) + 1, 1, 1);
        oblock(acc, of2, smem + 2 * WT + trb);
        oblock(acc, of2 + 4, smem + 3 * WT + trb);
        WAITV(0);
        BARX();
        const unsigned ob = qb * 8192 + 32768;
        auto ldo = [&](int k0) {
#pragma unroll
            for (int k = k0; k < k0 + 4; ++k)
                of2[k] = __builtin_bit_cast(bf16x8, __builtin_amdgcn_raw_buffer_load_b128(sof, vo16, (unsigned)hl1(1 + (k >> 3)) * 32768 + ob + (k & 7) * 1024, 16));
        };
        ldo(0); ldo(4); SB();
        oblock(acc, of2, smem + 4 * WT + trb);       ldo(8);  ldo(12); SB();
        oblock(acc, of2 + 4, smem + 5 * WT + trb);   ldo(16); SB();
        oblock(acc, of2 + 8, smem + 6 * WT + trb);   ldo(20); SB();
        oblock(acc, of2 + 12, smem + 1 * WT + trb);
        oblock(acc, of2 + 16, smem + 0 * WT + trb);
        oblock(acc, of2 + 20, smem + 7 * WT + trb);
        const rsrc_t spb = mkrsrc(ws + WS_PB, 256 * PB_WG);
        f32x4 mine[5];
#define UNIT(u, e) acc[(u) >> 2][4 * ((u) & 3) + (e)]
#define UPK(u) cvtpk(UNIT(u, 0), UNIT(u, 1)), cvtpk(UNIT(u, 2), UNIT(u, 3))
#pragma unroll
        for (int g = 0; g < 4; ++g) {
            if (g == xq) {
#pragma unroll
                for (int n = 0; n < 5; ++n) mine[n] = (f32x4){UNIT(5 * g + n, 0), UNIT(5 * g + n, 1), UNIT(5 * g + n, 2), UNIT(5 * g + n, 3)};
            } else {
                const unsigned so = (unsigned)((bid & ~3) | g) * PB_WG + (xq * 4 + qb) * 5120;
                const u32x4 w01 = {UPK(5 * g), UPK(5 * g + 1)}, w23 = {UPK(5 * g + 2), UPK(5 * g + 3)};
                const u32x2 w4 = {UPK(5 * g + 4)};
                __builtin_amdgcn_raw_buffer_store_b128(w01, spb, vo16, so, 16);
                __builtin_amdgcn_raw_buffer_store_b128(w23, spb, vo16, so + 1024, 16);
                __builtin_amdgcn_raw_buffer_store_b64(w4, spb, lane * 8, so + 2048, 16);
            }
        }
        WAITV(0);
        if (lane == 0) __hip_atomic_store(flagp(bid, 2, qb), tag, __ATOMIC_RELAXED, __HIP_MEMORY_SCOPE_AGENT);
        wait_flags(flagp((bid & ~3) | ((xq + 1 + (lane % 3)) & 3), 2, qb), tag);
        {
            const rsrc_t sout = mkrsrc(out + ((size_t)b * SQ * (2 * DM) + br * DM + 160 * jj + 40 * xq) * 2, SQ * 2 * DM * 2 - (br * DM + 160 * jj + 40 * xq) * 2);
            const unsigned vop = rowq * (4 * DM) + 8 * h;
            u32x4 a01[3], a23[3]; u32x2 a4[3];
#pragma unroll
            for (int s = 0; s < 3; ++s) {
                const unsigned so = (unsigned)bid * PB_WG + (((xq + 1 + s) & 3) * 4 + qb) * 5120;
                a01[s] = __builtin_amdgcn_raw_buffer_load_b128(spb, vo16, so, 16);
                a23[s] = __builtin_amdgcn_raw_buffer_load_b128(spb, vo16, so + 1024, 16);
                a4[s] = __builtin_amdgcn_raw_buffer_load_b64(spb, lane * 8, so + 2048, 16);
            }
            auto up = [](unsigned lo, unsigned hi) { return (f32x4){__uint_as_float(lo << 16), __uint_as_float(lo & 0xffff0000u), __uint_as_float(hi << 16), __uint_as_float(hi & 0xffff0000u)}; };
#pragma unroll
            for (int s = 0; s < 3; ++s) {
                mine[0] += up(a01[s][0], a01[s][1]); mine[1] += up(a01[s][2], a01[s][3]);
                mine[2] += up(a23[s][0], a23[s][1]); mine[3] += up(a23[s][2], a23[s][3]); mine[4] += up(a4[s][0], a4[s][1]);
            }
#pragma unroll
            for (int n = 0; n < 5; ++n) __builtin_amdgcn_raw_buffer_store_b64((u32x2){cvtpk(mine[n][0], mine[n][1]), cvtpk(mine[n][2], mine[n][3])}, sout, vop, 16 * n, 0);
        }
    }
    {
        if (xq == 0 && jj == 0 && wid == 0 && lane == 0)
            __hip_atomic_store((GAS u64*)(ws + WS_GEN + (br * NB + b) * 256), tag, __ATOMIC_RELAXED, __HIP_MEMORY_SCOPE_AGENT);
    }
}


extern "C" void kernel_launch(void* const* d_in, const int* in_sizes, int n_in, void* d_out, int out_size,
                              void* d_ws, size_t ws_size, hipStream_t stream) {
    (void)hipFuncSetAttribute((const void*)fused_kernel, hipFuncAttributeMaxDynamicSharedMemorySize, LDS_BYTES);
    hipLaunchKernelGGL(fused_kernel, dim3(256), dim3(512), LDS_BYTES, stream,
                       (const char*)d_in[0], (const char*)d_in[1], (const char*)d_in[2], (const char*)d_in[3], (const char*)d_in[4],
                       (const char*)d_in[5], (const char*)d_in[6], (const char*)d_in[7], (const char*)d_in[8], (const char*)d_in[9],
                       (char*)d_ws, (char*)d_out);
}
```
